```python
import torch

def _softmax(x, axis=-1):
    return torch.softmax(x, dim=axis)
def _gelu(x):
    return torch.nn.functional.gelu(x, approximate="tanh")
def _silu(x):
    return torch.nn.functional.silu(x)
def _sigmoid(x):
    return torch.sigmoid(x)
def _layernorm(x, axis=-1, eps=1e-5):
    m = x.mean(dim=axis, keepdim=True)
    v = x.var(dim=axis, keepdim=True, unbiased=False)
    return (x - m) / torch.sqrt(v + eps)
def _scatter_add(tgt, idx, src, axis=0):
    return tgt.clone().index_add_(axis, idx, src)
def _segment_sum(src, seg, n):
    out = torch.zeros((n,) + src.shape[1:], dtype=src.dtype)
    return out.index_add_(0, seg, src)
def _pad(x, pw):
    flat = tuple(v for pair in reversed(pw) for v in pair)
    return torch.nn.functional.pad(x, flat)

kernel_name = 'standalone__gqa_softmax__near_ridge'

def setup_inputs():

    _w = (lambda a: (lambda t: t.to(torch.bfloat16) if t.is_floating_point() else t)(torch.as_tensor(a)))
    B, Hq, Hkv, Sq, Sk, D = 8, 32, 32, 288, 512, 192
    return {
        "q": _w((lambda *s: torch.randn(*s, dtype=torch.bfloat16) * 0.8)(B, Hq, Sq, D)),
        "k": _w((lambda *s: torch.randn(*s, dtype=torch.bfloat16) * 0.8)(B, Hkv, Sk, D)),
        "v": _w((lambda *s: torch.randn(*s, dtype=torch.bfloat16) * 0.05)(B, Hkv, Sk, D)),
    }

def reference(q, k, v):
    B, Hq, Sq, D = q.shape
    Hkv = k.shape[1]
    rep = Hq // Hkv
    k = (lambda x, n, axis: torch.repeat_interleave(x, n, dim=axis))(k, rep, axis=1)
    v = (lambda x, n, axis: torch.repeat_interleave(x, n, dim=axis))(v, rep, axis=1)
    scale = D ** -0.5
    scores = torch.einsum("bhqd,bhkd->bhqk", q, k) * scale
    p = _softmax(scores, axis=-1)
    return torch.einsum("bhqk,bhkd->bhqd", p, v)
```

```cpp
#include <hip/hip_runtime.h>
#include <hip/hip_bf16.h>

typedef float    f32x16 __attribute__((ext_vector_type(16)));
typedef short    bf16x8 __attribute__((ext_vector_type(8)));
typedef short    s16x4  __attribute__((ext_vector_type(4)));
typedef unsigned u32x4  __attribute__((ext_vector_type(4)));
typedef unsigned u32x2  __attribute__((ext_vector_type(2)));

constexpr int SQ = 288, SK = 512, D = 192;
constexpr int KT = 32;
constexpr int ROWB = 400;

__device__ __forceinline__ unsigned cvtpk(float lo, float hi) {
  unsigned r; asm volatile("v_cvt_pk_bf16_f32 %0, %1, %2" : "=v"(r) : "v"(lo), "v"(hi)); return r;
}
__device__ __forceinline__ s16x4 tr_read(unsigned addr) {
  s16x4 r; asm volatile("ds_read_b64_tr_b16 %0, %1\n\ts_waitcnt lgkmcnt(0)" : "=&v"(r) : "v"(addr) : "memory"); return r;
}

__global__ __launch_bounds__(192) void attn_v0(const unsigned short* __restrict__ Q, const unsigned short* __restrict__ K,
                                               const unsigned short* __restrict__ V, unsigned short* __restrict__ O) {
  __shared__ __attribute__((aligned(16))) char lds[2 * KT * ROWB];
  char* Kl = lds; char* Vl = lds + KT * ROWB;
  const int tid = threadIdx.x, wid = tid >> 6, lane = tid & 63, r32 = lane & 31, hi = lane >> 5;
  const int head = blockIdx.x / 3, part = blockIdx.x % 3;
  const int row0 = part * 96 + wid * 32;
  const unsigned short* Qh = Q + (size_t)head * SQ * D;
  const unsigned short* Kh = K + (size_t)head * SK * D;
  const unsigned short* Vh = V + (size_t)head * SK * D;
  unsigned short* Oh = O + (size_t)head * SQ * D;
  bf16x8 qf[12];
#pragma unroll
  for (int s = 0; s < 12; ++s) qf[s] = *(const bf16x8*)(Qh + (size_t)(row0 + r32) * D + 16 * s + 8 * hi);
  f32x16 o[6];
#pragma unroll
  for (int i = 0; i < 6; ++i)
#pragma unroll
    for (int j = 0; j < 16; ++j) o[i][j] = 0.f;
  float m = -1e30f, l = 0.f;
  const float C = 0.07216878364870322f * 1.4426950408889634f;
  for (int t = 0; t < SK / KT; ++t) {
    __syncthreads();
    for (int i = tid; i < KT * 24; i += 192) {
      int row = i / 24, c = i % 24;
      *(u32x4*)(Kl + row * ROWB + c * 16) = *(const u32x4*)(Kh + (size_t)(t * KT + row) * D + c * 8);
      *(u32x4*)(Vl + row * ROWB + c * 16) = *(const u32x4*)(Vh + (size_t)(t * KT + row) * D + c * 8);
    }
    __syncthreads();
    f32x16 s;
#pragma unroll
    for (int j = 0; j < 16; ++j) s[j] = 0.f;
#pragma unroll
    for (int d = 0; d < 12; ++d) {
      bf16x8 kf = *(const bf16x8*)(Kl + r32 * ROWB + (2 * d + hi) * 16);
      s = __builtin_amdgcn_mfma_f32_32x32x16_bf16(kf, qf[d], s, 0, 0, 0);
    }
    float pm = s[0];
#pragma unroll
    for (int j = 1; j < 16; ++j) pm = fmaxf(pm, s[j]);
    pm = fmaxf(pm, __shfl_xor(pm, 32));
    float mn = fmaxf(m, pm);
    float alpha = __builtin_amdgcn_exp2f((m - mn) * C);
    m = mn;
    float ps = 0.f;
#pragma unroll
    for (int j = 0; j < 16; ++j) { s[j] = __builtin_amdgcn_exp2f((s[j] - mn) * C); ps += s[j]; }
    ps += __shfl_xor(ps, 32);
    l = l * alpha + ps;
#pragma unroll
    for (int i = 0; i < 6; ++i)
#pragma unroll
      for (int j = 0; j < 16; ++j) o[i][j] *= alpha;
    bf16x8 p[2];
#pragma unroll
    for (int ks = 0; ks < 2; ++ks) {
      u32x4 w = {cvtpk(s[8 * ks + 0], s[8 * ks + 1]), cvtpk(s[8 * ks + 2], s[8 * ks + 3]), cvtpk(s[8 * ks + 4], s[8 * ks + 5]), cvtpk(s[8 * ks + 6], s[8 * ks + 7])};
      p[ks] = __builtin_bit_cast(bf16x8, w);
    }
    const unsigned vb = (unsigned)(size_t)Vl;
    const int blk = (lane >> 4) & 1, q = (lane & 15) >> 2, pp = lane & 3;
#pragma unroll
    for (int db = 0; db < 6; ++db)
#pragma unroll
      for (int ks = 0; ks < 2; ++ks) {
        s16x4 lo = tr_read(vb + (16 * ks + 4 * hi + q) * ROWB + (32 * db + 16 * blk + 4 * pp) * 2);
        s16x4 hh = tr_read(vb + (16 * ks + 8 + 4 * hi + q) * ROWB + (32 * db + 16 * blk + 4 * pp) * 2);
        bf16x8 vf = __builtin_shufflevector(lo, hh, 0, 1, 2, 3, 4, 5, 6, 7);
        o[db] = __builtin_amdgcn_mfma_f32_32x32x16_bf16(vf, p[ks], o[db], 0, 0, 0);
      }
  }
  const float il = 1.f / l;
#pragma unroll
  for (int db = 0; db < 6; ++db)
#pragma unroll
    for (int g = 0; g < 4; ++g) {
      u32x2 w = {cvtpk(o[db][4 * g] * il, o[db][4 * g + 1] * il), cvtpk(o[db][4 * g + 2] * il, o[db][4 * g + 3] * il)};
      *(u32x2*)(Oh + (size_t)(row0 + r32) * D + 32 * db + 8 * g + 4 * hi) = w;
    }
}

extern "C" void kernel_launch(void* const* d_in, const int* in_sizes, int n_in,
                              void* d_out, int out_size, void* d_ws, size_t ws_size,
                              hipStream_t stream) {
  attn_v0<<<256 * 3, 192, 0, stream>>>((const unsigned short*)d_in[0], (const unsigned short*)d_in[1], (const unsigned short*)d_in[2], (unsigned short*)d_out);
}
```

```cpp
#include <hip/hip_runtime.h>
#include <hip/hip_bf16.h>

typedef float    f32x16 __attribute__((ext_vector_type(16)));
typedef short    bf16x8 __attribute__((ext_vector_type(8)));
typedef short    s16x4  __attribute__((ext_vector_type(4)));
typedef unsigned u32x4  __attribute__((ext_vector_type(4)));
typedef unsigned u32x2  __attribute__((ext_vector_type(2)));

constexpr int SQ = 288, SK = 512, D = 192;
constexpr int KT = 32;
constexpr int ROWB = 400;

__device__ __forceinline__ unsigned cvtpk(float lo, float hi) {
  unsigned r; asm volatile("v_cvt_pk_bf16_f32 %0, %1, %2" : "=v"(r) : "v"(lo), "v"(hi)); return r;
}
__device__ __forceinline__ s16x4 tr_read(unsigned addr) {
  s16x4 r; asm volatile("ds_read_b64_tr_b16 %0, %1\n\ts_waitcnt lgkmcnt(0)" : "=&v"(r) : "v"(addr) : "memory"); return r;
}

__global__ __launch_bounds__(192) void attn_v0(const unsigned short* __restrict__ Q, const unsigned short* __restrict__ K,
                                               const unsigned short* __restrict__ V, unsigned short* __restrict__ O) {
  __shared__ __attribute__((aligned(16))) char lds[2 * KT * ROWB];
  char* Kl = lds; char* Vl = lds + KT * ROWB;
  const int tid = threadIdx.x, wid = tid >> 6, lane = tid & 63, r32 = lane & 31, hi = lane >> 5;
  const int head = blockIdx.x / 3, part = blockIdx.x % 3;
  const int row0 = part * 96 + wid * 32;
  const unsigned short* Qh = Q + (size_t)head * SQ * D;
  const unsigned short* Kh = K + (size_t)head * SK * D;
  const unsigned short* Vh = V + (size_t)head * SK * D;
  unsigned short* Oh = O + (size_t)head * SQ * D;
  bf16x8 qf[12];
#pragma unroll
  for (int s = 0; s < 12; ++s) qf[s] = *(const bf16x8*)(Qh + (size_t)(row0 + r32) * D + 16 * s + 8 * hi);
  f32x16 o[6];
#pragma unroll
  for (int i = 0; i < 6; ++i)
#pragma unroll
    for (int j = 0; j < 16; ++j) o[i][j] = 0.f;
  float m = -1e30f, l = 0.f;
  const float C = 0.07216878364870322f * 1.4426950408889634f;
  for (int t = 0; t < SK / KT; ++t) {
    __syncthreads();
    for (int i = tid; i < KT * 24; i += 192) {
      int row = i / 24, c = i % 24;
      *(u32x4*)(Kl + row * ROWB + c * 16) = *(const u32x4*)(Kh + (size_t)(t * KT + row) * D + c * 8);
      *(u32x4*)(Vl + row * ROWB + c * 16) = *(const u32x4*)(Vh + (size_t)(t * KT + row) * D + c * 8);
    }
    __syncthreads();
    f32x16 s;
#pragma unroll
    for (int j = 0; j < 16; ++j) s[j] = 0.f;
#pragma unroll
    for (int d = 0; d < 12; ++d) {
      bf16x8 kf = *(const bf16x8*)(Kl + r32 * ROWB + (2 * d + hi) * 16);
      s = __builtin_amdgcn_mfma_f32_32x32x16_bf16(kf, qf[d], s, 0, 0, 0);
    }
    float pm = s[0];
#pragma unroll
    for (int j = 1; j < 16; ++j) pm = fmaxf(pm, s[j]);
    pm = fmaxf(pm, __shfl_xor(pm, 32));
    float mn = fmaxf(m, pm);
    float alpha = __builtin_amdgcn_exp2f((m - mn) * C);
    m = mn;
    float ps = 0.f;
#pragma unroll
    for (int j = 0; j < 16; ++j) { s[j] = __builtin_amdgcn_exp2f((s[j] - mn) * C); ps += s[j]; }
    ps += __shfl_xor(ps, 32);
    l = l * alpha + ps;
#pragma unroll
    for (int i = 0; i < 6; ++i)
#pragma unroll
      for (int j = 0; j < 16; ++j) o[i][j] *= alpha;
    bf16x8 p[2];
#pragma unroll
    for (int ks = 0; ks < 2; ++ks) {
      u32x4 w = {cvtpk(s[8 * ks + 0], s[8 * ks + 1]), cvtpk(s[8 * ks + 2], s[8 * ks + 3]), cvtpk(s[8 * ks + 4], s[8 * ks + 5]), cvtpk(s[8 * ks + 6], s[8 * ks + 7])};
      p[ks] = __builtin_bit_cast(bf16x8, w);
    }
    const unsigned vb = (unsigned)(size_t)Vl;
    const int blk = (lane >> 4) & 1, q = (lane & 15) >> 2, pp = lane & 3;
#pragma unroll
    for (int db = 0; db < 6; ++db)
#pragma unroll
      for (int ks = 0; ks < 2; ++ks) {
        s16x4 lo = tr_read(vb + (16 * ks + 4 * hi + q) * ROWB + (32 * db + 16 * blk + 4 * pp) * 2);
        s16x4 hh = tr_read(vb + (16 * ks + 8 + 4 * hi + q) * ROWB + (32 * db + 16 * blk + 4 * pp) * 2);
        bf16x8 vf = __builtin_shufflevector(lo, hh, 0, 1, 2, 3, 4, 5, 6, 7);
        o[db] = __builtin_amdgcn_mfma_f32_32x32x16_bf16(vf, p[ks], o[db], 0, 0, 0);
      }
  }
  const float il = 1.f / l;
#pragma unroll
  for (int db = 0; db < 6; ++db)
#pragma unroll
    for (int g = 0; g < 4; ++g) {
      u32x2 w = {cvtpk(o[db][4 * g] * il, o[db][4 * g + 1] * il), cvtpk(o[db][4 * g + 2] * il, o[db][4 * g + 3] * il)};
      *(u32x2*)(Oh + (size_t)(row0 + r32) * D + 32 * db + 8 * g + 4 * hi) = w;
    }
}

#define INFLIGHT 24
__global__ __launch_bounds__(256, 1) void memprobe(const char* __restrict__ Q, const char* __restrict__ K,
                                               const char* __restrict__ V, char* __restrict__ O) {
  extern __shared__ __attribute__((aligned(16))) char lds[];
  const int tid = threadIdx.x, lane = tid & 63;
  const int wid = __builtin_amdgcn_readfirstlane(tid >> 6);
  const int head = blockIdx.x;
  const char* Qh = Q + (size_t)head * SQ * D * 2;
  const char* Kh = K + (size_t)head * SK * D * 2;
  const char* Vh = V + (size_t)head * SK * D * 2;
  char* Oh = O + (size_t)head * SQ * D * 2;
  unsigned ldsb = (unsigned)(size_t)lds + wid * 36864;
  int slot = 0;
  for (int p = wid; p < 492; p += 4) {
    const char* src;
    if (p < 108) src = Qh + p * 1024;
    else { int j = p - 108; int t = j / 24, r = j % 24;
      int kt = t; (void)kt;
      src = (r < 12) ? (Kh + (t * 12 + r) * 1024) : (Vh + (t * 12 + (r - 12)) * 1024); }
    src += lane * 16;
    unsigned m0v = ldsb + slot * 1024;
    unsigned keep;
    asm volatile("s_mov_b32 %0, m0\n\ts_mov_b32 m0, %2\n\ts_nop 0\n\tglobal_load_lds_dwordx4 %1, off\n\ts_mov_b32 m0, %0" : "=&s"(keep) : "v"(src), "s"(m0v) : "memory");
    slot = (slot + 1) % 36;
    asm volatile("s_waitcnt vmcnt(%0)" :: "i"(INFLIGHT) : "memory");
  }
  asm volatile("s_waitcnt vmcnt(0)" ::: "memory");
  __syncthreads();
  for (int p = wid; p < 108; p += 4) {
    u32x4 v = *(const u32x4*)(lds + wid * 36864 + (p >> 2) * 1024 + lane * 16);
    *(u32x4*)(Oh + p * 1024 + lane * 16) = v;
  }
}

extern "C" void kernel_launch(void* const* d_in, const int* in_sizes, int n_in,
                              void* d_out, int out_size, void* d_ws, size_t ws_size,
                              hipStream_t stream) {
  hipFuncSetAttribute((const void*)memprobe, hipFuncAttributeMaxDynamicSharedMemorySize, 147456);
  memprobe<<<256, 256, 147456, stream>>>((const char*)d_in[0], (const char*)d_in[1], (const char*)d_in[2], (char*)d_out);
  attn_v0<<<256 * 3, 192, 0, stream>>>((const unsigned short*)d_in[0], (const unsigned short*)d_in[1], (const unsigned short*)d_in[2], (unsigned short*)d_out);
}
```

```cpp
#include <hip/hip_runtime.h>

__global__ __launch_bounds__(512, 2) void attn_fwd(const char* __restrict__ Q, const char* __restrict__ K, const char* __restrict__ V, char* __restrict__ O) {
  unsigned tid = threadIdx.x;
  unsigned head = blockIdx.x;
  asm volatile(
    "s_load_dwordx8 s[80:87], %1, 0x0\n"
    "v_lshrrev_b32_e32 v1, 6, %0\n"
    "v_and_b32_e32 v9, 63, %0\n"
    "v_readfirstlane_b32 s56, v1\n"
    "s_mul_i32 s88, %5, 196608\n"
    "s_mul_i32 s89, %5, 110592\n"
    "s_mov_b32 s46, 196608\n"
    "s_mov_b32 s47, 0x20000\n"
    "s_getpc_b64 s[72:73]\n.Lcpf0:\ns_and_b32 s73, s73, 0xffff\ns_mov_b32 s74, .Lcodeend-.Lcpf0\ns_mov_b32 s75, 0x20000\n"
    "s_lshr_b32 s66, %5, 3\n"
    "s_and_b32 s66, s66, 7\n"
    "s_mul_i32 s66, s66, 3072\n"
    "s_cmp_lg_u32 s56, 7\n"
    "s_cselect_b32 s67, 0x100000, 0\n"
    "s_add_u32 s66, s66, s67\n"
    "v_lshrrev_b32_e32 v81, 5, v9\n"
    "v_lshlrev_b32_e32 v81, 20, v81\n"
    "v_lshl_add_u32 v80, v9, 7, v81\n"
    "v_add_u32_e32 v80, s66, v80\n"
    "buffer_load_dword v81, v80, s[72:75], 0 offen\n"
    "s_mov_b32 s36, 0x3dd53b95\n"
    "s_mov_b32 s38, 0x45800000\n"
    "s_mov_b32 s42, 110592\n"
    "s_mov_b32 s43, 0x20000\n"
    "s_mov_b32 s50, 196608\n"
    "s_mov_b32 s51, 0x20000\n"
    "s_mov_b32 s54, 110592\n"
    "s_mov_b32 s55, 0x20000\n"
    "s_lshl_b32 s57, s56, 10\n"
    "s_mul_i32 s62, s56, 3072\n"
    "s_mul_i32 s63, s56, 12288\n"
    "s_add_u32 s64, s63, 6144\n"
    "s_lshr_b32 s66, s56, 2\n"
    "s_lshl_b32 s58, s66, 7\n"
    "s_cmp_lg_u32 s66, 0\n"
    "s_cselect_b32 s59, 47, 0\n"
    "s_add_u32 s59, s59, 1\n"
    "s_lshl_b32 s59, s59, 8\n"
    "s_add_u32 s60, s58, 12416\n"
    "v_lshrrev_b32_e32 v2, 3, v9\n"
    "v_and_b32_e32 v3, 7, v9\n"
    "v_lshrrev_b32_e32 v4, 4, v9\n"
    "s_and_b32 s66, s56, 1\n"
    "s_lshl_b32 s67, s66, 2\n"
    "s_lshl_b32 s68, s66, 1\n"
    "s_and_b32 s69, s56, 3\n"
    "s_lshl_b32 s69, s69, 3\n"
    "v_add_u32_e32 v5, s67, v4\n"
    "v_xor_b32_e32 v5, v5, v3\n"
    "v_add_u32_e32 v6, s69, v2\n"
    "v_mul_u32_u24_e32 v6, 0x180, v6\n"
    "v_lshl_add_u32 v126, v5, 4, v6\n"
    "s_waitcnt lgkmcnt(0)\n"
    "s_add_u32 s44, s82, s88\n"
    "s_addc_u32 s45, s83, 0\n"
    "s_and_b32 s45, s45, 0xffff\n"
    "s_add_u32 m0, s57, 0x0\n"
    "s_nop 0\n"
    "buffer_load_dwordx4 v126, s[44:47], s58 offen nt lds\n"
    "s_add_u32 m0, s57, 0x2000\n"
    "s_nop 0\n"
    "buffer_load_dwordx4 v126, s[44:47], s59 offen nt lds\n"
    "s_add_u32 m0, s57, 0x4000\n"
    "s_nop 0\n"
    "buffer_load_dwordx4 v126, s[44:47], s60 offen nt lds\n"
    "s_add_u32 s40, s80, s89\n"
    "s_addc_u32 s41, s81, 0\n"
    "s_and_b32 s41, s41, 0xffff\n"
    "v_lshlrev_b32_e32 v5, 1, v4\n"
    "v_xor_b32_e32 v5, v5, v3\n"
    "v_mul_u32_u24_e32 v6, 0x180, v2\n"
    "v_lshl_add_u32 v127, v5, 4, v6\n"
    "v_xor_b32_e32 v5, v4, v3\n"
    "v_lshl_add_u32 v7, v5, 4, v6\n"
    "v_xor_b32_e32 v8, 64, v7\n"
    "s_lshr_b32 s66, s56, 2\n"
    "s_lshl_b32 s66, s66, 7\n"
    "s_add_u32 m0, s57, 0x15000\n"
    "s_add_u32 s61, s66, 0x18000\n"
    "buffer_load_dwordx4 v126, s[40:43], s61 offen nt lds\n"
    "s_cmp_ge_u32 s56, 4\n"
    "s_cbranch_scc1 .Lskipq9\n"
    "s_mov_b32 s66, 0\n"
    "s_add_u32 m0, s57, 0x17000\n"
    "s_add_u32 s61, s66, 0x18100\n"
    "buffer_load_dwordx4 v126, s[40:43], s61 offen nt lds\n"
    ".Lskipq9:\n"
    "s_lshl_b32 s69, s56, 12\n"
    "s_add_u32 m0, s69, 0xc000\n"
    "s_nop 0\n"
    "buffer_load_dwordx4 v7, s[40:43], s63 offen nt lds\n"
    "v_and_b32_e32 v14, 15, v9\n"
    "v_lshrrev_b32_e32 v15, 4, v9\n"
    "v_lshrrev_b32_e32 v13, 1, v14\n"
    "v_xor_b32_e32 v13, v13, v15\n"
    "v_lshlrev_b32_e32 v13, 4, v13\n"
    "v_lshl_add_u32 v12, v14, 7, v13\n"
    "v_xor_b32_e32 v13, 64, v12\n"
    "s_add_u32 s48, s84, s88\n"
    "s_addc_u32 s49, s85, 0\n"
    "s_and_b32 s49, s49, 0xffff\n"
    "s_add_u32 s52, s86, s89\n"
    "s_addc_u32 s53, s87, 0\n"
    "s_and_b32 s53, s53, 0xffff\n"
    "s_and_b32 s18, s56, 3\n"
    "s_lshl_b32 s16, s18, 2\n"
    "s_lshr_b32 s66, s56, 2\n"
    "s_lshl_b32 s17, s66, 8\n"
    "s_xor_b32 s66, s18, 0\n"
    "s_and_b32 s67, s66, 1\n"
    "s_lshl_b32 s67, s67, 11\n"
    "s_lshr_b32 s66, s66, 1\n"
    "s_mul_i32 s66, s66, 12288\n"
    "s_add_u32 s66, s66, s67\n"
    "v_add_u32_e32 v244, s66, v12\n"
    "v_add_u32_e32 v245, s66, v13\n"
    "s_lshl_b32 s67, s17, 3\n"
    "s_sub_u32 s20, s67, s66\n"
    "s_add_u32 s20, s20, 0x15000\n"
    "s_xor_b32 s66, s18, 1\n"
    "s_and_b32 s67, s66, 1\n"
    "s_lshl_b32 s67, s67, 11\n"
    "s_lshr_b32 s66, s66, 1\n"
    "s_mul_i32 s66, s66, 12288\n"
    "s_add_u32 s66, s66, s67\n"
    "v_add_u32_e32 v246, s66, v12\n"
    "v_add_u32_e32 v247, s66, v13\n"
    "s_xor_b32 s66, s18, 2\n"
    "s_and_b32 s67, s66, 1\n"
    "s_lshl_b32 s67, s67, 11\n"
    "s_add_u32 m0, s69, 0xc400\n"
    "s_add_u32 s61, s63, 0xc00\n"
    "buffer_load_dwordx4 v8, s[40:43], s61 offen nt lds\n"
    "s_lshr_b32 s66, s66, 1\n"
    "s_mul_i32 s66, s66, 12288\n"
    "s_add_u32 s66, s66, s67\n"
    "v_add_u32_e32 v248, s66, v12\n"
    "v_add_u32_e32 v249, s66, v13\n"
    "s_xor_b32 s66, s18, 3\n"
    "s_and_b32 s67, s66, 1\n"
    "s_lshl_b32 s67, s67, 11\n"
    "s_lshr_b32 s66, s66, 1\n"
    "s_mul_i32 s66, s66, 12288\n"
    "s_add_u32 s66, s66, s67\n"
    "v_add_u32_e32 v250, s66, v12\n"
    "v_add_u32_e32 v251, s66, v13\n"
    "s_add_u32 s68, s69, 49152\n"
    "v_add_u32_e32 v16, s68, v12\n"
    "v_add_u32_e32 v17, s68, v13\n"
    "s_add_u32 s68, s68, 2048\n"
    "v_add_u32_e32 v18, s68, v12\n"
    "v_add_u32_e32 v19, s68, v13\n"
    "s_lshl_b32 s67, s17, 3\n"
    "s_add_u32 s67, s67, 0x15000\n"
    "v_add_u32_e32 v20, s67, v12\n"
    "v_add_u32_e32 v21, s67, v13\n"
    "s_and_b32 s66, s56, 1\n"
    "s_lshr_b32 s67, s56, 1\n"
    "s_mul_i32 s37, s66, 49664\n"
    "s_lshl_b32 s39, s67, 2\n"
    "s_lshl_b32 s35, s66, 8\n"
    "s_mul_i32 s27, s56, 2304\ns_mov_b32 s28, 72192\ns_cmp_lt_u32 s56, 6\ns_cselect_b32 s28, 66560, s28\ns_add_u32 s27, s27, s28\ns_movk_i32 s28, 144\ns_lshr_b32 s29, s39, 1\n"
    "s_mul_i32 s66, s66, 6144\n"
    "s_lshl_b32 s67, s67, 5\n"
    "s_add_u32 s65, s66, s67\n"
    "s_add_u32 s65, s65, 98304\n"
    "s_lshl_b32 s66, s18, 11\n"
    "s_xor_b32 s22, s66, s67\n"
    "s_lshr_b32 s66, s56, 2\n"
    "s_mul_i32 s21, s66, 49664\n"
    "s_xor_b32 s66, s66, s18\n"
    "s_and_b32 s66, s66, 1\n"
    "s_lshl_b32 s66, s66, 3\n"
    "s_add_u32 m0, s69, 0xc800\n"
    "s_add_u32 s61, s63, 0x1800\n"
    "buffer_load_dwordx4 v7, s[40:43], s61 offen nt lds\n"
    "s_add_u32 s21, s21, s66\n"
    "s_and_b32 s66, s18, 2\n"
    "s_lshl_b32 s66, s66, 5\n"
    "s_add_u32 s21, s21, s66\n"
    "s_add_u32 s23, s37, s66\n"
    "s_xor_b32 s66, s66, 64\n"
    "s_add_u32 s24, s37, s66\n"
    "v_lshrrev_b32_e32 v33, 2, v9\n"
    "v_lshlrev_b32_e32 v33, 7, v33\n"
    "v_bfe_u32 v34, v9, 3, 2\n"
    "v_lshl_add_u32 v33, v34, 5, v33\n"
    "v_and_b32_e32 v34, 3, v9\n"
    "v_lshl_add_u32 v33, v34, 3, v33\n"
    "v_add_u32_e32 v31, 0x18000, v33\n"
    "v_bfe_u32 v32, v9, 4, 1\n"
    "v_lshrrev_b32_e32 v35, 5, v9\n"
    "v_mul_u32_u24_e32 v33, 0x420, v14\n"
    "v_bfe_u32 v34, v9, 2, 1\n"
    "v_xor_b32_e32 v34, v34, v15\n"
    "v_lshl_add_u32 v24, v34, 4, v33\n"
    "v_mov_b32_e32 v26, v24\n"
    "v_mov_b32_e32 v28, v24\n"
    "v_mul_u32_u24_e32 v33, 0x180, v14\n"
    "v_lshl_add_u32 v25, v35, 5, v33\n"
    "v_lshl_add_u32 v25, v32, 4, v25\n"
    "v_lshl_add_u32 v30, v15, 3, v33\n"
    "v_lshrrev_b32_e32 v34, 3, v9\n"
    "v_mul_u32_u24_e32 v34, 0x180, v34\n"
    "v_and_b32_e32 v25, 7, v9\n"
    "v_lshl_add_u32 v25, v25, 4, v34\n"
    "v_lshlrev_b32_e32 v29, 4, v14\n"
    "v_add_u32_e32 v29, 0x14000, v29\n"
    "v_mov_b32_e32 v27, 0\n"
    "s_mov_b32 s34, 0x14440\n"
    "s_add_u32 s66, s57, 0x15000\n"
    "v_lshl_add_u32 v32, v9, 4, s34\n"
    "v_lshl_add_u32 v36, v9, 4, s66\n"
    "v_mov_b32_e32 v33, 0x14400\n"
    "v_add_u32_e32 v64, s17, v29\n"
    "v_add_u32_e32 v65, s16, v64\n"
    "s_add_u32 m0, s69, 0xcc00\n"
    "s_add_u32 s61, s63, 0x2400\n"
    "buffer_load_dwordx4 v8, s[40:43], s61 offen nt lds\n"
    "s_barrier\n"
    "s_add_u32 m0, s69, 0x18000\n"
    "s_add_u32 s61, s63, 0x80\n"
    "buffer_load_dwordx4 v7, s[40:43], s61 offen nt lds\n"
    "s_add_u32 m0, s69, 0x18400\n"
    "s_add_u32 s61, s63, 0xc80\n"
    "buffer_load_dwordx4 v8, s[40:43], s61 offen nt lds\n"
    "s_add_u32 m0, s69, 0x18800\n"
    "s_add_u32 s61, s63, 0x1880\n"
    "buffer_load_dwordx4 v7, s[40:43], s61 offen nt lds\n"
    "s_add_u32 m0, s69, 0x18c00\n"
    "s_add_u32 s61, s63, 0x2480\n"
    "buffer_load_dwordx4 v8, s[40:43], s61 offen nt lds\n"
    "s_barrier\n"
    "s_waitcnt vmcnt(8)\n"
    "ds_read_b128 v[44:47], v36\n"
    "s_waitcnt lgkmcnt(0)\n"
    "v_lshlrev_b32_e32 v40, 16, v44\n"
    "v_and_b32_e32 v41, 0xffff0000, v44\n"
    "v_lshlrev_b32_e32 v42, 16, v45\n"
    "v_and_b32_e32 v43, 0xffff0000, v45\n"
    "v_mul_f32_e32 v40, s36, v40\n"
    "v_mul_f32_e32 v41, s36, v41\n"
    "v_mul_f32_e32 v42, s36, v42\n"
    "v_mul_f32_e32 v43, s36, v43\n"
    "v_cvt_pk_bf16_f32 v44, v40, v41\n"
    "v_cvt_pk_bf16_f32 v45, v42, v43\n"
    "v_lshlrev_b32_e32 v40, 16, v46\n"
    "v_and_b32_e32 v41, 0xffff0000, v46\n"
    "v_lshlrev_b32_e32 v42, 16, v47\n"
    "v_and_b32_e32 v43, 0xffff0000, v47\n"
    "v_mul_f32_e32 v40, s36, v40\n"
    "v_mul_f32_e32 v41, s36, v41\n"
    "v_mul_f32_e32 v42, s36, v42\n"
    "v_mul_f32_e32 v43, s36, v43\n"
    "v_cvt_pk_bf16_f32 v46, v40, v41\n"
    "v_cvt_pk_bf16_f32 v47, v42, v43\n"
    "ds_write_b128 v36, v[44:47]\n"
    "s_cmp_ge_u32 s56, 4\n"
    "s_cbranch_scc1 .Lskipq9s\n"
    "ds_read_b128 v[44:47], v36 offset:8192\n"
    "s_waitcnt lgkmcnt(0)\n"
    "v_lshlrev_b32_e32 v40, 16, v44\n"
    "v_and_b32_e32 v41, 0xffff0000, v44\n"
    "v_lshlrev_b32_e32 v42, 16, v45\n"
    "v_and_b32_e32 v43, 0xffff0000, v45\n"
    "v_mul_f32_e32 v40, s36, v40\n"
    "v_mul_f32_e32 v41, s36, v41\n"
    "v_mul_f32_e32 v42, s36, v42\n"
    "v_mul_f32_e32 v43, s36, v43\n"
    "v_cvt_pk_bf16_f32 v44, v40, v41\n"
    "v_cvt_pk_bf16_f32 v45, v42, v43\n"
    "v_lshlrev_b32_e32 v40, 16, v46\n"
    "v_and_b32_e32 v41, 0xffff0000, v46\n"
    "v_lshlrev_b32_e32 v42, 16, v47\n"
    "v_and_b32_e32 v43, 0xffff0000, v47\n"
    "v_mul_f32_e32 v40, s36, v40\n"
    "v_mul_f32_e32 v41, s36, v41\n"
    "v_mul_f32_e32 v42, s36, v42\n"
    "v_mul_f32_e32 v43, s36, v43\n"
    "v_cvt_pk_bf16_f32 v46, v40, v41\n"
    "v_cvt_pk_bf16_f32 v47, v42, v43\n"
    "ds_write_b128 v36, v[44:47] offset:8192\n"
    "s_waitcnt lgkmcnt(0)\n"
    ".Lskipq9s:\n"
    "s_waitcnt vmcnt(4)\n"
    "ds_read_b128 v[160:163], v16\n"
    "ds_read_b128 v[164:167], v18\n"
    "ds_read_b128 v[168:171], v17\n"
    "ds_read_b128 v[172:175], v19\n"
    "s_waitcnt lgkmcnt(3)\n"
    "v_lshlrev_b32_e32 v40, 16, v160\n"
    "v_and_b32_e32 v41, 0xffff0000, v160\n"
    "v_lshlrev_b32_e32 v42, 16, v161\n"
    "v_and_b32_e32 v43, 0xffff0000, v161\n"
    "v_mul_f32_e32 v40, s36, v40\n"
    "v_mul_f32_e32 v41, s36, v41\n"
    "v_mul_f32_e32 v42, s36, v42\n"
    "v_mul_f32_e32 v43, s36, v43\n"
    "v_cvt_pk_bf16_f32 v160, v40, v41\n"
    "v_cvt_pk_bf16_f32 v161, v42, v43\n"
    "v_lshlrev_b32_e32 v40, 16, v162\n"
    "v_and_b32_e32 v41, 0xffff0000, v162\n"
    "v_lshlrev_b32_e32 v42, 16, v163\n"
    "v_and_b32_e32 v43, 0xffff0000, v163\n"
    "v_mul_f32_e32 v40, s36, v40\n"
    "v_mul_f32_e32 v41, s36, v41\n"
    "v_mul_f32_e32 v42, s36, v42\n"
    "v_mul_f32_e32 v43, s36, v43\n"
    "v_cvt_pk_bf16_f32 v162, v40, v41\n"
    "v_cvt_pk_bf16_f32 v163, v42, v43\n"
    "s_waitcnt lgkmcnt(2)\n"
    "v_lshlrev_b32_e32 v40, 16, v164\n"
    "v_and_b32_e32 v41, 0xffff0000, v164\n"
    "v_lshlrev_b32_e32 v42, 16, v165\n"
    "v_and_b32_e32 v43, 0xffff0000, v165\n"
    "v_mul_f32_e32 v40, s36, v40\n"
    "v_mul_f32_e32 v41, s36, v41\n"
    "v_mul_f32_e32 v42, s36, v42\n"
    "v_mul_f32_e32 v43, s36, v43\n"
    "v_cvt_pk_bf16_f32 v164, v40, v41\n"
    "v_cvt_pk_bf16_f32 v165, v42, v43\n"
    "v_lshlrev_b32_e32 v40, 16, v166\n"
    "v_and_b32_e32 v41, 0xffff0000, v166\n"
    "v_lshlrev_b32_e32 v42, 16, v167\n"
    "v_and_b32_e32 v43, 0xffff0000, v167\n"
    "v_mul_f32_e32 v40, s36, v40\n"
    "v_mul_f32_e32 v41, s36, v41\n"
    "v_mul_f32_e32 v42, s36, v42\n"
    "v_mul_f32_e32 v43, s36, v43\n"
    "v_cvt_pk_bf16_f32 v166, v40, v41\n"
    "v_cvt_pk_bf16_f32 v167, v42, v43\n"
    "s_waitcnt lgkmcnt(1)\n"
    "v_lshlrev_b32_e32 v40, 16, v168\n"
    "v_and_b32_e32 v41, 0xffff0000, v168\n"
    "v_lshlrev_b32_e32 v42, 16, v169\n"
    "v_and_b32_e32 v43, 0xffff0000, v169\n"
    "v_mul_f32_e32 v40, s36, v40\n"
    "v_mul_f32_e32 v41, s36, v41\n"
    "v_mul_f32_e32 v42, s36, v42\n"
    "v_mul_f32_e32 v43, s36, v43\n"
    "v_cvt_pk_bf16_f32 v168, v40, v41\n"
    "v_cvt_pk_bf16_f32 v169, v42, v43\n"
    "v_lshlrev_b32_e32 v40, 16, v170\n"
    "v_and_b32_e32 v41, 0xffff0000, v170\n"
    "v_lshlrev_b32_e32 v42, 16, v171\n"
    "v_and_b32_e32 v43, 0xffff0000, v171\n"
    "v_mul_f32_e32 v40, s36, v40\n"
    "v_mul_f32_e32 v41, s36, v41\n"
    "v_mul_f32_e32 v42, s36, v42\n"
    "v_mul_f32_e32 v43, s36, v43\n"
    "v_cvt_pk_bf16_f32 v170, v40, v41\n"
    "v_cvt_pk_bf16_f32 v171, v42, v43\n"
    "s_waitcnt lgkmcnt(0)\n"
    "v_lshlrev_b32_e32 v40, 16, v172\n"
    "v_and_b32_e32 v41, 0xffff0000, v172\n"
    "v_lshlrev_b32_e32 v42, 16, v173\n"
    "v_and_b32_e32 v43, 0xffff0000, v173\n"
    "v_mul_f32_e32 v40, s36, v40\n"
    "v_mul_f32_e32 v41, s36, v41\n"
    "v_mul_f32_e32 v42, s36, v42\n"
    "v_mul_f32_e32 v43, s36, v43\n"
    "v_cvt_pk_bf16_f32 v172, v40, v41\n"
    "v_cvt_pk_bf16_f32 v173, v42, v43\n"
    "v_lshlrev_b32_e32 v40, 16, v174\n"
    "v_and_b32_e32 v41, 0xffff0000, v174\n"
    "v_lshlrev_b32_e32 v42, 16, v175\n"
    "v_and_b32_e32 v43, 0xffff0000, v175\n"
    "v_mul_f32_e32 v40, s36, v40\n"
    "v_mul_f32_e32 v41, s36, v41\n"
    "v_mul_f32_e32 v42, s36, v42\n"
    "v_mul_f32_e32 v43, s36, v43\n"
    "v_cvt_pk_bf16_f32 v174, v40, v41\n"
    "v_cvt_pk_bf16_f32 v175, v42, v43\n"
    "s_waitcnt vmcnt(4) lgkmcnt(0)\n"
    "s_barrier\n"
    "ds_read_b128 v[88:91], v20\n"
    "ds_read_b128 v[96:99], v21\n"
    "ds_read_b128 v[92:95], v20 offset:4096\n"
    "ds_read_b128 v[100:103], v21 offset:4096\n"
    "ds_read_b128 v[104:107], v20 offset:8192\n"
    "ds_read_b128 v[108:111], v21 offset:8192\n"
    "ds_read_b128 v[208:211], v244\n"
    "ds_read_b128 v[212:215], v245\n"
    "ds_read_b128 v[216:219], v244 offset:4096\n"
    "ds_read_b128 v[220:223], v245 offset:4096\n"
    "s_waitcnt lgkmcnt(3)\n"
    "v_mfma_f32_16x16x32_bf16 v[240:243], v[208:211], v[88:91], 0\n"
    "s_add_u32 m0, s69, 0x20000\n"
    "s_add_u32 s61, s63, 0x100\n"
    "buffer_load_dwordx4 v7, s[40:43], s61 offen nt lds\n"
    "ds_read_b128 v[208:211], v244 offset:8192\n"
    "s_waitcnt lgkmcnt(1)\n"
    "v_mfma_f32_16x16x32_bf16 v[240:243], v[212:215], v[96:99], v[240:243]\n"
    "s_add_u32 m0, s69, 0x20400\n"
    "s_add_u32 s61, s63, 0xd00\n"
    "buffer_load_dwordx4 v8, s[40:43], s61 offen nt lds\n"
    "ds_read_b128 v[212:215], v245 offset:8192\n"
    "v_mfma_f32_16x16x32_bf16 v[240:243], v[216:219], v[92:95], v[240:243]\n"
    "s_add_u32 m0, s69, 0x20800\n"
    "s_add_u32 s61, s63, 0x1900\n"
    "buffer_load_dwordx4 v7, s[40:43], s61 offen nt lds\n"
    "ds_read_b128 v[216:219], v244\n"
    "v_mfma_f32_16x16x32_bf16 v[240:243], v[220:223], v[100:103], v[240:243]\n"
    "s_add_u32 m0, s69, 0x20c00\n"
    "s_add_u32 s61, s63, 0x2500\n"
    "buffer_load_dwordx4 v8, s[40:43], s61 offen nt lds\n"
    "ds_read_b128 v[220:223], v246\n"
    "s_waitcnt lgkmcnt(1)\n"
    "v_mfma_f32_16x16x32_bf16 v[240:243], v[208:211], v[104:107], v[240:243]\n"
    "s_add_u32 m0, s57, 0x6000\n"
    "s_add_u32 s61, s58, 0x6000\n"
    "buffer_load_dwordx4 v126, s[44:47], s61 offen nt lds\n"
    "ds_read_b128 v[208:211], v248\n"
    "v_mfma_f32_16x16x32_bf16 v[240:243], v[212:215], v[108:111], v[240:243]\n"
    "s_add_u32 m0, s57, 0x8000\n"
    "s_add_u32 s61, s59, 0x6000\n"
    "buffer_load_dwordx4 v126, s[44:47], s61 offen nt lds\n"
    "ds_read_b128 v[212:215], v250\n"
    "v_mfma_f32_16x16x32_bf16 v[176:179], v[216:219], v[160:163], 0\n"
    "s_add_u32 m0, s57, 0xa000\n"
    "s_add_u32 s61, s60, 0x6000\n"
    "buffer_load_dwordx4 v126, s[44:47], s61 offen nt lds\n"
    "v_mfma_f32_16x16x32_bf16 v[180:183], v[216:219], v[164:167], 0\n"
    "ds_read_b128 v[216:219], v245\n"
    "s_waitcnt lgkmcnt(1)\n"
    "v_mfma_f32_16x16x32_bf16 v[184:187], v[220:223], v[160:163], 0\n"
    "v_max3_f32 v124, v240, v241, v242\n"
    "v_max_f32_e32 v124, v124, v243\n"
    "v_mfma_f32_16x16x32_bf16 v[188:191], v[220:223], v[164:167], 0\n"
    "v_mov_b32_e32 v125, v124\n"
    "ds_read_b128 v[220:223], v247\n"
    "v_mfma_f32_16x16x32_bf16 v[192:195], v[208:211], v[160:163], 0\n"
    "v_permlane16_swap_b32_e32 v124, v125\n"
    "v_mfma_f32_16x16x32_bf16 v[196:199], v[208:211], v[164:167], 0\n"
    "v_max_f32_e32 v124, v124, v125\n"
    "ds_read_b128 v[208:211], v249\n"
    "v_mfma_f32_16x16x32_bf16 v[200:203], v[212:215], v[160:163], 0\n"
    "v_mov_b32_e32 v125, v124\n"
    "s_nop 1\n"
    "v_permlane32_swap_b32_e32 v124, v125\n"
    "v_mfma_f32_16x16x32_bf16 v[204:207], v[212:215], v[164:167], 0\n"
    "v_max_f32_e32 v124, v124, v125\n"
    "ds_read_b128 v[212:215], v251\n"
    "s_waitcnt lgkmcnt(1)\n"
    "v_mfma_f32_16x16x32_bf16 v[176:179], v[216:219], v[168:171], v[176:179]\n"
    "v_floor_f32_e64 v124, -v124\n"
    "ds_write_b32 v65, v124\n"
    "v_mfma_f32_16x16x32_bf16 v[180:183], v[216:219], v[172:175], v[180:183]\n"
    "ds_read_b128 v[216:219], v244 offset:4096\n"
    "v_mfma_f32_16x16x32_bf16 v[184:187], v[220:223], v[168:171], v[184:187]\n"
    "ds_write_b128 v32, v[24:27]\n"
    "v_mfma_f32_16x16x32_bf16 v[188:191], v[220:223], v[172:175], v[188:191]\n"
    "ds_read_b128 v[220:223], v246 offset:4096\n"
    "v_mfma_f32_16x16x32_bf16 v[192:195], v[208:211], v[168:171], v[192:195]\n"
    "ds_write_b128 v32, v[28:31] offset:1024\n"
    "v_mfma_f32_16x16x32_bf16 v[196:199], v[208:211], v[172:175], v[196:199]\n"
    "ds_write_b32 v33, v27\n"
    "ds_read_b128 v[208:211], v248 offset:4096\n"
    "s_waitcnt lgkmcnt(2)\n"
    "v_mfma_f32_16x16x32_bf16 v[200:203], v[212:215], v[168:171], v[200:203]\n"
    "s_waitcnt vmcnt(7)\n"
    "ds_read_b128 v[128:131], v16 offset:49152\n"
    "ds_read_b128 v[132:135], v18 offset:49152\n"
    "ds_read_b128 v[136:139], v17 offset:49152\n"
    "ds_read_b128 v[140:143], v19 offset:49152\n"
    "s_waitcnt lgkmcnt(3)\n"
    "v_lshlrev_b32_e32 v40, 16, v128\n"
    "v_and_b32_e32 v41, 0xffff0000, v128\n"
    "v_lshlrev_b32_e32 v42, 16, v129\n"
    "v_and_b32_e32 v43, 0xffff0000, v129\n"
    "v_mul_f32_e32 v40, s36, v40\n"
    "v_mul_f32_e32 v41, s36, v41\n"
    "v_mul_f32_e32 v42, s36, v42\n"
    "v_mul_f32_e32 v43, s36, v43\n"
    "v_cvt_pk_bf16_f32 v128, v40, v41\n"
    "v_cvt_pk_bf16_f32 v129, v42, v43\n"
    "v_lshlrev_b32_e32 v40, 16, v130\n"
    "v_and_b32_e32 v41, 0xffff0000, v130\n"
    "v_lshlrev_b32_e32 v42, 16, v131\n"
    "v_and_b32_e32 v43, 0xffff0000, v131\n"
    "v_mul_f32_e32 v40, s36, v40\n"
    "v_mul_f32_e32 v41, s36, v41\n"
    "v_mul_f32_e32 v42, s36, v42\n"
    "v_mul_f32_e32 v43, s36, v43\n"
    "v_cvt_pk_bf16_f32 v130, v40, v41\n"
    "v_mfma_f32_16x16x32_bf16 v[204:207], v[212:215], v[172:175], v[204:207]\n"
    "v_cvt_pk_bf16_f32 v131, v42, v43\n"
    "s_waitcnt lgkmcnt(0)\n"
    "v_lshlrev_b32_e32 v40, 16, v132\n"
    "v_and_b32_e32 v41, 0xffff0000, v132\n"
    "v_lshlrev_b32_e32 v42, 16, v133\n"
    "v_and_b32_e32 v43, 0xffff0000, v133\n"
    "v_mul_f32_e32 v40, s36, v40\n"
    "v_mul_f32_e32 v41, s36, v41\n"
    "v_mul_f32_e32 v42, s36, v42\n"
    "v_mul_f32_e32 v43, s36, v43\n"
    "v_cvt_pk_bf16_f32 v132, v40, v41\n"
    "v_cvt_pk_bf16_f32 v133, v42, v43\n"
    "v_lshlrev_b32_e32 v40, 16, v134\n"
    "v_and_b32_e32 v41, 0xffff0000, v134\n"
    "v_lshlrev_b32_e32 v42, 16, v135\n"
    "v_and_b32_e32 v43, 0xffff0000, v135\n"
    "v_mul_f32_e32 v40, s36, v40\n"
    "v_mul_f32_e32 v41, s36, v41\n"
    "v_mul_f32_e32 v42, s36, v42\n"
    "v_mul_f32_e32 v43, s36, v43\n"
    "v_cvt_pk_bf16_f32 v134, v40, v41\n"
    "v_cvt_pk_bf16_f32 v135, v42, v43\n"
    "ds_read_b128 v[212:215], v250 offset:4096\n"
    "v_mfma_f32_16x16x32_bf16 v[176:179], v[216:219], v[128:131], v[176:179]\n"
    "v_lshlrev_b32_e32 v40, 16, v136\n"
    "v_and_b32_e32 v41, 0xffff0000, v136\n"
    "v_lshlrev_b32_e32 v42, 16, v137\n"
    "v_and_b32_e32 v43, 0xffff0000, v137\n"
    "v_mul_f32_e32 v40, s36, v40\n"
    "v_mul_f32_e32 v41, s36, v41\n"
    "v_mul_f32_e32 v42, s36, v42\n"
    "v_mfma_f32_16x16x32_bf16 v[180:183], v[216:219], v[132:135], v[180:183]\n"
    "v_mul_f32_e32 v43, s36, v43\n"
    "v_cvt_pk_bf16_f32 v136, v40, v41\n"
    "v_cvt_pk_bf16_f32 v137, v42, v43\n"
    "v_lshlrev_b32_e32 v40, 16, v138\n"
    "v_and_b32_e32 v41, 0xffff0000, v138\n"
    "v_lshlrev_b32_e32 v42, 16, v139\n"
    "ds_read_b128 v[216:219], v245 offset:4096\n"
    "v_mfma_f32_16x16x32_bf16 v[184:187], v[220:223], v[128:131], v[184:187]\n"
    "v_and_b32_e32 v43, 0xffff0000, v139\n"
    "v_mul_f32_e32 v40, s36, v40\n"
    "v_mul_f32_e32 v41, s36, v41\n"
    "v_mul_f32_e32 v42, s36, v42\n"
    "v_mul_f32_e32 v43, s36, v43\n"
    "v_cvt_pk_bf16_f32 v138, v40, v41\n"
    "v_cvt_pk_bf16_f32 v139, v42, v43\n"
    "v_mfma_f32_16x16x32_bf16 v[188:191], v[220:223], v[132:135], v[188:191]\n"
    "v_lshlrev_b32_e32 v40, 16, v140\n"
    "v_and_b32_e32 v41, 0xffff0000, v140\n"
    "v_lshlrev_b32_e32 v42, 16, v141\n"
    "v_and_b32_e32 v43, 0xffff0000, v141\n"
    "v_mul_f32_e32 v40, s36, v40\n"
    "v_mul_f32_e32 v41, s36, v41\n"
    "v_mul_f32_e32 v42, s36, v42\n"
    "ds_read_b128 v[220:223], v247 offset:4096\n"
    "v_mfma_f32_16x16x32_bf16 v[192:195], v[208:211], v[128:131], v[192:195]\n"
    "v_mul_f32_e32 v43, s36, v43\n"
    "v_cvt_pk_bf16_f32 v140, v40, v41\n"
    "v_cvt_pk_bf16_f32 v141, v42, v43\n"
    "v_lshlrev_b32_e32 v40, 16, v142\n"
    "v_and_b32_e32 v41, 0xffff0000, v142\n"
    "v_lshlrev_b32_e32 v42, 16, v143\n"
    "v_and_b32_e32 v43, 0xffff0000, v143\n"
    "v_mfma_f32_16x16x32_bf16 v[196:199], v[208:211], v[132:135], v[196:199]\n"
    "v_mul_f32_e32 v40, s36, v40\n"
    "v_mul_f32_e32 v41, s36, v41\n"
    "v_mul_f32_e32 v42, s36, v42\n"
    "v_mul_f32_e32 v43, s36, v43\n"
    "v_cvt_pk_bf16_f32 v142, v40, v41\n"
    "v_cvt_pk_bf16_f32 v143, v42, v43\n"
    "ds_read_b128 v[208:211], v249 offset:4096\n"
    "s_waitcnt lgkmcnt(1)\n"
    "v_mfma_f32_16x16x32_bf16 v[200:203], v[212:215], v[128:131], v[200:203]\n"
    "v_mfma_f32_16x16x32_bf16 v[204:207], v[212:215], v[132:135], v[204:207]\n"
    "ds_read_b128 v[212:215], v251 offset:4096\n"
    "v_mfma_f32_16x16x32_bf16 v[176:179], v[216:219], v[136:139], v[176:179]\n"
    "v_mfma_f32_16x16x32_bf16 v[180:183], v[216:219], v[140:143], v[180:183]\n"
    "ds_read_b128 v[216:219], v244 offset:8192\n"
    "v_mfma_f32_16x16x32_bf16 v[184:187], v[220:223], v[136:139], v[184:187]\n"
    "v_mfma_f32_16x16x32_bf16 v[188:191], v[220:223], v[140:143], v[188:191]\n"
    "ds_read_b128 v[220:223], v246 offset:8192\n"
    "s_waitcnt lgkmcnt(1)\n"
    "v_mfma_f32_16x16x32_bf16 v[192:195], v[208:211], v[136:139], v[192:195]\n"
    "v_mfma_f32_16x16x32_bf16 v[196:199], v[208:211], v[140:143], v[196:199]\n"
    "ds_read_b128 v[208:211], v245 offset:8192\n"
    "v_mfma_f32_16x16x32_bf16 v[200:203], v[212:215], v[136:139], v[200:203]\n"
    "v_add_u32_e32 v16, 0x14000, v16\n"
    "v_add_u32_e32 v17, 0x14000, v17\n"
    "v_add_u32_e32 v18, 0x14000, v18\n"
    "v_add_u32_e32 v19, 0x14000, v19\n"
    "s_waitcnt vmcnt(3)\n"
    "ds_read_b128 v[144:147], v16\n"
    "ds_read_b128 v[148:151], v18\n"
    "ds_read_b128 v[152:155], v17\n"
    "ds_read_b128 v[156:159], v19\n"
    "s_waitcnt lgkmcnt(3)\n"
    "v_lshlrev_b32_e32 v40, 16, v144\n"
    "v_and_b32_e32 v41, 0xffff0000, v144\n"
    "v_lshlrev_b32_e32 v42, 16, v145\n"
    "v_and_b32_e32 v43, 0xffff0000, v145\n"
    "v_mul_f32_e32 v40, s36, v40\n"
    "v_mul_f32_e32 v41, s36, v41\n"
    "v_mul_f32_e32 v42, s36, v42\n"
    "v_mul_f32_e32 v43, s36, v43\n"
    "v_cvt_pk_bf16_f32 v144, v40, v41\n"
    "v_cvt_pk_bf16_f32 v145, v42, v43\n"
    "v_lshlrev_b32_e32 v40, 16, v146\n"
    "v_and_b32_e32 v41, 0xffff0000, v146\n"
    "v_lshlrev_b32_e32 v42, 16, v147\n"
    "v_and_b32_e32 v43, 0xffff0000, v147\n"
    "v_mul_f32_e32 v40, s36, v40\n"
    "v_mul_f32_e32 v41, s36, v41\n"
    "v_mul_f32_e32 v42, s36, v42\n"
    "v_mfma_f32_16x16x32_bf16 v[204:207], v[212:215], v[140:143], v[204:207]\n"
    "v_mul_f32_e32 v43, s36, v43\n"
    "v_cvt_pk_bf16_f32 v146, v40, v41\n"
    "v_cvt_pk_bf16_f32 v147, v42, v43\n"
    "s_waitcnt lgkmcnt(0)\n"
    "v_lshlrev_b32_e32 v40, 16, v148\n"
    "v_and_b32_e32 v41, 0xffff0000, v148\n"
    "v_lshlrev_b32_e32 v42, 16, v149\n"
    "v_and_b32_e32 v43, 0xffff0000, v149\n"
    "v_mul_f32_e32 v40, s36, v40\n"
    "v_mul_f32_e32 v41, s36, v41\n"
    "v_mul_f32_e32 v42, s36, v42\n"
    "v_mul_f32_e32 v43, s36, v43\n"
    "v_cvt_pk_bf16_f32 v148, v40, v41\n"
    "v_cvt_pk_bf16_f32 v149, v42, v43\n"
    "v_lshlrev_b32_e32 v40, 16, v150\n"
    "v_and_b32_e32 v41, 0xffff0000, v150\n"
    "v_lshlrev_b32_e32 v42, 16, v151\n"
    "v_and_b32_e32 v43, 0xffff0000, v151\n"
    "v_mul_f32_e32 v40, s36, v40\n"
    "v_mul_f32_e32 v41, s36, v41\n"
    "v_mul_f32_e32 v42, s36, v42\n"
    "v_mul_f32_e32 v43, s36, v43\n"
    "v_cvt_pk_bf16_f32 v150, v40, v41\n"
    "v_cvt_pk_bf16_f32 v151, v42, v43\n"
    "ds_read_b128 v[212:215], v247 offset:8192\n"
    "v_mfma_f32_16x16x32_bf16 v[176:179], v[216:219], v[144:147], v[176:179]\n"
    "v_lshlrev_b32_e32 v40, 16, v152\n"
    "v_and_b32_e32 v41, 0xffff0000, v152\n"
    "v_lshlrev_b32_e32 v42, 16, v153\n"
    "v_and_b32_e32 v43, 0xffff0000, v153\n"
    "v_mul_f32_e32 v40, s36, v40\n"
    "v_mul_f32_e32 v41, s36, v41\n"
    "v_mul_f32_e32 v42, s36, v42\n"
    "v_mul_f32_e32 v43, s36, v43\n"
    "v_cvt_pk_bf16_f32 v152, v40, v41\n"
    "v_cvt_pk_bf16_f32 v153, v42, v43\n"
    "v_mfma_f32_16x16x32_bf16 v[180:183], v[216:219], v[148:151], v[180:183]\n"
    "v_lshlrev_b32_e32 v40, 16, v154\n"
    "v_and_b32_e32 v41, 0xffff0000, v154\n"
    "v_lshlrev_b32_e32 v42, 16, v155\n"
    "v_and_b32_e32 v43, 0xffff0000, v155\n"
    "v_mul_f32_e32 v40, s36, v40\n"
    "v_mul_f32_e32 v41, s36, v41\n"
    "v_mul_f32_e32 v42, s36, v42\n"
    "v_mul_f32_e32 v43, s36, v43\n"
    "v_cvt_pk_bf16_f32 v154, v40, v41\n"
    "v_cvt_pk_bf16_f32 v155, v42, v43\n"
    "ds_read_b128 v[216:219], v248 offset:8192\n"
    "v_mfma_f32_16x16x32_bf16 v[184:187], v[220:223], v[144:147], v[184:187]\n"
    "v_lshlrev_b32_e32 v40, 16, v156\n"
    "v_and_b32_e32 v41, 0xffff0000, v156\n"
    "v_lshlrev_b32_e32 v42, 16, v157\n"
    "v_and_b32_e32 v43, 0xffff0000, v157\n"
    "v_mul_f32_e32 v40, s36, v40\n"
    "v_mul_f32_e32 v41, s36, v41\n"
    "v_mul_f32_e32 v42, s36, v42\n"
    "v_mul_f32_e32 v43, s36, v43\n"
    "v_cvt_pk_bf16_f32 v156, v40, v41\n"
    "v_cvt_pk_bf16_f32 v157, v42, v43\n"
    "v_mfma_f32_16x16x32_bf16 v[188:191], v[220:223], v[148:151], v[188:191]\n"
    "v_lshlrev_b32_e32 v40, 16, v158\n"
    "v_and_b32_e32 v41, 0xffff0000, v158\n"
    "v_lshlrev_b32_e32 v42, 16, v159\n"
    "v_and_b32_e32 v43, 0xffff0000, v159\n"
    "v_mul_f32_e32 v40, s36, v40\n"
    "v_mul_f32_e32 v41, s36, v41\n"
    "v_mul_f32_e32 v42, s36, v42\n"
    "v_mul_f32_e32 v43, s36, v43\n"
    "v_cvt_pk_bf16_f32 v158, v40, v41\n"
    "v_cvt_pk_bf16_f32 v159, v42, v43\n"
    "ds_read_b128 v[220:223], v250 offset:8192\n"
    "v_mfma_f32_16x16x32_bf16 v[176:179], v[208:211], v[152:155], v[176:179]\n"
    "s_add_u32 m0, s57, 0xc000\n"
    "s_add_u32 s61, s58, 0xc000\n"
    "buffer_load_dwordx4 v126, s[44:47], s61 offen nt lds\n"
    "v_mfma_f32_16x16x32_bf16 v[180:183], v[208:211], v[156:159], v[180:183]\n"
    "ds_read_b128 v[208:211], v249 offset:8192\n"
    "s_waitcnt lgkmcnt(1)\n"
    "v_mfma_f32_16x16x32_bf16 v[184:187], v[212:215], v[152:155], v[184:187]\n"
    "v_mfma_f32_16x16x32_bf16 v[188:191], v[212:215], v[156:159], v[188:191]\n"
    "ds_read_b128 v[212:215], v251 offset:8192\n"
    "v_mfma_f32_16x16x32_bf16 v[192:195], v[216:219], v[144:147], v[192:195]\n"
    "s_add_u32 m0, s57, 0xe000\n"
    "s_add_u32 s61, s59, 0xc000\n"
    "buffer_load_dwordx4 v126, s[44:47], s61 offen nt lds\n"
    "v_max3_f32 v124, v176, v177, v178\n"
    "s_nop 0\n"
    "v_max3_f32 v124, v124, v179, v184\n"
    "v_max3_f32 v124, v124, v185, v186\n"
    "v_max_f32_e32 v124, v124, v187\n"
    "v_max3_f32 v125, v180, v181, v182\n"
    "v_max3_f32 v125, v125, v183, v188\n"
    "v_max3_f32 v125, v125, v189, v190\n"
    "v_max_f32_e32 v125, v125, v191\n"
    "v_mov_b32_e32 v70, v124\n"
    "s_nop 1\n"
    "v_permlane16_swap_b32_e32 v124, v70\n"
    "v_max_f32_e32 v124, v124, v70\n"
    "v_mov_b32_e32 v70, v124\n"
    "v_mfma_f32_16x16x32_bf16 v[196:199], v[216:219], v[148:151], v[196:199]\n"
    "s_nop 0\n"
    "v_permlane32_swap_b32_e32 v124, v70\n"
    "v_max_f32_e32 v124, v124, v70\n"
    "v_mov_b32_e32 v70, v125\n"
    "s_nop 1\n"
    "v_permlane16_swap_b32_e32 v125, v70\n"
    "v_max_f32_e32 v125, v125, v70\n"
    "v_mov_b32_e32 v70, v125\n"
    "s_nop 1\n"
    "v_permlane32_swap_b32_e32 v125, v70\n"
    "v_max_f32_e32 v125, v125, v70\n"
    "v_floor_f32_e64 v112, -v124\n"
    "v_floor_f32_e64 v116, -v125\n"
    "v_mov_b32_e32 v113, v112\n"
    "v_mov_b32_e32 v114, v112\n"
    "v_mov_b32_e32 v115, v112\n"
    "v_mfma_f32_16x16x32_bf16 v[200:203], v[220:223], v[144:147], v[200:203]\n"
    "v_mov_b32_e32 v117, v116\n"
    "v_mov_b32_e32 v118, v116\n"
    "v_mov_b32_e32 v119, v116\n"
    "v_add_f32_e32 v176, v112, v176\n"
    "v_add_f32_e32 v177, v112, v177\n"
    "v_add_f32_e32 v178, v112, v178\n"
    "v_exp_f32_e32 v176, v176\n"
    "v_add_f32_e32 v179, v112, v179\n"
    "v_exp_f32_e32 v177, v177\n"
    "v_add_f32_e32 v180, v116, v180\n"
    "v_exp_f32_e32 v178, v178\n"
    "v_mfma_f32_16x16x32_bf16 v[204:207], v[220:223], v[148:151], v[204:207]\n"
    "v_add_f32_e32 v181, v116, v181\n"
    "v_exp_f32_e32 v179, v179\n"
    "v_add_f32_e32 v124, v176, v177\n"
    "v_cvt_pk_bf16_f32 v0, v176, v177\n"
    "v_add_f32_e32 v182, v116, v182\n"
    "v_exp_f32_e32 v180, v180\n"
    "v_add_f32_e32 v124, v178, v124\n"
    "v_add_f32_e32 v183, v116, v183\n"
    "v_exp_f32_e32 v181, v181\n"
    "s_waitcnt lgkmcnt(0)\n"
    "v_mfma_f32_16x16x32_bf16 v[192:195], v[208:211], v[152:155], v[192:195]\n"
    "s_add_u32 m0, s57, 0x10000\n"
    "s_add_u32 s61, s60, 0xc000\n"
    "buffer_load_dwordx4 v126, s[44:47], s61 offen nt lds\n"
    "v_add_f32_e32 v124, v179, v124\n"
    "v_cvt_pk_bf16_f32 v1, v178, v179\n"
    "v_add_f32_e32 v184, v112, v184\n"
    "v_exp_f32_e32 v182, v182\n"
    "v_add_f32_e32 v185, v112, v185\n"
    "v_exp_f32_e32 v183, v183\n"
    "v_add_f32_e32 v125, v180, v181\n"
    "v_cvt_pk_bf16_f32 v4, v180, v181\n"
    "v_add_f32_e32 v186, v112, v186\n"
    "v_exp_f32_e32 v184, v184\n"
    "v_mfma_f32_16x16x32_bf16 v[196:199], v[208:211], v[156:159], v[196:199]\n"
    "v_add_f32_e32 v125, v182, v125\n"
    "v_add_f32_e32 v187, v112, v187\n"
    "v_exp_f32_e32 v185, v185\n"
    "v_add_f32_e32 v125, v183, v125\n"
    "v_cvt_pk_bf16_f32 v5, v182, v183\n"
    "v_add_f32_e32 v188, v116, v188\n"
    "v_exp_f32_e32 v186, v186\n"
    "v_add_f32_e32 v124, v184, v124\n"
    "v_add_f32_e32 v189, v116, v189\n"
    "v_mfma_f32_16x16x32_bf16 v[200:203], v[212:215], v[152:155], v[200:203]\n"
    "v_exp_f32_e32 v187, v187\n"
    "v_add_f32_e32 v124, v185, v124\n"
    "v_cvt_pk_bf16_f32 v2, v184, v185\n"
    "v_add_f32_e32 v190, v116, v190\n"
    "v_exp_f32_e32 v188, v188\n"
    "v_add_f32_e32 v124, v186, v124\n"
    "v_add_f32_e32 v191, v116, v191\n"
    "v_exp_f32_e32 v189, v189\n"
    "v_add_f32_e32 v124, v187, v124\n"
    "v_cvt_pk_bf16_f32 v3, v186, v187\n"
    "v_mfma_f32_16x16x32_bf16 v[204:207], v[212:215], v[156:159], v[204:207]\n"
    "v_exp_f32_e32 v190, v190\n"
    "v_add_f32_e32 v125, v188, v125\n"
    "v_exp_f32_e32 v191, v191\n"
    "v_add_f32_e32 v125, v189, v125\n"
    "v_cvt_pk_bf16_f32 v6, v188, v189\n"
    "v_add_f32_e32 v125, v190, v125\n"
    "v_add_f32_e32 v125, v191, v125\n"
    "v_cvt_pk_bf16_f32 v7, v190, v191\n"
    "v_mov_b32_e32 v252, v124\n"
    "v_mov_b32_e32 v253, v125\n"
    "s_waitcnt vmcnt(3) lgkmcnt(0)\n"
    "s_barrier\n"
    "s_setprio 3\n"
    "ds_read_b128 v[216:219], v244 offset:24576\n"
    "ds_read_b128 v[220:223], v245 offset:24576\n"
    "ds_read_b128 v[208:211], v244 offset:28672\n"
    "s_waitcnt lgkmcnt(2)\n"
    "v_mfma_f32_16x16x32_bf16 v[176:179], v[216:219], v[160:163], v[112:115]\n"
    "s_add_u32 m0, s57, 0x0\n"
    "s_add_u32 s61, s58, 0x12000\n"
    "buffer_load_dwordx4 v126, s[44:47], s61 offen nt lds\n"
    "v_add_f32_e32 v192, v112, v192\n"
    "v_add_f32_e32 v193, v112, v193\n"
    "ds_read_b128 v[212:215], v245 offset:28672\n"
    "v_mfma_f32_16x16x32_bf16 v[180:183], v[216:219], v[164:167], v[116:119]\n"
    "v_add_f32_e32 v194, v112, v194\n"
    "v_exp_f32_e32 v192, v192\n"
    "ds_read_b128 v[216:219], v244 offset:32768\n"
    "s_waitcnt lgkmcnt(1)\n"
    "v_mfma_f32_16x16x32_bf16 v[176:179], v[220:223], v[168:171], v[176:179]\n"
    "ds_read_b128 v[66:69], v64\n"
    "s_waitcnt lgkmcnt(0)\n"
    "v_min_f32_e32 v66, v66, v67\n"
    "v_min_f32_e32 v68, v68, v69\n"
    "v_add_f32_e32 v195, v112, v195\n"
    "v_mfma_f32_16x16x32_bf16 v[180:183], v[220:223], v[172:175], v[180:183]\n"
    "v_exp_f32_e32 v193, v193\n"
    "v_add_f32_e32 v196, v116, v196\n"
    "ds_read_b128 v[220:223], v245 offset:32768\n"
    "v_mfma_f32_16x16x32_bf16 v[176:179], v[208:211], v[128:131], v[176:179]\n"
    "v_min_f32_e32 v254, v66, v68\n"
    "v_mov_b32_e32 v120, v254\n"
    "v_exp_f32_e32 v194, v194\n"
    "v_mfma_f32_16x16x32_bf16 v[180:183], v[208:211], v[132:135], v[180:183]\n"
    "v_add_f32_e32 v197, v116, v197\n"
    "v_exp_f32_e32 v195, v195\n"
    "v_add_f32_e32 v124, v192, v193\n"
    "ds_read_b128 v[208:211], v246 offset:24576\n"
    "v_mfma_f32_16x16x32_bf16 v[176:179], v[212:215], v[136:139], v[176:179]\n"
    "v_mov_b32_e32 v121, v254\n"
    "v_mov_b32_e32 v122, v254\n"
    "v_mov_b32_e32 v123, v254\n"
    "v_cvt_pk_bf16_f32 v8, v192, v193\n"
    "v_mfma_f32_16x16x32_bf16 v[180:183], v[212:215], v[140:143], v[180:183]\n"
    "v_add_f32_e32 v198, v116, v198\n"
    "v_exp_f32_e32 v196, v196\n"
    "v_add_f32_e32 v124, v194, v124\n"
    "ds_read_b128 v[212:215], v247 offset:24576\n"
    "v_mfma_f32_16x16x32_bf16 v[176:179], v[216:219], v[144:147], v[176:179]\n"
    "v_add_f32_e32 v240, v254, v240\n"
    "v_add_f32_e32 v241, v254, v241\n"
    "v_add_f32_e32 v242, v254, v242\n"
    "v_add_f32_e32 v199, v116, v199\n"
    "v_mfma_f32_16x16x32_bf16 v[180:183], v[216:219], v[148:151], v[180:183]\n"
    "v_exp_f32_e32 v197, v197\n"
    "v_add_f32_e32 v124, v195, v124\n"
    "v_cvt_pk_bf16_f32 v9, v194, v195\n"
    "ds_read_b128 v[216:219], v246 offset:28672\n"
    "s_waitcnt lgkmcnt(1)\n"
    "v_mfma_f32_16x16x32_bf16 v[176:179], v[220:223], v[152:155], v[176:179]\n"
    "v_add_f32_e32 v243, v254, v243\n"
    "v_exp_f32_e32 v240, v240\n"
    "v_add_f32_e32 v200, v112, v200\n"
    "v_mfma_f32_16x16x32_bf16 v[180:183], v[220:223], v[156:159], v[180:183]\n"
    "v_exp_f32_e32 v198, v198\n"
    "v_add_f32_e32 v201, v112, v201\n"
    "ds_read_b128 v[220:223], v247 offset:28672\n"
    "v_mfma_f32_16x16x32_bf16 v[184:187], v[208:211], v[160:163], v[112:115]\n"
    "v_exp_f32_e32 v241, v241\n"
    "v_exp_f32_e32 v199, v199\n"
    "v_mfma_f32_16x16x32_bf16 v[188:191], v[208:211], v[164:167], v[116:119]\n"
    "v_add_f32_e32 v125, v196, v197\n"
    "v_cvt_pk_bf16_f32 v12, v196, v197\n"
    "v_add_f32_e32 v202, v112, v202\n"
    "ds_read_b128 v[208:211], v246 offset:32768\n"
    "v_mfma_f32_16x16x32_bf16 v[184:187], v[212:215], v[168:171], v[184:187]\n"
    "v_exp_f32_e32 v242, v242\n"
    "v_exp_f32_e32 v200, v200\n"
    "v_mfma_f32_16x16x32_bf16 v[188:191], v[212:215], v[172:175], v[188:191]\n"
    "v_add_f32_e32 v125, v198, v125\n"
    "v_add_f32_e32 v203, v112, v203\n"
    "v_exp_f32_e32 v201, v201\n"
    "ds_read_b128 v[212:215], v247 offset:32768\n"
    "s_waitcnt lgkmcnt(1)\n"
    "v_mfma_f32_16x16x32_bf16 v[184:187], v[216:219], v[128:131], v[184:187]\n"
    "v_exp_f32_e32 v243, v243\n"
    "v_cvt_pk_bf16_f32 v224, v240, v241\n"
    "v_add_f32_e32 v125, v199, v125\n"
    "v_mfma_f32_16x16x32_bf16 v[188:191], v[216:219], v[132:135], v[188:191]\n"
    "v_cvt_pk_bf16_f32 v13, v198, v199\n"
    "v_add_f32_e32 v204, v116, v204\n"
    "v_exp_f32_e32 v202, v202\n"
    "ds_read_b128 v[216:219], v244 offset:24576\n"
    "v_mfma_f32_16x16x32_bf16 v[184:187], v[220:223], v[136:139], v[184:187]\n"
    "s_setprio 2\n"
    "v_add_f32_e32 v240, v240, v241\n"
    "v_cvt_pk_bf16_f32 v225, v242, v243\n"
    "v_add_f32_e32 v124, v200, v124\n"
    "v_add_f32_e32 v205, v116, v205\n"
    "v_mfma_f32_16x16x32_bf16 v[188:191], v[220:223], v[140:143], v[188:191]\n"
    "v_exp_f32_e32 v203, v203\n"
    "v_add_f32_e32 v124, v201, v124\n"
    "ds_read_b128 v[220:223], v245 offset:24576\n"
    "v_mfma_f32_16x16x32_bf16 v[184:187], v[208:211], v[144:147], v[184:187]\n"
    "v_add_f32_e32 v242, v242, v243\n"
    "v_add_f32_e32 v240, v240, v242\n"
    "v_mov_b32_e32 v255, v240\n"
    "v_cvt_pk_bf16_f32 v10, v200, v201\n"
    "v_mfma_f32_16x16x32_bf16 v[188:191], v[208:211], v[148:151], v[188:191]\n"
    "v_add_f32_e32 v206, v116, v206\n"
    "v_exp_f32_e32 v204, v204\n"
    "v_add_f32_e32 v124, v202, v124\n"
    "ds_read_b128 v[208:211], v244 offset:28672\n"
    "s_waitcnt lgkmcnt(1)\n"
    "v_mfma_f32_16x16x32_bf16 v[184:187], v[212:215], v[152:155], v[184:187]\n"
    "s_add_u32 m0, s57, 0x2000\n"
    "s_add_u32 s61, s59, 0x12000\n"
    "buffer_load_dwordx4 v126, s[44:47], s61 offen nt lds\n"
    "v_add_f32_e32 v207, v116, v207\n"
    "v_exp_f32_e32 v205, v205\n"
    "v_mfma_f32_16x16x32_bf16 v[188:191], v[212:215], v[156:159], v[188:191]\n"
    "v_add_f32_e32 v124, v203, v124\n"
    "v_cvt_pk_bf16_f32 v11, v202, v203\n"
    "ds_read_b128 v[212:215], v245 offset:28672\n"
    "v_mfma_f32_16x16x32_bf16 v[240:243], v[216:219], v[88:91], v[120:123]\n"
    "v_exp_f32_e32 v206, v206\n"
    "ds_read_b128 v[216:219], v244 offset:32768\n"
    "v_mfma_f32_16x16x32_bf16 v[240:243], v[220:223], v[96:99], v[240:243]\n"
    "v_add_f32_e32 v125, v204, v125\n"
    "v_exp_f32_e32 v207, v207\n"
    "ds_read_b128 v[220:223], v245 offset:32768\n"
    "s_waitcnt lgkmcnt(1)\n"
    "v_mfma_f32_16x16x32_bf16 v[240:243], v[208:211], v[92:95], v[240:243]\n"
    "v_add_f32_e32 v125, v205, v125\n"
    "v_cvt_pk_bf16_f32 v14, v204, v205\n"
    "ds_read_b128 v[208:211], v248 offset:24576\n"
    "v_mfma_f32_16x16x32_bf16 v[240:243], v[212:215], v[100:103], v[240:243]\n"
    "v_add_f32_e32 v125, v206, v125\n"
    "v_add_f32_e32 v125, v207, v125\n"
    "v_cvt_pk_bf16_f32 v15, v206, v207\n"
    "ds_read_b128 v[212:215], v249 offset:24576\n"
    "v_mfma_f32_16x16x32_bf16 v[240:243], v[216:219], v[104:107], v[240:243]\n"
    "v_add_f32_e32 v192, v124, v125\n"
    "v_cmp_nge_f32_e32 vcc, s38, v192\n"
    "ds_read_b128 v[216:219], v248 offset:28672\n"
    "s_waitcnt lgkmcnt(1)\n"
    "v_mfma_f32_16x16x32_bf16 v[240:243], v[220:223], v[108:111], v[240:243]\n"
    "s_cbranch_vccnz .Lrare_a1\n"
    ".Lback_a1:\n"
    "v_add_f32_e32 v252, v252, v124\n"
    "v_add_f32_e32 v253, v253, v125\n"
    "ds_read_b128 v[220:223], v249 offset:28672\n"
    "v_mfma_f32_16x16x32_bf16 v[192:195], v[208:211], v[160:163], v[112:115]\n"
    "v_exp_f32_e32 v176, v176\n"
    "v_exp_f32_e32 v177, v177\n"
    "v_mfma_f32_16x16x32_bf16 v[196:199], v[208:211], v[164:167], v[116:119]\n"
    "v_exp_f32_e32 v178, v178\n"
    "ds_read_b128 v[208:211], v248 offset:32768\n"
    "v_mfma_f32_16x16x32_bf16 v[192:195], v[212:215], v[168:171], v[192:195]\n"
    "v_exp_f32_e32 v179, v179\n"
    "v_add_f32_e32 v124, v176, v177\n"
    "v_mfma_f32_16x16x32_bf16 v[196:199], v[212:215], v[172:175], v[196:199]\n"
    "v_cvt_pk_bf16_f32 v16, v176, v177\n"
    "v_exp_f32_e32 v180, v180\n"
    "ds_read_b128 v[212:215], v249 offset:32768\n"
    "s_waitcnt lgkmcnt(1)\n"
    "v_mfma_f32_16x16x32_bf16 v[192:195], v[216:219], v[128:131], v[192:195]\n"
    "v_add_f32_e32 v124, v178, v124\n"
    "v_exp_f32_e32 v181, v181\n"
    "v_add_f32_e32 v124, v179, v124\n"
    "v_mfma_f32_16x16x32_bf16 v[196:199], v[216:219], v[132:135], v[196:199]\n"
    "v_cvt_pk_bf16_f32 v17, v178, v179\n"
    "v_exp_f32_e32 v182, v182\n"
    "ds_read_b128 v[216:219], v250 offset:24576\n"
    "v_mfma_f32_16x16x32_bf16 v[192:195], v[220:223], v[136:139], v[192:195]\n"
    "s_setprio 1\n"
    "v_exp_f32_e32 v183, v183\n"
    "v_add_f32_e32 v125, v180, v181\n"
    "v_mfma_f32_16x16x32_bf16 v[196:199], v[220:223], v[140:143], v[196:199]\n"
    "v_cvt_pk_bf16_f32 v20, v180, v181\n"
    "v_exp_f32_e32 v184, v184\n"
    "ds_read_b128 v[220:223], v251 offset:24576\n"
    "v_mfma_f32_16x16x32_bf16 v[192:195], v[208:211], v[144:147], v[192:195]\n"
    "s_add_u32 m0, s57, 0x4000\n"
    "s_add_u32 s61, s60, 0x12000\n"
    "buffer_load_dwordx4 v126, s[44:47], s61 offen nt lds\n"
    "v_add_f32_e32 v125, v182, v125\n"
    "v_exp_f32_e32 v185, v185\n"
    "v_mfma_f32_16x16x32_bf16 v[196:199], v[208:211], v[148:151], v[196:199]\n"
    "v_add_f32_e32 v125, v183, v125\n"
    "v_cvt_pk_bf16_f32 v21, v182, v183\n"
    "ds_read_b128 v[208:211], v250 offset:28672\n"
    "s_waitcnt lgkmcnt(1)\n"
    "v_mfma_f32_16x16x32_bf16 v[192:195], v[212:215], v[152:155], v[192:195]\n"
    "v_exp_f32_e32 v186, v186\n"
    "v_add_f32_e32 v124, v184, v124\n"
    "v_exp_f32_e32 v187, v187\n"
    "v_mfma_f32_16x16x32_bf16 v[196:199], v[212:215], v[156:159], v[196:199]\n"
    "v_add_f32_e32 v124, v185, v124\n"
    "v_cvt_pk_bf16_f32 v18, v184, v185\n"
    "ds_read_b128 v[212:215], v251 offset:28672\n"
    "v_mfma_f32_16x16x32_bf16 v[200:203], v[216:219], v[160:163], v[112:115]\n"
    "v_exp_f32_e32 v188, v188\n"
    "v_add_f32_e32 v124, v186, v124\n"
    "v_mfma_f32_16x16x32_bf16 v[204:207], v[216:219], v[164:167], v[116:119]\n"
    "v_exp_f32_e32 v189, v189\n"
    "v_add_f32_e32 v124, v187, v124\n"
    "ds_read_b128 v[216:219], v250 offset:32768\n"
    "v_mfma_f32_16x16x32_bf16 v[200:203], v[220:223], v[168:171], v[200:203]\n"
    "s_setprio 0\n"
    "v_cvt_pk_bf16_f32 v19, v186, v187\n"
    "v_exp_f32_e32 v190, v190\n"
    "v_mfma_f32_16x16x32_bf16 v[204:207], v[220:223], v[172:175], v[204:207]\n"
    "v_add_f32_e32 v125, v188, v125\n"
    "v_exp_f32_e32 v191, v191\n"
    "ds_read_b128 v[220:223], v251 offset:32768\n"
    "s_waitcnt lgkmcnt(1)\n"
    "v_mfma_f32_16x16x32_bf16 v[200:203], v[208:211], v[128:131], v[200:203]\n"
    "v_add_f32_e32 v125, v189, v125\n"
    "v_cvt_pk_bf16_f32 v22, v188, v189\n"
    "v_add_f32_e32 v125, v190, v125\n"
    "v_add_f32_e32 v125, v191, v125\n"
    "v_cvt_pk_bf16_f32 v23, v190, v191\n"
    "v_mfma_f32_16x16x32_bf16 v[204:207], v[208:211], v[132:135], v[204:207]\n"
    "v_add_f32_e32 v176, v124, v125\n"
    "v_cmp_nge_f32_e32 vcc, s38, v176\n"
    "s_add_u32 m0, s57, 0x18000\n"
    "s_nop 0\n"
    "buffer_load_dwordx4 v127, s[48:51], s62 offen nt lds\n"
    "v_mfma_f32_16x16x32_bf16 v[200:203], v[212:215], v[136:139], v[200:203]\n"
    "s_cbranch_vccnz .Lrare_a2\n"
    ".Lback_a2:\n"
    "v_add_f32_e32 v252, v252, v124\n"
    "v_add_f32_e32 v253, v253, v125\n"
    "v_exp_f32_e32 v240, v240\n"
    "v_mfma_f32_16x16x32_bf16 v[204:207], v[212:215], v[140:143], v[204:207]\n"
    "v_exp_f32_e32 v241, v241\n"
    "s_waitcnt vmcnt(4) lgkmcnt(0)\n"
    "s_barrier\n"
    "v_mfma_f32_16x16x32_bf16 v[200:203], v[216:219], v[144:147], v[200:203]\n"
    "v_exp_f32_e32 v242, v242\n"
    "v_exp_f32_e32 v243, v243\n"
    "s_setprio 3\n"
    "ds_read_b128 v[208:211], v244 offset:49152\n"
    "ds_read_b128 v[212:215], v245 offset:49152\n"
    "v_mfma_f32_16x16x32_bf16 v[204:207], v[216:219], v[148:151], v[204:207]\n"
    "v_cvt_pk_bf16_f32 v226, v240, v241\n"
    "ds_read_b128 v[216:219], v244 offset:53248\n"
    "v_mfma_f32_16x16x32_bf16 v[200:203], v[220:223], v[152:155], v[200:203]\n"
    "v_add_f32_e32 v240, v240, v241\n"
    "v_cvt_pk_bf16_f32 v227, v242, v243\n"
    "v_add_f32_e32 v242, v242, v243\n"
    "v_add_f32_e32 v240, v240, v242\n"
    "v_mfma_f32_16x16x32_bf16 v[204:207], v[220:223], v[156:159], v[204:207]\n"
    "v_cmp_nge_f32_e32 vcc, s38, v240\n"
    "s_cbranch_vccnz .Lrare_n1\n"
    ".Lback_n1:\n"
    "v_add_f32_e32 v255, v255, v240\n"
    "ds_read_b128 v[220:223], v245 offset:53248\n"
    "s_waitcnt lgkmcnt(1)\n"
    "v_mfma_f32_16x16x32_bf16 v[176:179], v[208:211], v[160:163], v[112:115]\n"
    "s_add_u32 m0, s57, 0x6000\n"
    "s_add_u32 s61, s58, 0x18000\n"
    "buffer_load_dwordx4 v126, s[44:47], s61 offen nt lds\n"
    "v_exp_f32_e32 v192, v192\n"
    "v_mfma_f32_16x16x32_bf16 v[180:183], v[208:211], v[164:167], v[116:119]\n"
    "v_exp_f32_e32 v193, v193\n"
    "v_exp_f32_e32 v194, v194\n"
    "v_mfma_f32_16x16x32_bf16 v[240:243], v[208:211], v[88:91], v[120:123]\n"
    "v_exp_f32_e32 v195, v195\n"
    "ds_read_b128 v[208:211], v244 offset:57344\n"
    "v_mfma_f32_16x16x32_bf16 v[176:179], v[212:215], v[168:171], v[176:179]\n"
    "v_add_f32_e32 v124, v192, v193\n"
    "v_cvt_pk_bf16_f32 v24, v192, v193\n"
    "v_exp_f32_e32 v196, v196\n"
    "v_mfma_f32_16x16x32_bf16 v[180:183], v[212:215], v[172:175], v[180:183]\n"
    "v_add_f32_e32 v124, v194, v124\n"
    "v_mfma_f32_16x16x32_bf16 v[240:243], v[212:215], v[96:99], v[240:243]\n"
    "v_exp_f32_e32 v197, v197\n"
    "ds_read_b128 v[212:215], v245 offset:57344\n"
    "v_mfma_f32_16x16x32_bf16 v[176:179], v[216:219], v[128:131], v[176:179]\n"
    "v_add_f32_e32 v124, v195, v124\n"
    "v_cvt_pk_bf16_f32 v25, v194, v195\n"
    "v_exp_f32_e32 v198, v198\n"
    "v_mfma_f32_16x16x32_bf16 v[180:183], v[216:219], v[132:135], v[180:183]\n"
    "v_exp_f32_e32 v199, v199\n"
    "v_mfma_f32_16x16x32_bf16 v[240:243], v[216:219], v[92:95], v[240:243]\n"
    "v_add_f32_e32 v125, v196, v197\n"
    "ds_read_b128 v[216:219], v246 offset:49152\n"
    "s_waitcnt lgkmcnt(1)\n"
    "v_mfma_f32_16x16x32_bf16 v[176:179], v[220:223], v[136:139], v[176:179]\n"
    "v_cvt_pk_bf16_f32 v28, v196, v197\n"
    "v_exp_f32_e32 v200, v200\n"
    "v_mfma_f32_16x16x32_bf16 v[180:183], v[220:223], v[140:143], v[180:183]\n"
    "v_add_f32_e32 v125, v198, v125\n"
    "v_exp_f32_e32 v201, v201\n"
    "v_mfma_f32_16x16x32_bf16 v[240:243], v[220:223], v[100:103], v[240:243]\n"
    "v_add_f32_e32 v125, v199, v125\n"
    "v_cvt_pk_bf16_f32 v29, v198, v199\n"
    "ds_read_b128 v[220:223], v247 offset:49152\n"
    "v_mfma_f32_16x16x32_bf16 v[176:179], v[208:211], v[144:147], v[176:179]\n"
    "v_exp_f32_e32 v202, v202\n"
    "v_add_f32_e32 v124, v200, v124\n"
    "v_mfma_f32_16x16x32_bf16 v[180:183], v[208:211], v[148:151], v[180:183]\n"
    "v_exp_f32_e32 v203, v203\n"
    "v_mfma_f32_16x16x32_bf16 v[240:243], v[208:211], v[104:107], v[240:243]\n"
    "v_add_f32_e32 v124, v201, v124\n"
    "v_cvt_pk_bf16_f32 v26, v200, v201\n"
    "ds_read_b128 v[208:211], v246 offset:53248\n"
    "v_mfma_f32_16x16x32_bf16 v[176:179], v[212:215], v[152:155], v[176:179]\n"
    "v_exp_f32_e32 v204, v204\n"
    "v_add_f32_e32 v124, v202, v124\n"
    "v_mfma_f32_16x16x32_bf16 v[180:183], v[212:215], v[156:159], v[180:183]\n"
    "v_exp_f32_e32 v205, v205\n"
    "v_mfma_f32_16x16x32_bf16 v[240:243], v[212:215], v[108:111], v[240:243]\n"
    "v_add_f32_e32 v124, v203, v124\n"
    "v_cvt_pk_bf16_f32 v27, v202, v203\n"
    "ds_read_b128 v[212:215], v247 offset:53248\n"
    "s_waitcnt lgkmcnt(1)\n"
    "v_mfma_f32_16x16x32_bf16 v[184:187], v[216:219], v[160:163], v[112:115]\n"
    "v_exp_f32_e32 v206, v206\n"
    "v_add_f32_e32 v125, v204, v125\n"
    "v_mfma_f32_16x16x32_bf16 v[188:191], v[216:219], v[164:167], v[116:119]\n"
    "v_exp_f32_e32 v207, v207\n"
    "ds_read_b128 v[216:219], v246 offset:57344\n"
    "v_mfma_f32_16x16x32_bf16 v[184:187], v[220:223], v[168:171], v[184:187]\n"
    "v_add_f32_e32 v125, v205, v125\n"
    "v_cvt_pk_bf16_f32 v30, v204, v205\n"
    "v_add_f32_e32 v125, v206, v125\n"
    "v_mfma_f32_16x16x32_bf16 v[188:191], v[220:223], v[172:175], v[188:191]\n"
    "v_add_f32_e32 v125, v207, v125\n"
    "v_cvt_pk_bf16_f32 v31, v206, v207\n"
    "ds_read_b128 v[220:223], v247 offset:57344\n"
    "v_mfma_f32_16x16x32_bf16 v[184:187], v[208:211], v[128:131], v[184:187]\n"
    "v_add_f32_e32 v192, v124, v125\n"
    "v_cmp_nge_f32_e32 vcc, s38, v192\n"
    "s_cbranch_vccnz .Lrare_a3\n"
    ".Lback_a3:\n"
    "v_mfma_f32_16x16x32_bf16 v[188:191], v[208:211], v[132:135], v[188:191]\n"
    "v_add_f32_e32 v252, v252, v124\n"
    "v_add_f32_e32 v253, v253, v125\n"
    "ds_read_b128 v[208:211], v248 offset:49152\n"
    "s_waitcnt lgkmcnt(1)\n"
    "v_mfma_f32_16x16x32_bf16 v[184:187], v[212:215], v[136:139], v[184:187]\n"
    "s_setprio 2\n"
    "v_exp_f32_e32 v240, v240\n"
    "v_exp_f32_e32 v241, v241\n"
    "v_mfma_f32_16x16x32_bf16 v[188:191], v[212:215], v[140:143], v[188:191]\n"
    "v_exp_f32_e32 v242, v242\n"
    "ds_read_b128 v[212:215], v249 offset:49152\n"
    "v_mfma_f32_16x16x32_bf16 v[184:187], v[216:219], v[144:147], v[184:187]\n"
    "v_exp_f32_e32 v243, v243\n"
    "v_cvt_pk_bf16_f32 v228, v240, v241\n"
    "v_mfma_f32_16x16x32_bf16 v[188:191], v[216:219], v[148:151], v[188:191]\n"
    "v_add_f32_e32 v240, v240, v241\n"
    "v_cvt_pk_bf16_f32 v229, v242, v243\n"
    "ds_read_b128 v[216:219], v248 offset:53248\n"
    "v_mfma_f32_16x16x32_bf16 v[184:187], v[220:223], v[152:155], v[184:187]\n"
    "s_add_u32 m0, s57, 0x8000\n"
    "s_add_u32 s61, s59, 0x18000\n"
    "buffer_load_dwordx4 v126, s[44:47], s61 offen nt lds\n"
    "v_add_f32_e32 v242, v242, v243\n"
    "v_add_f32_e32 v240, v240, v242\n"
    "v_mfma_f32_16x16x32_bf16 v[188:191], v[220:223], v[156:159], v[188:191]\n"
    "v_cmp_nge_f32_e32 vcc, s38, v240\n"
    "s_cbranch_vccnz .Lrare_n2\n"
    ".Lback_n2:\n"
    "v_add_f32_e32 v255, v255, v240\n"
    "ds_read_b128 v[220:223], v249 offset:53248\n"
    "s_waitcnt lgkmcnt(1)\n"
    "v_mfma_f32_16x16x32_bf16 v[192:195], v[208:211], v[160:163], v[112:115]\n"
    "v_exp_f32_e32 v176, v176\n"
    "v_mfma_f32_16x16x32_bf16 v[196:199], v[208:211], v[164:167], v[116:119]\n"
    "v_exp_f32_e32 v177, v177\n"
    "ds_read_b128 v[208:211], v248 offset:57344\n"
    "v_mfma_f32_16x16x32_bf16 v[192:195], v[212:215], v[168:171], v[192:195]\n"
    "v_exp_f32_e32 v178, v178\n"
    "v_exp_f32_e32 v179, v179\n"
    "v_mfma_f32_16x16x32_bf16 v[196:199], v[212:215], v[172:175], v[196:199]\n"
    "v_add_f32_e32 v124, v176, v177\n"
    "v_cvt_pk_bf16_f32 v32, v176, v177\n"
    "ds_read_b128 v[212:215], v249 offset:57344\n"
    "v_mfma_f32_16x16x32_bf16 v[192:195], v[216:219], v[128:131], v[192:195]\n"
    "v_exp_f32_e32 v180, v180\n"
    "v_add_f32_e32 v124, v178, v124\n"
    "v_mfma_f32_16x16x32_bf16 v[196:199], v[216:219], v[132:135], v[196:199]\n"
    "v_exp_f32_e32 v181, v181\n"
    "ds_read_b128 v[216:219], v250 offset:49152\n"
    "s_waitcnt lgkmcnt(1)\n"
    "v_mfma_f32_16x16x32_bf16 v[192:195], v[220:223], v[136:139], v[192:195]\n"
    "s_setprio 1\n"
    "v_add_f32_e32 v124, v179, v124\n"
    "v_cvt_pk_bf16_f32 v33, v178, v179\n"
    "v_mfma_f32_16x16x32_bf16 v[196:199], v[220:223], v[140:143], v[196:199]\n"
    "v_exp_f32_e32 v182, v182\n"
    "ds_read_b128 v[220:223], v251 offset:49152\n"
    "v_mfma_f32_16x16x32_bf16 v[192:195], v[208:211], v[144:147], v[192:195]\n"
    "s_add_u32 m0, s57, 0xa000\n"
    "s_add_u32 s61, s60, 0x18000\n"
    "buffer_load_dwordx4 v126, s[44:47], s61 offen nt lds\n"
    "v_exp_f32_e32 v183, v183\n"
    "v_add_f32_e32 v125, v180, v181\n"
    "v_mfma_f32_16x16x32_bf16 v[196:199], v[208:211], v[148:151], v[196:199]\n"
    "v_cvt_pk_bf16_f32 v36, v180, v181\n"
    "v_exp_f32_e32 v184, v184\n"
    "ds_read_b128 v[208:211], v250 offset:53248\n"
    "v_mfma_f32_16x16x32_bf16 v[192:195], v[212:215], v[152:155], v[192:195]\n"
    "v_add_f32_e32 v125, v182, v125\n"
    "v_exp_f32_e32 v185, v185\n"
    "v_mfma_f32_16x16x32_bf16 v[196:199], v[212:215], v[156:159], v[196:199]\n"
    "v_add_f32_e32 v125, v183, v125\n"
    "ds_read_b128 v[212:215], v251 offset:53248\n"
    "s_waitcnt lgkmcnt(1)\n"
    "v_mfma_f32_16x16x32_bf16 v[200:203], v[216:219], v[160:163], v[112:115]\n"
    "v_cvt_pk_bf16_f32 v37, v182, v183\n"
    "v_exp_f32_e32 v186, v186\n"
    "v_mfma_f32_16x16x32_bf16 v[204:207], v[216:219], v[164:167], v[116:119]\n"
    "v_add_f32_e32 v124, v184, v124\n"
    "v_exp_f32_e32 v187, v187\n"
    "ds_read_b128 v[216:219], v250 offset:57344\n"
    "v_mfma_f32_16x16x32_bf16 v[200:203], v[220:223], v[168:171], v[200:203]\n"
    "s_setprio 0\n"
    "v_add_f32_e32 v124, v185, v124\n"
    "v_cvt_pk_bf16_f32 v34, v184, v185\n"
    "v_mfma_f32_16x16x32_bf16 v[204:207], v[220:223], v[172:175], v[204:207]\n"
    "v_exp_f32_e32 v188, v188\n"
    "ds_read_b128 v[220:223], v251 offset:57344\n"
    "v_mfma_f32_16x16x32_bf16 v[200:203], v[208:211], v[128:131], v[200:203]\n"
    "v_add_f32_e32 v124, v186, v124\n"
    "v_exp_f32_e32 v189, v189\n"
    "v_add_f32_e32 v124, v187, v124\n"
    "v_mfma_f32_16x16x32_bf16 v[204:207], v[208:211], v[132:135], v[204:207]\n"
    "v_cvt_pk_bf16_f32 v35, v186, v187\n"
    "s_add_u32 m0, s57, 0x1a000\n"
    "s_add_u32 s61, s62, 0x6000\n"
    "buffer_load_dwordx4 v127, s[48:51], s61 offen nt lds\n"
    "s_waitcnt lgkmcnt(0)\n"
    "v_mfma_f32_16x16x32_bf16 v[200:203], v[212:215], v[136:139], v[200:203]\n"
    "v_exp_f32_e32 v190, v190\n"
    "v_add_f32_e32 v125, v188, v125\n"
    "v_mfma_f32_16x16x32_bf16 v[204:207], v[212:215], v[140:143], v[204:207]\n"
    "v_exp_f32_e32 v191, v191\n"
    "s_waitcnt vmcnt(5) lgkmcnt(0)\n"
    "s_barrier\n"
    "v_mfma_f32_16x16x32_bf16 v[200:203], v[216:219], v[144:147], v[200:203]\n"
    "v_add_f32_e32 v125, v189, v125\n"
    "v_cvt_pk_bf16_f32 v38, v188, v189\n"
    "v_add_f32_e32 v125, v190, v125\n"
    "s_setprio 3\n"
    "ds_read_b128 v[208:211], v244\n"
    "ds_read_b128 v[212:215], v245\n"
    "v_mfma_f32_16x16x32_bf16 v[204:207], v[216:219], v[148:151], v[204:207]\n"
    "v_add_f32_e32 v125, v191, v125\n"
    "v_cvt_pk_bf16_f32 v39, v190, v191\n"
    "ds_read_b128 v[216:219], v244 offset:4096\n"
    "v_mfma_f32_16x16x32_bf16 v[200:203], v[220:223], v[152:155], v[200:203]\n"
    "v_add_f32_e32 v176, v124, v125\n"
    "v_cmp_nge_f32_e32 vcc, s38, v176\n"
    "s_cbranch_vccnz .Lrare_a4\n"
    ".Lback_a4:\n"
    "v_mfma_f32_16x16x32_bf16 v[204:207], v[220:223], v[156:159], v[204:207]\n"
    "v_add_f32_e32 v252, v252, v124\n"
    "v_add_f32_e32 v253, v253, v125\n"
    "ds_read_b128 v[220:223], v245 offset:4096\n"
    "s_waitcnt lgkmcnt(1)\n"
    "v_mfma_f32_16x16x32_bf16 v[176:179], v[208:211], v[160:163], v[112:115]\n"
    "s_add_u32 m0, s57, 0xc000\n"
    "s_add_u32 s61, s58, 0x1e000\n"
    "buffer_load_dwordx4 v126, s[44:47], s61 offen nt lds\n"
    "v_exp_f32_e32 v192, v192\n"
    "v_mfma_f32_16x16x32_bf16 v[180:183], v[208:211], v[164:167], v[116:119]\n"
    "v_exp_f32_e32 v193, v193\n"
    "v_exp_f32_e32 v194, v194\n"
    "v_mfma_f32_16x16x32_bf16 v[240:243], v[208:211], v[88:91], v[120:123]\n"
    "v_exp_f32_e32 v195, v195\n"
    "ds_read_b128 v[208:211], v244 offset:8192\n"
    "v_mfma_f32_16x16x32_bf16 v[176:179], v[212:215], v[168:171], v[176:179]\n"
    "v_add_f32_e32 v124, v192, v193\n"
    "v_cvt_pk_bf16_f32 v40, v192, v193\n"
    "v_exp_f32_e32 v196, v196\n"
    "v_mfma_f32_16x16x32_bf16 v[180:183], v[212:215], v[172:175], v[180:183]\n"
    "v_add_f32_e32 v124, v194, v124\n"
    "v_mfma_f32_16x16x32_bf16 v[240:243], v[212:215], v[96:99], v[240:243]\n"
    "v_exp_f32_e32 v197, v197\n"
    "ds_read_b128 v[212:215], v245 offset:8192\n"
    "v_mfma_f32_16x16x32_bf16 v[176:179], v[216:219], v[128:131], v[176:179]\n"
    "v_add_f32_e32 v124, v195, v124\n"
    "v_cvt_pk_bf16_f32 v41, v194, v195\n"
    "v_exp_f32_e32 v198, v198\n"
    "v_mfma_f32_16x16x32_bf16 v[180:183], v[216:219], v[132:135], v[180:183]\n"
    "v_exp_f32_e32 v199, v199\n"
    "v_mfma_f32_16x16x32_bf16 v[240:243], v[216:219], v[92:95], v[240:243]\n"
    "v_add_f32_e32 v125, v196, v197\n"
    "ds_read_b128 v[216:219], v246\n"
    "s_waitcnt lgkmcnt(1)\n"
    "v_mfma_f32_16x16x32_bf16 v[176:179], v[220:223], v[136:139], v[176:179]\n"
    "v_cvt_pk_bf16_f32 v44, v196, v197\n"
    "v_exp_f32_e32 v200, v200\n"
    "v_mfma_f32_16x16x32_bf16 v[180:183], v[220:223], v[140:143], v[180:183]\n"
    "v_add_f32_e32 v125, v198, v125\n"
    "v_exp_f32_e32 v201, v201\n"
    "v_mfma_f32_16x16x32_bf16 v[240:243], v[220:223], v[100:103], v[240:243]\n"
    "v_add_f32_e32 v125, v199, v125\n"
    "v_cvt_pk_bf16_f32 v45, v198, v199\n"
    "ds_read_b128 v[220:223], v247\n"
    "v_mfma_f32_16x16x32_bf16 v[176:179], v[208:211], v[144:147], v[176:179]\n"
    "v_exp_f32_e32 v202, v202\n"
    "v_add_f32_e32 v124, v200, v124\n"
    "v_mfma_f32_16x16x32_bf16 v[180:183], v[208:211], v[148:151], v[180:183]\n"
    "v_exp_f32_e32 v203, v203\n"
    "v_mfma_f32_16x16x32_bf16 v[240:243], v[208:211], v[104:107], v[240:243]\n"
    "v_add_f32_e32 v124, v201, v124\n"
    "v_cvt_pk_bf16_f32 v42, v200, v201\n"
    "ds_read_b128 v[208:211], v246 offset:4096\n"
    "v_mfma_f32_16x16x32_bf16 v[176:179], v[212:215], v[152:155], v[176:179]\n"
    "v_exp_f32_e32 v204, v204\n"
    "v_add_f32_e32 v124, v202, v124\n"
    "v_mfma_f32_16x16x32_bf16 v[180:183], v[212:215], v[156:159], v[180:183]\n"
    "v_exp_f32_e32 v205, v205\n"
    "v_mfma_f32_16x16x32_bf16 v[240:243], v[212:215], v[108:111], v[240:243]\n"
    "v_add_f32_e32 v124, v203, v124\n"
    "v_cvt_pk_bf16_f32 v43, v202, v203\n"
    "ds_read_b128 v[212:215], v247 offset:4096\n"
    "s_waitcnt lgkmcnt(1)\n"
    "v_mfma_f32_16x16x32_bf16 v[184:187], v[216:219], v[160:163], v[112:115]\n"
    "v_exp_f32_e32 v206, v206\n"
    "v_add_f32_e32 v125, v204, v125\n"
    "v_mfma_f32_16x16x32_bf16 v[188:191], v[216:219], v[164:167], v[116:119]\n"
    "v_exp_f32_e32 v207, v207\n"
    "ds_read_b128 v[216:219], v246 offset:8192\n"
    "v_mfma_f32_16x16x32_bf16 v[184:187], v[220:223], v[168:171], v[184:187]\n"
    "v_add_f32_e32 v125, v205, v125\n"
    "v_cvt_pk_bf16_f32 v46, v204, v205\n"
    "v_add_f32_e32 v125, v206, v125\n"
    "v_mfma_f32_16x16x32_bf16 v[188:191], v[220:223], v[172:175], v[188:191]\n"
    "v_add_f32_e32 v125, v207, v125\n"
    "v_cvt_pk_bf16_f32 v47, v206, v207\n"
    "ds_read_b128 v[220:223], v247 offset:8192\n"
    "v_mfma_f32_16x16x32_bf16 v[184:187], v[208:211], v[128:131], v[184:187]\n"
    "v_add_f32_e32 v192, v124, v125\n"
    "v_cmp_nge_f32_e32 vcc, s38, v192\n"
    "s_cbranch_vccnz .Lrare_a5\n"
    ".Lback_a5:\n"
    "v_mfma_f32_16x16x32_bf16 v[188:191], v[208:211], v[132:135], v[188:191]\n"
    "v_add_f32_e32 v252, v252, v124\n"
    "v_add_f32_e32 v253, v253, v125\n"
    "ds_read_b128 v[208:211], v248\n"
    "s_waitcnt lgkmcnt(1)\n"
    "v_mfma_f32_16x16x32_bf16 v[184:187], v[212:215], v[136:139], v[184:187]\n"
    "s_setprio 2\n"
    "v_exp_f32_e32 v240, v240\n"
    "v_exp_f32_e32 v241, v241\n"
    "v_mfma_f32_16x16x32_bf16 v[188:191], v[212:215], v[140:143], v[188:191]\n"
    "v_exp_f32_e32 v242, v242\n"
    "ds_read_b128 v[212:215], v249\n"
    "v_mfma_f32_16x16x32_bf16 v[184:187], v[216:219], v[144:147], v[184:187]\n"
    "v_exp_f32_e32 v243, v243\n"
    "v_cvt_pk_bf16_f32 v230, v240, v241\n"
    "v_mfma_f32_16x16x32_bf16 v[188:191], v[216:219], v[148:151], v[188:191]\n"
    "v_add_f32_e32 v240, v240, v241\n"
    "v_cvt_pk_bf16_f32 v231, v242, v243\n"
    "ds_read_b128 v[216:219], v248 offset:4096\n"
    "v_mfma_f32_16x16x32_bf16 v[184:187], v[220:223], v[152:155], v[184:187]\n"
    "s_add_u32 m0, s57, 0xe000\n"
    "s_add_u32 s61, s59, 0x1e000\n"
    "buffer_load_dwordx4 v126, s[44:47], s61 offen nt lds\n"
    "v_add_f32_e32 v242, v242, v243\n"
    "v_add_f32_e32 v240, v240, v242\n"
    "v_mfma_f32_16x16x32_bf16 v[188:191], v[220:223], v[156:159], v[188:191]\n"
    "v_cmp_nge_f32_e32 vcc, s38, v240\n"
    "s_cbranch_vccnz .Lrare_n3\n"
    ".Lback_n3:\n"
    "v_add_f32_e32 v255, v255, v240\n"
    "ds_read_b128 v[220:223], v249 offset:4096\n"
    "s_waitcnt lgkmcnt(1)\n"
    "v_mfma_f32_16x16x32_bf16 v[192:195], v[208:211], v[160:163], v[112:115]\n"
    "v_exp_f32_e32 v176, v176\n"
    "v_mfma_f32_16x16x32_bf16 v[196:199], v[208:211], v[164:167], v[116:119]\n"
    "v_exp_f32_e32 v177, v177\n"
    "ds_read_b128 v[208:211], v248 offset:8192\n"
    "v_mfma_f32_16x16x32_bf16 v[192:195], v[212:215], v[168:171], v[192:195]\n"
    "v_exp_f32_e32 v178, v178\n"
    "v_exp_f32_e32 v179, v179\n"
    "v_mfma_f32_16x16x32_bf16 v[196:199], v[212:215], v[172:175], v[196:199]\n"
    "v_add_f32_e32 v124, v176, v177\n"
    "v_cvt_pk_bf16_f32 v48, v176, v177\n"
    "ds_read_b128 v[212:215], v249 offset:8192\n"
    "v_mfma_f32_16x16x32_bf16 v[192:195], v[216:219], v[128:131], v[192:195]\n"
    "v_exp_f32_e32 v180, v180\n"
    "v_add_f32_e32 v124, v178, v124\n"
    "v_mfma_f32_16x16x32_bf16 v[196:199], v[216:219], v[132:135], v[196:199]\n"
    "v_exp_f32_e32 v181, v181\n"
    "ds_read_b128 v[216:219], v250\n"
    "s_waitcnt lgkmcnt(1)\n"
    "v_mfma_f32_16x16x32_bf16 v[192:195], v[220:223], v[136:139], v[192:195]\n"
    "s_setprio 1\n"
    "v_add_f32_e32 v124, v179, v124\n"
    "v_cvt_pk_bf16_f32 v49, v178, v179\n"
    "v_mfma_f32_16x16x32_bf16 v[196:199], v[220:223], v[140:143], v[196:199]\n"
    "v_exp_f32_e32 v182, v182\n"
    "ds_read_b128 v[220:223], v251\n"
    "v_mfma_f32_16x16x32_bf16 v[192:195], v[208:211], v[144:147], v[192:195]\n"
    "s_add_u32 m0, s57, 0x10000\n"
    "s_add_u32 s61, s60, 0x1e000\n"
    "buffer_load_dwordx4 v126, s[44:47], s61 offen nt lds\n"
    "v_exp_f32_e32 v183, v183\n"
    "v_add_f32_e32 v125, v180, v181\n"
    "v_mfma_f32_16x16x32_bf16 v[196:199], v[208:211], v[148:151], v[196:199]\n"
    "v_cvt_pk_bf16_f32 v52, v180, v181\n"
    "v_exp_f32_e32 v184, v184\n"
    "ds_read_b128 v[208:211], v250 offset:4096\n"
    "v_mfma_f32_16x16x32_bf16 v[192:195], v[212:215], v[152:155], v[192:195]\n"
    "v_add_f32_e32 v125, v182, v125\n"
    "v_exp_f32_e32 v185, v185\n"
    "v_mfma_f32_16x16x32_bf16 v[196:199], v[212:215], v[156:159], v[196:199]\n"
    "v_add_f32_e32 v125, v183, v125\n"
    "ds_read_b128 v[212:215], v251 offset:4096\n"
    "s_waitcnt lgkmcnt(1)\n"
    "v_mfma_f32_16x16x32_bf16 v[200:203], v[216:219], v[160:163], v[112:115]\n"
    "v_cvt_pk_bf16_f32 v53, v182, v183\n"
    "v_exp_f32_e32 v186, v186\n"
    "v_mfma_f32_16x16x32_bf16 v[204:207], v[216:219], v[164:167], v[116:119]\n"
    "v_add_f32_e32 v124, v184, v124\n"
    "v_exp_f32_e32 v187, v187\n"
    "ds_read_b128 v[216:219], v250 offset:8192\n"
    "v_mfma_f32_16x16x32_bf16 v[200:203], v[220:223], v[168:171], v[200:203]\n"
    "s_setprio 0\n"
    "v_add_f32_e32 v124, v185, v124\n"
    "v_cvt_pk_bf16_f32 v50, v184, v185\n"
    "v_mfma_f32_16x16x32_bf16 v[204:207], v[220:223], v[172:175], v[204:207]\n"
    "v_exp_f32_e32 v188, v188\n"
    "ds_read_b128 v[220:223], v251 offset:8192\n"
    "v_mfma_f32_16x16x32_bf16 v[200:203], v[208:211], v[128:131], v[200:203]\n"
    "v_add_f32_e32 v124, v186, v124\n"
    "v_exp_f32_e32 v189, v189\n"
    "v_add_f32_e32 v124, v187, v124\n"
    "v_mfma_f32_16x16x32_bf16 v[204:207], v[208:211], v[132:135], v[204:207]\n"
    "v_cvt_pk_bf16_f32 v51, v186, v187\n"
    "s_add_u32 m0, s57, 0x1c000\n"
    "s_add_u32 s61, s62, 0xc000\n"
    "buffer_load_dwordx4 v127, s[48:51], s61 offen nt lds\n"
    "s_waitcnt lgkmcnt(0)\n"
    "v_mfma_f32_16x16x32_bf16 v[200:203], v[212:215], v[136:139], v[200:203]\n"
    "v_exp_f32_e32 v190, v190\n"
    "v_add_f32_e32 v125, v188, v125\n"
    "v_mfma_f32_16x16x32_bf16 v[204:207], v[212:215], v[140:143], v[204:207]\n"
    "v_exp_f32_e32 v191, v191\n"
    "s_waitcnt vmcnt(5) lgkmcnt(0)\n"
    "s_barrier\n"
    "v_mfma_f32_16x16x32_bf16 v[200:203], v[216:219], v[144:147], v[200:203]\n"
    "v_add_f32_e32 v125, v189, v125\n"
    "v_cvt_pk_bf16_f32 v54, v188, v189\n"
    "v_add_f32_e32 v125, v190, v125\n"
    "s_setprio 3\n"
    "ds_read_b128 v[208:211], v244 offset:24576\n"
    "ds_read_b128 v[212:215], v245 offset:24576\n"
    "v_mfma_f32_16x16x32_bf16 v[204:207], v[216:219], v[148:151], v[204:207]\n"
    "v_add_f32_e32 v125, v191, v125\n"
    "v_cvt_pk_bf16_f32 v55, v190, v191\n"
    "ds_read_b128 v[216:219], v244 offset:28672\n"
    "v_mfma_f32_16x16x32_bf16 v[200:203], v[220:223], v[152:155], v[200:203]\n"
    "v_add_f32_e32 v176, v124, v125\n"
    "v_cmp_nge_f32_e32 vcc, s38, v176\n"
    "s_cbranch_vccnz .Lrare_a6\n"
    ".Lback_a6:\n"
    "v_mfma_f32_16x16x32_bf16 v[204:207], v[220:223], v[156:159], v[204:207]\n"
    "v_add_f32_e32 v252, v252, v124\n"
    "v_add_f32_e32 v253, v253, v125\n"
    "ds_read_b128 v[220:223], v245 offset:28672\n"
    "s_waitcnt lgkmcnt(1)\n"
    "v_mfma_f32_16x16x32_bf16 v[176:179], v[208:211], v[160:163], v[112:115]\n"
    "s_add_u32 m0, s57, 0x0\n"
    "s_add_u32 s61, s58, 0x24000\n"
    "buffer_load_dwordx4 v126, s[44:47], s61 offen nt lds\n"
    "v_exp_f32_e32 v192, v192\n"
    "v_mfma_f32_16x16x32_bf16 v[180:183], v[208:211], v[164:167], v[116:119]\n"
    "v_exp_f32_e32 v193, v193\n"
    "v_exp_f32_e32 v194, v194\n"
    "v_mfma_f32_16x16x32_bf16 v[240:243], v[208:211], v[88:91], v[120:123]\n"
    "v_exp_f32_e32 v195, v195\n"
    "ds_read_b128 v[208:211], v244 offset:32768\n"
    "v_mfma_f32_16x16x32_bf16 v[176:179], v[212:215], v[168:171], v[176:179]\n"
    "v_add_f32_e32 v124, v192, v193\n"
    "v_cvt_pk_bf16_f32 v56, v192, v193\n"
    "v_exp_f32_e32 v196, v196\n"
    "v_mfma_f32_16x16x32_bf16 v[180:183], v[212:215], v[172:175], v[180:183]\n"
    "v_add_f32_e32 v124, v194, v124\n"
    "v_mfma_f32_16x16x32_bf16 v[240:243], v[212:215], v[96:99], v[240:243]\n"
    "v_exp_f32_e32 v197, v197\n"
    "ds_read_b128 v[212:215], v245 offset:32768\n"
    "v_mfma_f32_16x16x32_bf16 v[176:179], v[216:219], v[128:131], v[176:179]\n"
    "v_add_f32_e32 v124, v195, v124\n"
    "v_cvt_pk_bf16_f32 v57, v194, v195\n"
    "v_exp_f32_e32 v198, v198\n"
    "v_mfma_f32_16x16x32_bf16 v[180:183], v[216:219], v[132:135], v[180:183]\n"
    "v_exp_f32_e32 v199, v199\n"
    "v_mfma_f32_16x16x32_bf16 v[240:243], v[216:219], v[92:95], v[240:243]\n"
    "v_add_f32_e32 v125, v196, v197\n"
    "ds_read_b128 v[216:219], v246 offset:24576\n"
    "s_waitcnt lgkmcnt(1)\n"
    "v_mfma_f32_16x16x32_bf16 v[176:179], v[220:223], v[136:139], v[176:179]\n"
    "v_cvt_pk_bf16_f32 v60, v196, v197\n"
    "v_exp_f32_e32 v200, v200\n"
    "v_mfma_f32_16x16x32_bf16 v[180:183], v[220:223], v[140:143], v[180:183]\n"
    "v_add_f32_e32 v125, v198, v125\n"
    "v_exp_f32_e32 v201, v201\n"
    "v_mfma_f32_16x16x32_bf16 v[240:243], v[220:223], v[100:103], v[240:243]\n"
    "v_add_f32_e32 v125, v199, v125\n"
    "v_cvt_pk_bf16_f32 v61, v198, v199\n"
    "ds_read_b128 v[220:223], v247 offset:24576\n"
    "v_mfma_f32_16x16x32_bf16 v[176:179], v[208:211], v[144:147], v[176:179]\n"
    "v_exp_f32_e32 v202, v202\n"
    "v_add_f32_e32 v124, v200, v124\n"
    "v_mfma_f32_16x16x32_bf16 v[180:183], v[208:211], v[148:151], v[180:183]\n"
    "v_exp_f32_e32 v203, v203\n"
    "v_mfma_f32_16x16x32_bf16 v[240:243], v[208:211], v[104:107], v[240:243]\n"
    "v_add_f32_e32 v124, v201, v124\n"
    "v_cvt_pk_bf16_f32 v58, v200, v201\n"
    "ds_read_b128 v[208:211], v246 offset:28672\n"
    "v_mfma_f32_16x16x32_bf16 v[176:179], v[212:215], v[152:155], v[176:179]\n"
    "v_exp_f32_e32 v204, v204\n"
    "v_add_f32_e32 v124, v202, v124\n"
    "v_mfma_f32_16x16x32_bf16 v[180:183], v[212:215], v[156:159], v[180:183]\n"
    "v_exp_f32_e32 v205, v205\n"
    "v_mfma_f32_16x16x32_bf16 v[240:243], v[212:215], v[108:111], v[240:243]\n"
    "v_add_f32_e32 v124, v203, v124\n"
    "v_cvt_pk_bf16_f32 v59, v202, v203\n"
    "ds_read_b128 v[212:215], v247 offset:28672\n"
    "s_waitcnt lgkmcnt(1)\n"
    "v_mfma_f32_16x16x32_bf16 v[184:187], v[216:219], v[160:163], v[112:115]\n"
    "v_exp_f32_e32 v206, v206\n"
    "v_add_f32_e32 v125, v204, v125\n"
    "v_mfma_f32_16x16x32_bf16 v[188:191], v[216:219], v[164:167], v[116:119]\n"
    "v_exp_f32_e32 v207, v207\n"
    "ds_read_b128 v[216:219], v246 offset:32768\n"
    "v_mfma_f32_16x16x32_bf16 v[184:187], v[220:223], v[168:171], v[184:187]\n"
    "v_add_f32_e32 v125, v205, v125\n"
    "v_cvt_pk_bf16_f32 v62, v204, v205\n"
    "v_add_f32_e32 v125, v206, v125\n"
    "v_mfma_f32_16x16x32_bf16 v[188:191], v[220:223], v[172:175], v[188:191]\n"
    "v_add_f32_e32 v125, v207, v125\n"
    "v_cvt_pk_bf16_f32 v63, v206, v207\n"
    "ds_read_b128 v[220:223], v247 offset:32768\n"
    "v_mfma_f32_16x16x32_bf16 v[184:187], v[208:211], v[128:131], v[184:187]\n"
    "v_add_f32_e32 v192, v124, v125\n"
    "v_cmp_nge_f32_e32 vcc, s38, v192\n"
    "s_cbranch_vccnz .Lrare_a7\n"
    ".Lback_a7:\n"
    "v_mfma_f32_16x16x32_bf16 v[188:191], v[208:211], v[132:135], v[188:191]\n"
    "v_add_f32_e32 v252, v252, v124\n"
    "v_add_f32_e32 v253, v253, v125\n"
    "ds_read_b128 v[208:211], v248 offset:24576\n"
    "s_waitcnt lgkmcnt(1)\n"
    "v_mfma_f32_16x16x32_bf16 v[184:187], v[212:215], v[136:139], v[184:187]\n"
    "s_setprio 2\n"
    "v_exp_f32_e32 v240, v240\n"
    "v_exp_f32_e32 v241, v241\n"
    "v_mfma_f32_16x16x32_bf16 v[188:191], v[212:215], v[140:143], v[188:191]\n"
    "v_exp_f32_e32 v242, v242\n"
    "ds_read_b128 v[212:215], v249 offset:24576\n"
    "v_mfma_f32_16x16x32_bf16 v[184:187], v[216:219], v[144:147], v[184:187]\n"
    "v_exp_f32_e32 v243, v243\n"
    "v_cvt_pk_bf16_f32 v232, v240, v241\n"
    "v_mfma_f32_16x16x32_bf16 v[188:191], v[216:219], v[148:151], v[188:191]\n"
    "v_add_f32_e32 v240, v240, v241\n"
    "v_cvt_pk_bf16_f32 v233, v242, v243\n"
    "ds_read_b128 v[216:219], v248 offset:28672\n"
    "v_mfma_f32_16x16x32_bf16 v[184:187], v[220:223], v[152:155], v[184:187]\n"
    "s_add_u32 m0, s57, 0x2000\n"
    "s_add_u32 s61, s59, 0x24000\n"
    "buffer_load_dwordx4 v126, s[44:47], s61 offen nt lds\n"
    "v_add_f32_e32 v242, v242, v243\n"
    "v_add_f32_e32 v240, v240, v242\n"
    "v_mfma_f32_16x16x32_bf16 v[188:191], v[220:223], v[156:159], v[188:191]\n"
    "v_cmp_nge_f32_e32 vcc, s38, v240\n"
    "s_cbranch_vccnz .Lrare_n4\n"
    ".Lback_n4:\n"
    "v_add_f32_e32 v255, v255, v240\n"
    "ds_read_b128 v[220:223], v249 offset:28672\n"
    "s_waitcnt lgkmcnt(1)\n"
    "v_mfma_f32_16x16x32_bf16 v[192:195], v[208:211], v[160:163], v[112:115]\n"
    "v_exp_f32_e32 v176, v176\n"
    "v_mfma_f32_16x16x32_bf16 v[196:199], v[208:211], v[164:167], v[116:119]\n"
    "v_exp_f32_e32 v177, v177\n"
    "ds_read_b128 v[208:211], v248 offset:32768\n"
    "v_mfma_f32_16x16x32_bf16 v[192:195], v[212:215], v[168:171], v[192:195]\n"
    "v_exp_f32_e32 v178, v178\n"
    "v_exp_f32_e32 v179, v179\n"
    "v_mfma_f32_16x16x32_bf16 v[196:199], v[212:215], v[172:175], v[196:199]\n"
    "v_add_f32_e32 v124, v176, v177\n"
    "v_cvt_pk_bf16_f32 v64, v176, v177\n"
    "ds_read_b128 v[212:215], v249 offset:32768\n"
    "v_mfma_f32_16x16x32_bf16 v[192:195], v[216:219], v[128:131], v[192:195]\n"
    "v_exp_f32_e32 v180, v180\n"
    "v_add_f32_e32 v124, v178, v124\n"
    "v_mfma_f32_16x16x32_bf16 v[196:199], v[216:219], v[132:135], v[196:199]\n"
    "v_exp_f32_e32 v181, v181\n"
    "ds_read_b128 v[216:219], v250 offset:24576\n"
    "s_waitcnt lgkmcnt(1)\n"
    "v_mfma_f32_16x16x32_bf16 v[192:195], v[220:223], v[136:139], v[192:195]\n"
    "s_setprio 1\n"
    "v_add_f32_e32 v124, v179, v124\n"
    "v_cvt_pk_bf16_f32 v65, v178, v179\n"
    "v_mfma_f32_16x16x32_bf16 v[196:199], v[220:223], v[140:143], v[196:199]\n"
    "v_exp_f32_e32 v182, v182\n"
    "ds_read_b128 v[220:223], v251 offset:24576\n"
    "v_mfma_f32_16x16x32_bf16 v[192:195], v[208:211], v[144:147], v[192:195]\n"
    "s_add_u32 m0, s57, 0x4000\n"
    "s_add_u32 s61, s60, 0x24000\n"
    "buffer_load_dwordx4 v126, s[44:47], s61 offen nt lds\n"
    "v_exp_f32_e32 v183, v183\n"
    "v_add_f32_e32 v125, v180, v181\n"
    "v_mfma_f32_16x16x32_bf16 v[196:199], v[208:211], v[148:151], v[196:199]\n"
    "v_cvt_pk_bf16_f32 v68, v180, v181\n"
    "v_exp_f32_e32 v184, v184\n"
    "ds_read_b128 v[208:211], v250 offset:28672\n"
    "v_mfma_f32_16x16x32_bf16 v[192:195], v[212:215], v[152:155], v[192:195]\n"
    "v_add_f32_e32 v125, v182, v125\n"
    "v_exp_f32_e32 v185, v185\n"
    "v_mfma_f32_16x16x32_bf16 v[196:199], v[212:215], v[156:159], v[196:199]\n"
    "v_add_f32_e32 v125, v183, v125\n"
    "ds_read_b128 v[212:215], v251 offset:28672\n"
    "s_waitcnt lgkmcnt(1)\n"
    "v_mfma_f32_16x16x32_bf16 v[200:203], v[216:219], v[160:163], v[112:115]\n"
    "v_cvt_pk_bf16_f32 v69, v182, v183\n"
    "v_exp_f32_e32 v186, v186\n"
    "v_mfma_f32_16x16x32_bf16 v[204:207], v[216:219], v[164:167], v[116:119]\n"
    "v_add_f32_e32 v124, v184, v124\n"
    "v_exp_f32_e32 v187, v187\n"
    "ds_read_b128 v[216:219], v250 offset:32768\n"
    "v_mfma_f32_16x16x32_bf16 v[200:203], v[220:223], v[168:171], v[200:203]\n"
    "s_setprio 0\n"
    "v_add_f32_e32 v124, v185, v124\n"
    "v_cvt_pk_bf16_f32 v66, v184, v185\n"
    "v_mfma_f32_16x16x32_bf16 v[204:207], v[220:223], v[172:175], v[204:207]\n"
    "v_exp_f32_e32 v188, v188\n"
    "ds_read_b128 v[220:223], v251 offset:32768\n"
    "v_mfma_f32_16x16x32_bf16 v[200:203], v[208:211], v[128:131], v[200:203]\n"
    "v_add_f32_e32 v124, v186, v124\n"
    "v_exp_f32_e32 v189, v189\n"
    "v_add_f32_e32 v124, v187, v124\n"
    "v_mfma_f32_16x16x32_bf16 v[204:207], v[208:211], v[132:135], v[204:207]\n"
    "v_cvt_pk_bf16_f32 v67, v186, v187\n"
    "s_add_u32 m0, s57, 0x1e000\n"
    "s_add_u32 s61, s62, 0x12000\n"
    "buffer_load_dwordx4 v127, s[48:51], s61 offen nt lds\n"
    "s_waitcnt lgkmcnt(0)\n"
    "v_mfma_f32_16x16x32_bf16 v[200:203], v[212:215], v[136:139], v[200:203]\n"
    "v_exp_f32_e32 v190, v190\n"
    "v_add_f32_e32 v125, v188, v125\n"
    "v_mfma_f32_16x16x32_bf16 v[204:207], v[212:215], v[140:143], v[204:207]\n"
    "v_exp_f32_e32 v191, v191\n"
    "s_waitcnt vmcnt(5) lgkmcnt(0)\n"
    "s_barrier\n"
    "v_mfma_f32_16x16x32_bf16 v[200:203], v[216:219], v[144:147], v[200:203]\n"
    "v_add_f32_e32 v125, v189, v125\n"
    "v_cvt_pk_bf16_f32 v70, v188, v189\n"
    "v_add_f32_e32 v125, v190, v125\n"
    "s_setprio 3\n"
    "ds_read_b128 v[208:211], v244 offset:49152\n"
    "ds_read_b128 v[212:215], v245 offset:49152\n"
    "v_mfma_f32_16x16x32_bf16 v[204:207], v[216:219], v[148:151], v[204:207]\n"
    "v_add_f32_e32 v125, v191, v125\n"
    "v_cvt_pk_bf16_f32 v71, v190, v191\n"
    "ds_read_b128 v[216:219], v244 offset:53248\n"
    "v_mfma_f32_16x16x32_bf16 v[200:203], v[220:223], v[152:155], v[200:203]\n"
    "v_add_f32_e32 v176, v124, v125\n"
    "v_cmp_nge_f32_e32 vcc, s38, v176\n"
    "s_cbranch_vccnz .Lrare_a8\n"
    ".Lback_a8:\n"
    "v_mfma_f32_16x16x32_bf16 v[204:207], v[220:223], v[156:159], v[204:207]\n"
    "v_add_f32_e32 v252, v252, v124\n"
    "v_add_f32_e32 v253, v253, v125\n"
    "ds_read_b128 v[220:223], v245 offset:53248\n"
    "s_waitcnt lgkmcnt(1)\n"
    "v_mfma_f32_16x16x32_bf16 v[176:179], v[208:211], v[160:163], v[112:115]\n"
    "s_add_u32 m0, s57, 0x6000\n"
    "s_add_u32 s61, s58, 0x2a000\n"
    "buffer_load_dwordx4 v126, s[44:47], s61 offen nt lds\n"
    "v_exp_f32_e32 v192, v192\n"
    "v_mfma_f32_16x16x32_bf16 v[180:183], v[208:211], v[164:167], v[116:119]\n"
    "v_exp_f32_e32 v193, v193\n"
    "v_exp_f32_e32 v194, v194\n"
    "v_mfma_f32_16x16x32_bf16 v[240:243], v[208:211], v[88:91], v[120:123]\n"
    "v_exp_f32_e32 v195, v195\n"
    "ds_read_b128 v[208:211], v244 offset:57344\n"
    "v_mfma_f32_16x16x32_bf16 v[176:179], v[212:215], v[168:171], v[176:179]\n"
    "v_add_f32_e32 v124, v192, v193\n"
    "v_cvt_pk_bf16_f32 v72, v192, v193\n"
    "v_exp_f32_e32 v196, v196\n"
    "v_mfma_f32_16x16x32_bf16 v[180:183], v[212:215], v[172:175], v[180:183]\n"
    "v_add_f32_e32 v124, v194, v124\n"
    "v_mfma_f32_16x16x32_bf16 v[240:243], v[212:215], v[96:99], v[240:243]\n"
    "v_exp_f32_e32 v197, v197\n"
    "ds_read_b128 v[212:215], v245 offset:57344\n"
    "v_mfma_f32_16x16x32_bf16 v[176:179], v[216:219], v[128:131], v[176:179]\n"
    "v_add_f32_e32 v124, v195, v124\n"
    "v_cvt_pk_bf16_f32 v73, v194, v195\n"
    "v_exp_f32_e32 v198, v198\n"
    "v_mfma_f32_16x16x32_bf16 v[180:183], v[216:219], v[132:135], v[180:183]\n"
    "v_exp_f32_e32 v199, v199\n"
    "v_mfma_f32_16x16x32_bf16 v[240:243], v[216:219], v[92:95], v[240:243]\n"
    "v_add_f32_e32 v125, v196, v197\n"
    "ds_read_b128 v[216:219], v246 offset:49152\n"
    "s_waitcnt lgkmcnt(1)\n"
    "v_mfma_f32_16x16x32_bf16 v[176:179], v[220:223], v[136:139], v[176:179]\n"
    "v_cvt_pk_bf16_f32 v76, v196, v197\n"
    "v_exp_f32_e32 v200, v200\n"
    "v_mfma_f32_16x16x32_bf16 v[180:183], v[220:223], v[140:143], v[180:183]\n"
    "v_add_f32_e32 v125, v198, v125\n"
    "v_exp_f32_e32 v201, v201\n"
    "v_mfma_f32_16x16x32_bf16 v[240:243], v[220:223], v[100:103], v[240:243]\n"
    "v_add_f32_e32 v125, v199, v125\n"
    "v_cvt_pk_bf16_f32 v77, v198, v199\n"
    "ds_read_b128 v[220:223], v247 offset:49152\n"
    "v_mfma_f32_16x16x32_bf16 v[176:179], v[208:211], v[144:147], v[176:179]\n"
    "v_exp_f32_e32 v202, v202\n"
    "v_add_f32_e32 v124, v200, v124\n"
    "v_mfma_f32_16x16x32_bf16 v[180:183], v[208:211], v[148:151], v[180:183]\n"
    "v_exp_f32_e32 v203, v203\n"
    "v_mfma_f32_16x16x32_bf16 v[240:243], v[208:211], v[104:107], v[240:243]\n"
    "v_add_f32_e32 v124, v201, v124\n"
    "v_cvt_pk_bf16_f32 v74, v200, v201\n"
    "ds_read_b128 v[208:211], v246 offset:53248\n"
    "v_mfma_f32_16x16x32_bf16 v[176:179], v[212:215], v[152:155], v[176:179]\n"
    "v_exp_f32_e32 v204, v204\n"
    "v_add_f32_e32 v124, v202, v124\n"
    "v_mfma_f32_16x16x32_bf16 v[180:183], v[212:215], v[156:159], v[180:183]\n"
    "v_exp_f32_e32 v205, v205\n"
    "v_mfma_f32_16x16x32_bf16 v[240:243], v[212:215], v[108:111], v[240:243]\n"
    "v_add_f32_e32 v124, v203, v124\n"
    "v_cvt_pk_bf16_f32 v75, v202, v203\n"
    "ds_read_b128 v[212:215], v247 offset:53248\n"
    "s_waitcnt lgkmcnt(1)\n"
    "v_mfma_f32_16x16x32_bf16 v[184:187], v[216:219], v[160:163], v[112:115]\n"
    "v_exp_f32_e32 v206, v206\n"
    "v_add_f32_e32 v125, v204, v125\n"
    "v_mfma_f32_16x16x32_bf16 v[188:191], v[216:219], v[164:167], v[116:119]\n"
    "v_exp_f32_e32 v207, v207\n"
    "ds_read_b128 v[216:219], v246 offset:57344\n"
    "v_mfma_f32_16x16x32_bf16 v[184:187], v[220:223], v[168:171], v[184:187]\n"
    "v_add_f32_e32 v125, v205, v125\n"
    "v_cvt_pk_bf16_f32 v78, v204, v205\n"
    "v_add_f32_e32 v125, v206, v125\n"
    "v_mfma_f32_16x16x32_bf16 v[188:191], v[220:223], v[172:175], v[188:191]\n"
    "v_add_f32_e32 v125, v207, v125\n"
    "v_cvt_pk_bf16_f32 v79, v206, v207\n"
    "ds_read_b128 v[220:223], v247 offset:57344\n"
    "v_mfma_f32_16x16x32_bf16 v[184:187], v[208:211], v[128:131], v[184:187]\n"
    "v_add_f32_e32 v192, v124, v125\n"
    "v_cmp_nge_f32_e32 vcc, s38, v192\n"
    "s_cbranch_vccnz .Lrare_a9\n"
    ".Lback_a9:\n"
    "v_mfma_f32_16x16x32_bf16 v[188:191], v[208:211], v[132:135], v[188:191]\n"
    "v_add_f32_e32 v252, v252, v124\n"
    "v_add_f32_e32 v253, v253, v125\n"
    "ds_read_b128 v[208:211], v248 offset:49152\n"
    "s_waitcnt lgkmcnt(1)\n"
    "v_mfma_f32_16x16x32_bf16 v[184:187], v[212:215], v[136:139], v[184:187]\n"
    "s_setprio 2\n"
    "v_exp_f32_e32 v240, v240\n"
    "v_exp_f32_e32 v241, v241\n"
    "v_mfma_f32_16x16x32_bf16 v[188:191], v[212:215], v[140:143], v[188:191]\n"
    "v_exp_f32_e32 v242, v242\n"
    "ds_read_b128 v[212:215], v249 offset:49152\n"
    "v_mfma_f32_16x16x32_bf16 v[184:187], v[216:219], v[144:147], v[184:187]\n"
    "v_exp_f32_e32 v243, v243\n"
    "v_cvt_pk_bf16_f32 v234, v240, v241\n"
    "v_mfma_f32_16x16x32_bf16 v[188:191], v[216:219], v[148:151], v[188:191]\n"
    "v_add_f32_e32 v240, v240, v241\n"
    "v_cvt_pk_bf16_f32 v235, v242, v243\n"
    "ds_read_b128 v[216:219], v248 offset:53248\n"
    "v_mfma_f32_16x16x32_bf16 v[184:187], v[220:223], v[152:155], v[184:187]\n"
    "s_add_u32 m0, s57, 0x8000\n"
    "s_add_u32 s61, s59, 0x2a000\n"
    "buffer_load_dwordx4 v126, s[44:47], s61 offen nt lds\n"
    "v_add_f32_e32 v242, v242, v243\n"
    "v_add_f32_e32 v240, v240, v242\n"
    "v_mfma_f32_16x16x32_bf16 v[188:191], v[220:223], v[156:159], v[188:191]\n"
    "v_cmp_nge_f32_e32 vcc, s38, v240\n"
    "s_cbranch_vccnz .Lrare_n5\n"
    ".Lback_n5:\n"
    "v_add_f32_e32 v255, v255, v240\n"
    "ds_read_b128 v[220:223], v249 offset:53248\n"
    "s_waitcnt lgkmcnt(1)\n"
    "v_mfma_f32_16x16x32_bf16 v[192:195], v[208:211], v[160:163], v[112:115]\n"
    "v_exp_f32_e32 v176, v176\n"
    "v_mfma_f32_16x16x32_bf16 v[196:199], v[208:211], v[164:167], v[116:119]\n"
    "v_exp_f32_e32 v177, v177\n"
    "ds_read_b128 v[208:211], v248 offset:57344\n"
    "v_mfma_f32_16x16x32_bf16 v[192:195], v[212:215], v[168:171], v[192:195]\n"
    "v_exp_f32_e32 v178, v178\n"
    "v_exp_f32_e32 v179, v179\n"
    "v_mfma_f32_16x16x32_bf16 v[196:199], v[212:215], v[172:175], v[196:199]\n"
    "v_add_f32_e32 v124, v176, v177\n"
    "v_cvt_pk_bf16_f32 v80, v176, v177\n"
    "ds_read_b128 v[212:215], v249 offset:57344\n"
    "v_mfma_f32_16x16x32_bf16 v[192:195], v[216:219], v[128:131], v[192:195]\n"
    "v_exp_f32_e32 v180, v180\n"
    "v_add_f32_e32 v124, v178, v124\n"
    "v_mfma_f32_16x16x32_bf16 v[196:199], v[216:219], v[132:135], v[196:199]\n"
    "v_exp_f32_e32 v181, v181\n"
    "ds_read_b128 v[216:219], v250 offset:49152\n"
    "s_waitcnt lgkmcnt(1)\n"
    "v_mfma_f32_16x16x32_bf16 v[192:195], v[220:223], v[136:139], v[192:195]\n"
    "s_setprio 1\n"
    "v_add_u32_e32 v238, s20, v244\n"
    "v_add_f32_e32 v124, v179, v124\n"
    "v_cvt_pk_bf16_f32 v81, v178, v179\n"
    "v_mfma_f32_16x16x32_bf16 v[196:199], v[220:223], v[140:143], v[196:199]\n"
    "v_exp_f32_e32 v182, v182\n"
    "ds_read_b128 v[220:223], v251 offset:49152\n"
    "v_mfma_f32_16x16x32_bf16 v[192:195], v[208:211], v[144:147], v[192:195]\n"
    "s_add_u32 m0, s57, 0xa000\n"
    "s_add_u32 s61, s60, 0x2a000\n"
    "buffer_load_dwordx4 v126, s[44:47], s61 offen nt lds\n"
    "v_exp_f32_e32 v183, v183\n"
    "v_add_f32_e32 v125, v180, v181\n"
    "v_mfma_f32_16x16x32_bf16 v[196:199], v[208:211], v[148:151], v[196:199]\n"
    "v_cvt_pk_bf16_f32 v84, v180, v181\n"
    "v_exp_f32_e32 v184, v184\n"
    "ds_read_b128 v[208:211], v250 offset:53248\n"
    "v_mfma_f32_16x16x32_bf16 v[192:195], v[212:215], v[152:155], v[192:195]\n"
    "v_add_f32_e32 v125, v182, v125\n"
    "v_exp_f32_e32 v185, v185\n"
    "v_mfma_f32_16x16x32_bf16 v[196:199], v[212:215], v[156:159], v[196:199]\n"
    "v_add_f32_e32 v125, v183, v125\n"
    "ds_read_b128 v[212:215], v251 offset:53248\n"
    "s_waitcnt lgkmcnt(1)\n"
    "v_mfma_f32_16x16x32_bf16 v[200:203], v[216:219], v[160:163], v[112:115]\n"
    "v_add_u32_e32 v239, s20, v245\n"
    "v_cvt_pk_bf16_f32 v85, v182, v183\n"
    "v_exp_f32_e32 v186, v186\n"
    "v_mfma_f32_16x16x32_bf16 v[204:207], v[216:219], v[164:167], v[116:119]\n"
    "v_add_f32_e32 v124, v184, v124\n"
    "v_exp_f32_e32 v187, v187\n"
    "ds_read_b128 v[216:219], v250 offset:57344\n"
    "v_mfma_f32_16x16x32_bf16 v[200:203], v[220:223], v[168:171], v[200:203]\n"
    "s_setprio 0\n"
    "v_add_f32_e32 v124, v185, v124\n"
    "v_cvt_pk_bf16_f32 v82, v184, v185\n"
    "v_mfma_f32_16x16x32_bf16 v[204:207], v[220:223], v[172:175], v[204:207]\n"
    "v_exp_f32_e32 v188, v188\n"
    "ds_read_b128 v[220:223], v251 offset:57344\n"
    "v_mfma_f32_16x16x32_bf16 v[200:203], v[208:211], v[128:131], v[200:203]\n"
    "v_add_f32_e32 v124, v186, v124\n"
    "v_exp_f32_e32 v189, v189\n"
    "v_add_f32_e32 v124, v187, v124\n"
    "v_mfma_f32_16x16x32_bf16 v[204:207], v[208:211], v[132:135], v[204:207]\n"
    "v_cvt_pk_bf16_f32 v83, v186, v187\n"
    "s_add_u32 m0, s57, 0x20000\n"
    "s_add_u32 s61, s62, 0x18000\n"
    "buffer_load_dwordx4 v127, s[48:51], s61 offen nt lds\n"
    "s_waitcnt lgkmcnt(0)\n"
    "v_mfma_f32_16x16x32_bf16 v[200:203], v[212:215], v[136:139], v[200:203]\n"
    "v_exp_f32_e32 v190, v190\n"
    "v_add_f32_e32 v125, v188, v125\n"
    "v_mfma_f32_16x16x32_bf16 v[204:207], v[212:215], v[140:143], v[204:207]\n"
    "v_exp_f32_e32 v191, v191\n"
    "s_waitcnt vmcnt(5) lgkmcnt(0)\n"
    "s_barrier\n"
    "v_mfma_f32_16x16x32_bf16 v[200:203], v[216:219], v[144:147], v[200:203]\n"
    "v_add_f32_e32 v125, v189, v125\n"
    "v_cvt_pk_bf16_f32 v86, v188, v189\n"
    "v_add_f32_e32 v125, v190, v125\n"
    "v_add_f32_e32 v125, v191, v125\n"
    "s_setprio 3\n"
    "ds_read_b128 v[208:211], v244\n"
    "ds_read_b128 v[212:215], v238\n"
    "v_mfma_f32_16x16x32_bf16 v[204:207], v[216:219], v[148:151], v[204:207]\n"
    "v_cvt_pk_bf16_f32 v87, v190, v191\n"
    "s_add_u32 m0, s57, 0x22000\n"
    "s_add_u32 s61, s62, 0x1e000\n"
    "buffer_load_dwordx4 v127, s[48:51], s61 offen nt lds\n"
    "ds_read_b128 v[216:219], v245\n"
    "v_mfma_f32_16x16x32_bf16 v[200:203], v[220:223], v[152:155], v[200:203]\n"
    "v_add_f32_e32 v176, v124, v125\n"
    "v_cmp_nge_f32_e32 vcc, s38, v176\n"
    "s_cbranch_vccnz .Lrare_a10\n"
    ".Lback_a10:\n"
    "v_mfma_f32_16x16x32_bf16 v[204:207], v[220:223], v[156:159], v[204:207]\n"
    "v_add_f32_e32 v252, v252, v124\n"
    "v_add_f32_e32 v253, v253, v125\n"
    "s_waitcnt lgkmcnt(0)\n"
    "v_mfma_f32_16x16x32_bf16 v[176:179], v[208:211], v[160:163], v[112:115]\n"
    "v_exp_f32_e32 v192, v192\n"
    "v_exp_f32_e32 v193, v193\n"
    "v_mfma_f32_16x16x32_bf16 v[180:183], v[208:211], v[164:167], v[116:119]\n"
    "v_exp_f32_e32 v194, v194\n"
    "v_exp_f32_e32 v195, v195\n"
    "v_mfma_f32_16x16x32_bf16 v[240:243], v[208:211], v[212:215], v[120:123]\n"
    "s_add_u32 m0, s57, 0x24000\n"
    "s_add_u32 s61, s62, 0x24000\n"
    "buffer_load_dwordx4 v127, s[48:51], s61 offen nt lds\n"
    "ds_read_b128 v[220:223], v244 offset:4096\n"
    "v_mfma_f32_16x16x32_bf16 v[176:179], v[216:219], v[168:171], v[176:179]\n"
    "v_add_f32_e32 v124, v192, v193\n"
    "v_cvt_pk_bf16_f32 v88, v192, v193\n"
    "v_exp_f32_e32 v196, v196\n"
    "ds_read_b128 v[208:211], v238 offset:4096\n"
    "v_mfma_f32_16x16x32_bf16 v[180:183], v[216:219], v[172:175], v[180:183]\n"
    "v_add_f32_e32 v124, v194, v124\n"
    "ds_read_b128 v[212:215], v245 offset:4096\n"
    "v_mfma_f32_16x16x32_bf16 v[240:243], v[216:219], v[96:99], v[240:243]\n"
    "v_exp_f32_e32 v197, v197\n"
    "ds_read_b128 v[216:219], v244 offset:8192\n"
    "s_waitcnt lgkmcnt(1)\n"
    "v_mfma_f32_16x16x32_bf16 v[176:179], v[220:223], v[128:131], v[176:179]\n"
    "v_add_f32_e32 v124, v195, v124\n"
    "v_cvt_pk_bf16_f32 v89, v194, v195\n"
    "v_exp_f32_e32 v198, v198\n"
    "v_mfma_f32_16x16x32_bf16 v[180:183], v[220:223], v[132:135], v[180:183]\n"
    "v_exp_f32_e32 v199, v199\n"
    "v_mfma_f32_16x16x32_bf16 v[240:243], v[220:223], v[208:211], v[240:243]\n"
    "v_add_f32_e32 v125, v196, v197\n"
    "ds_read_b128 v[220:223], v245 offset:8192\n"
    "v_mfma_f32_16x16x32_bf16 v[176:179], v[212:215], v[136:139], v[176:179]\n"
    "v_cvt_pk_bf16_f32 v92, v196, v197\n"
    "v_exp_f32_e32 v200, v200\n"
    "ds_read_b128 v[208:211], v246\n"
    "v_mfma_f32_16x16x32_bf16 v[180:183], v[212:215], v[140:143], v[180:183]\n"
    "v_add_f32_e32 v125, v198, v125\n"
    "v_exp_f32_e32 v201, v201\n"
    "v_mfma_f32_16x16x32_bf16 v[240:243], v[212:215], v[100:103], v[240:243]\n"
    "v_add_f32_e32 v125, v199, v125\n"
    "v_cvt_pk_bf16_f32 v93, v198, v199\n"
    "ds_read_b128 v[212:215], v247\n"
    "s_waitcnt lgkmcnt(1)\n"
    "v_mfma_f32_16x16x32_bf16 v[176:179], v[216:219], v[144:147], v[176:179]\n"
    "v_exp_f32_e32 v202, v202\n"
    "v_add_f32_e32 v124, v200, v124\n"
    "v_mfma_f32_16x16x32_bf16 v[180:183], v[216:219], v[148:151], v[180:183]\n"
    "v_exp_f32_e32 v203, v203\n"
    "v_mfma_f32_16x16x32_bf16 v[240:243], v[216:219], v[104:107], v[240:243]\n"
    "v_add_f32_e32 v124, v201, v124\n"
    "v_cvt_pk_bf16_f32 v90, v200, v201\n"
    "ds_read_b128 v[216:219], v246 offset:4096\n"
    "v_mfma_f32_16x16x32_bf16 v[176:179], v[220:223], v[152:155], v[176:179]\n"
    "v_exp_f32_e32 v204, v204\n"
    "v_add_f32_e32 v124, v202, v124\n"
    "v_mfma_f32_16x16x32_bf16 v[180:183], v[220:223], v[156:159], v[180:183]\n"
    "v_exp_f32_e32 v205, v205\n"
    "v_mfma_f32_16x16x32_bf16 v[240:243], v[220:223], v[108:111], v[240:243]\n"
    "v_add_f32_e32 v124, v203, v124\n"
    "v_cvt_pk_bf16_f32 v91, v202, v203\n"
    "ds_read_b128 v[220:223], v247 offset:4096\n"
    "v_mfma_f32_16x16x32_bf16 v[184:187], v[208:211], v[160:163], v[112:115]\n"
    "v_exp_f32_e32 v206, v206\n"
    "v_add_f32_e32 v125, v204, v125\n"
    "v_mfma_f32_16x16x32_bf16 v[188:191], v[208:211], v[164:167], v[116:119]\n"
    "v_exp_f32_e32 v207, v207\n"
    "ds_read_b128 v[208:211], v246 offset:8192\n"
    "s_waitcnt lgkmcnt(1)\n"
    "v_mfma_f32_16x16x32_bf16 v[184:187], v[212:215], v[168:171], v[184:187]\n"
    "v_add_f32_e32 v125, v205, v125\n"
    "v_cvt_pk_bf16_f32 v94, v204, v205\n"
    "v_add_f32_e32 v125, v206, v125\n"
    "v_mfma_f32_16x16x32_bf16 v[188:191], v[212:215], v[172:175], v[188:191]\n"
    "v_add_f32_e32 v125, v207, v125\n"
    "v_cvt_pk_bf16_f32 v95, v206, v207\n"
    "ds_read_b128 v[212:215], v247 offset:8192\n"
    "v_mfma_f32_16x16x32_bf16 v[184:187], v[216:219], v[128:131], v[184:187]\n"
    "v_add_f32_e32 v192, v124, v125\n"
    "v_cmp_nge_f32_e32 vcc, s38, v192\n"
    "s_cbranch_vccnz .Lrare_a11\n"
    ".Lback_a11:\n"
    "v_add_f32_e32 v252, v252, v124\n"
    "v_mfma_f32_16x16x32_bf16 v[188:191], v[216:219], v[132:135], v[188:191]\n"
    "v_add_f32_e32 v253, v253, v125\n"
    "s_add_u32 m0, s57, 0x26000\n"
    "s_add_u32 s61, s62, 0x2a000\n"
    "buffer_load_dwordx4 v127, s[48:51], s61 offen nt lds\n"
    "ds_read_b128 v[216:219], v248\n"
    "v_mfma_f32_16x16x32_bf16 v[184:187], v[220:223], v[136:139], v[184:187]\n"
    "s_setprio 2\n"
    "v_exp_f32_e32 v240, v240\n"
    "v_exp_f32_e32 v241, v241\n"
    "v_mfma_f32_16x16x32_bf16 v[188:191], v[220:223], v[140:143], v[188:191]\n"
    "v_exp_f32_e32 v242, v242\n"
    "ds_read_b128 v[220:223], v249\n"
    "s_waitcnt lgkmcnt(1)\n"
    "v_mfma_f32_16x16x32_bf16 v[184:187], v[208:211], v[144:147], v[184:187]\n"
    "v_exp_f32_e32 v243, v243\n"
    "v_cvt_pk_bf16_f32 v236, v240, v241\n"
    "v_mfma_f32_16x16x32_bf16 v[188:191], v[208:211], v[148:151], v[188:191]\n"
    "v_add_f32_e32 v240, v240, v241\n"
    "v_cvt_pk_bf16_f32 v237, v242, v243\n"
    "ds_read_b128 v[208:211], v248 offset:4096\n"
    "v_mfma_f32_16x16x32_bf16 v[184:187], v[212:215], v[152:155], v[184:187]\n"
    "v_add_f32_e32 v242, v242, v243\n"
    "v_add_f32_e32 v240, v240, v242\n"
    "v_cmp_nge_f32_e32 vcc, s38, v240\n"
    "v_mfma_f32_16x16x32_bf16 v[188:191], v[212:215], v[156:159], v[188:191]\n"
    "s_cbranch_vccnz .Lrare_n6\n"
    ".Lback_n6:\n"
    "v_add_f32_e32 v255, v255, v240\n"
    "ds_read_b128 v[212:215], v249 offset:4096\n"
    "v_mfma_f32_16x16x32_bf16 v[192:195], v[216:219], v[160:163], v[112:115]\n"
    "v_exp_f32_e32 v176, v176\n"
    "v_mfma_f32_16x16x32_bf16 v[196:199], v[216:219], v[164:167], v[116:119]\n"
    "v_exp_f32_e32 v177, v177\n"
    "ds_read_b128 v[216:219], v248 offset:8192\n"
    "s_waitcnt lgkmcnt(1)\n"
    "v_mfma_f32_16x16x32_bf16 v[192:195], v[220:223], v[168:171], v[192:195]\n"
    "v_exp_f32_e32 v178, v178\n"
    "v_exp_f32_e32 v179, v179\n"
    "v_mfma_f32_16x16x32_bf16 v[196:199], v[220:223], v[172:175], v[196:199]\n"
    "v_add_f32_e32 v124, v176, v177\n"
    "v_cvt_pk_bf16_f32 v96, v176, v177\n"
    "ds_read_b128 v[220:223], v249 offset:8192\n"
    "v_mfma_f32_16x16x32_bf16 v[192:195], v[208:211], v[128:131], v[192:195]\n"
    "v_mbcnt_lo_u32_b32 v126, -1, 0\n"
    "v_mbcnt_hi_u32_b32 v126, -1, v126\n"
    "v_exp_f32_e32 v180, v180\n"
    "v_mfma_f32_16x16x32_bf16 v[196:199], v[208:211], v[132:135], v[196:199]\n"
    "v_add_f32_e32 v124, v178, v124\n"
    "v_exp_f32_e32 v181, v181\n"
    "ds_read_b128 v[208:211], v250\n"
    "v_mfma_f32_16x16x32_bf16 v[192:195], v[212:215], v[136:139], v[192:195]\n"
    "s_setprio 1\n"
    "v_lshl_add_u32 v126, v126, 4, s34\n"
    "v_add_f32_e32 v124, v179, v124\n"
    "v_cvt_pk_bf16_f32 v97, v178, v179\n"
    "v_mfma_f32_16x16x32_bf16 v[196:199], v[212:215], v[140:143], v[196:199]\n"
    "v_exp_f32_e32 v182, v182\n"
    "ds_read_b128 v[212:215], v251\n"
    "s_waitcnt lgkmcnt(1)\n"
    "v_mfma_f32_16x16x32_bf16 v[192:195], v[216:219], v[144:147], v[192:195]\n"
    "ds_read_b32 v126, v126 offset:1024\n"
    "s_and_b32 s66, s39, 8\n"
    "s_lshl_b32 s66, s66, 3\n"
    "v_exp_f32_e32 v183, v183\n"
    "v_add_f32_e32 v125, v180, v181\n"
    "v_mfma_f32_16x16x32_bf16 v[196:199], v[216:219], v[148:151], v[196:199]\n"
    "v_cvt_pk_bf16_f32 v100, v180, v181\n"
    "v_exp_f32_e32 v184, v184\n"
    "ds_read_b128 v[216:219], v250 offset:4096\n"
    "v_mfma_f32_16x16x32_bf16 v[192:195], v[220:223], v[152:155], v[192:195]\n"
    "s_add_u32 s58, s63, s66\n"
    "s_xor_b32 s67, s66, 64\n"
    "s_add_u32 s59, s63, s67\n"
    "s_add_u32 s60, s58, 6144\n"
    "v_add_f32_e32 v125, v182, v125\n"
    "v_exp_f32_e32 v185, v185\n"
    "v_mfma_f32_16x16x32_bf16 v[196:199], v[220:223], v[156:159], v[196:199]\n"
    "v_add_f32_e32 v125, v183, v125\n"
    "ds_read_b128 v[220:223], v251 offset:4096\n"
    "v_mfma_f32_16x16x32_bf16 v[200:203], v[208:211], v[160:163], v[112:115]\n"
    "s_add_u32 s64, s59, 6144\n"
    "s_mov_b32 s58, s63\n"
    "s_add_u32 s59, s63, 3072\n"
    "s_add_u32 s60, s63, 6144\n"
    "v_cvt_pk_bf16_f32 v101, v182, v183\n"
    "v_exp_f32_e32 v186, v186\n"
    "v_mfma_f32_16x16x32_bf16 v[204:207], v[208:211], v[164:167], v[116:119]\n"
    "v_add_f32_e32 v124, v184, v124\n"
    "v_exp_f32_e32 v187, v187\n"
    "ds_read_b128 v[208:211], v250 offset:8192\n"
    "s_waitcnt lgkmcnt(1)\n"
    "v_mfma_f32_16x16x32_bf16 v[200:203], v[212:215], v[168:171], v[200:203]\n"
    "s_setprio 0\n"
    "s_add_u32 s64, s63, 9216\n"
    "v_add_u32_e32 v126, s21, v126\n"
    "v_add_f32_e32 v124, v185, v124\n"
    "v_cvt_pk_bf16_f32 v98, v184, v185\n"
    "v_mfma_f32_16x16x32_bf16 v[204:207], v[212:215], v[172:175], v[204:207]\n"
    "v_exp_f32_e32 v188, v188\n"
    "ds_read_b128 v[212:215], v251 offset:8192\n"
    "v_mfma_f32_16x16x32_bf16 v[200:203], v[216:219], v[128:131], v[200:203]\n"
    "v_add_f32_e32 v124, v186, v124\n"
    "v_exp_f32_e32 v189, v189\n"
    "v_mfma_f32_16x16x32_bf16 v[204:207], v[216:219], v[132:135], v[204:207]\n"
    "v_add_f32_e32 v124, v187, v124\n"
    "v_cvt_pk_bf16_f32 v99, v186, v187\n"
    "v_mfma_f32_16x16x32_bf16 v[200:203], v[220:223], v[136:139], v[200:203]\n"
    "v_exp_f32_e32 v190, v190\n"
    "v_add_f32_e32 v125, v188, v125\n"
    "v_mfma_f32_16x16x32_bf16 v[204:207], v[220:223], v[140:143], v[204:207]\n"
    "v_exp_f32_e32 v191, v191\n"
    "s_waitcnt vmcnt(4) lgkmcnt(0)\n"
    "s_barrier\n"
    "v_mfma_f32_16x16x32_bf16 v[200:203], v[208:211], v[144:147], v[200:203]\n"
    "v_add_f32_e32 v125, v189, v125\n"
    "v_cvt_pk_bf16_f32 v102, v188, v189\n"
    "v_add_f32_e32 v125, v190, v125\n"
    "s_setprio 3\n"
    "ds_read_b128 v[216:219], v244 offset:24576\n"
    "ds_read_b128 v[220:223], v238\n"
    "v_mfma_f32_16x16x32_bf16 v[204:207], v[208:211], v[148:151], v[204:207]\n"
    "v_add_f32_e32 v125, v191, v125\n"
    "v_cvt_pk_bf16_f32 v103, v190, v191\n"
    "v_mfma_f32_16x16x32_bf16 v[200:203], v[212:215], v[152:155], v[200:203]\n"
    "v_add_f32_e32 v176, v124, v125\n"
    "v_cmp_nge_f32_e32 vcc, s38, v176\n"
    "s_cbranch_vccnz .Lrare_a12\n"
    ".Lback_a12:\n"
    "v_mfma_f32_16x16x32_bf16 v[204:207], v[212:215], v[156:159], v[204:207]\n"
    "v_add_f32_e32 v252, v252, v124\n"
    "v_add_f32_e32 v253, v253, v125\n"
    "ds_read_b128 v[208:211], v245 offset:24576\n"
    "s_waitcnt lgkmcnt(1)\n"
    "v_mfma_f32_16x16x32_bf16 v[176:179], v[216:219], v[160:163], v[112:115]\n"
    "v_exp_f32_e32 v192, v192\n"
    "ds_read_b128 v[212:215], v239\n"
    "v_mfma_f32_16x16x32_bf16 v[180:183], v[216:219], v[164:167], v[116:119]\n"
    "v_exp_f32_e32 v193, v193\n"
    "v_mfma_f32_16x16x32_bf16 v[240:243], v[216:219], v[220:223], v[120:123]\n"
    "v_exp_f32_e32 v194, v194\n"
    "ds_read_b128 v[216:219], v244 offset:28672\n"
    "s_waitcnt lgkmcnt(1)\n"
    "v_mfma_f32_16x16x32_bf16 v[176:179], v[208:211], v[168:171], v[176:179]\n"
    "v_exp_f32_e32 v195, v195\n"
    "ds_read_b128 v[220:223], v238 offset:4096\n"
    "v_mfma_f32_16x16x32_bf16 v[180:183], v[208:211], v[172:175], v[180:183]\n"
    "v_add_f32_e32 v124, v192, v193\n"
    "v_cvt_pk_bf16_f32 v104, v192, v193\n"
    "v_mfma_f32_16x16x32_bf16 v[240:243], v[208:211], v[212:215], v[240:243]\n"
    "v_exp_f32_e32 v196, v196\n"
    "ds_read_b128 v[208:211], v245 offset:28672\n"
    "s_waitcnt lgkmcnt(1)\n"
    "v_mfma_f32_16x16x32_bf16 v[176:179], v[216:219], v[128:131], v[176:179]\n"
    "v_add_f32_e32 v124, v194, v124\n"
    "v_exp_f32_e32 v197, v197\n"
    "ds_read_b128 v[212:215], v239 offset:4096\n"
    "v_mfma_f32_16x16x32_bf16 v[180:183], v[216:219], v[132:135], v[180:183]\n"
    "v_add_f32_e32 v124, v195, v124\n"
    "v_mfma_f32_16x16x32_bf16 v[240:243], v[216:219], v[220:223], v[240:243]\n"
    "v_cvt_pk_bf16_f32 v105, v194, v195\n"
    "ds_read_b128 v[216:219], v244 offset:32768\n"
    "s_waitcnt lgkmcnt(1)\n"
    "v_mfma_f32_16x16x32_bf16 v[176:179], v[208:211], v[136:139], v[176:179]\n"
    "v_exp_f32_e32 v198, v198\n"
    "ds_read_b128 v[220:223], v238 offset:8192\n"
    "v_mfma_f32_16x16x32_bf16 v[180:183], v[208:211], v[140:143], v[180:183]\n"
    "v_exp_f32_e32 v199, v199\n"
    "v_mfma_f32_16x16x32_bf16 v[240:243], v[208:211], v[212:215], v[240:243]\n"
    "v_add_f32_e32 v125, v196, v197\n"
    "v_cvt_pk_bf16_f32 v108, v196, v197\n"
    "ds_read_b128 v[208:211], v245 offset:32768\n"
    "s_waitcnt lgkmcnt(1)\n"
    "v_mfma_f32_16x16x32_bf16 v[176:179], v[216:219], v[144:147], v[176:179]\n"
    "v_exp_f32_e32 v200, v200\n"
    "ds_read_b128 v[212:215], v239 offset:8192\n"
    "v_mfma_f32_16x16x32_bf16 v[180:183], v[216:219], v[148:151], v[180:183]\n"
    "v_add_f32_e32 v125, v198, v125\n"
    "v_exp_f32_e32 v201, v201\n"
    "v_mfma_f32_16x16x32_bf16 v[240:243], v[216:219], v[220:223], v[240:243]\n"
    "v_add_f32_e32 v125, v199, v125\n"
    "ds_read_b128 v[216:219], v246 offset:24576\n"
    "s_waitcnt lgkmcnt(1)\n"
    "v_mfma_f32_16x16x32_bf16 v[176:179], v[208:211], v[152:155], v[176:179]\n"
    "v_cvt_pk_bf16_f32 v109, v198, v199\n"
    "v_exp_f32_e32 v202, v202\n"
    "ds_read_b128 v[220:223], v247 offset:24576\n"
    "v_mfma_f32_16x16x32_bf16 v[180:183], v[208:211], v[156:159], v[180:183]\n"
    "v_add_f32_e32 v124, v200, v124\n"
    "v_mfma_f32_16x16x32_bf16 v[240:243], v[208:211], v[212:215], v[240:243]\n"
    "v_exp_f32_e32 v203, v203\n"
    "ds_read_b128 v[208:211], v246 offset:28672\n"
    "s_waitcnt lgkmcnt(1)\n"
    "v_mfma_f32_16x16x32_bf16 v[184:187], v[216:219], v[160:163], v[112:115]\n"
    "v_add_f32_e32 v124, v201, v124\n"
    "v_cvt_pk_bf16_f32 v106, v200, v201\n"
    "ds_read_b128 v[212:215], v247 offset:28672\n"
    "v_mfma_f32_16x16x32_bf16 v[188:191], v[216:219], v[164:167], v[116:119]\n"
    "v_exp_f32_e32 v204, v204\n"
    "ds_read_b128 v[216:219], v246 offset:32768\n"
    "v_mfma_f32_16x16x32_bf16 v[184:187], v[220:223], v[168:171], v[184:187]\n"
    "v_add_f32_e32 v124, v202, v124\n"
    "v_exp_f32_e32 v205, v205\n"
    "v_mfma_f32_16x16x32_bf16 v[188:191], v[220:223], v[172:175], v[188:191]\n"
    "v_add_f32_e32 v124, v203, v124\n"
    "ds_read_b128 v[220:223], v247 offset:32768\n"
    "s_waitcnt lgkmcnt(1)\n"
    "v_mfma_f32_16x16x32_bf16 v[184:187], v[208:211], v[128:131], v[184:187]\n"
    "v_cvt_pk_bf16_f32 v107, v202, v203\n"
    "v_exp_f32_e32 v206, v206\n"
    "v_mfma_f32_16x16x32_bf16 v[188:191], v[208:211], v[132:135], v[188:191]\n"
    "v_add_f32_e32 v125, v204, v125\n"
    "ds_read_b128 v[208:211], v248 offset:24576\n"
    "v_mfma_f32_16x16x32_bf16 v[184:187], v[212:215], v[136:139], v[184:187]\n"
    "s_setprio 2\n"
    "v_exp_f32_e32 v207, v207\n"
    "v_mfma_f32_16x16x32_bf16 v[188:191], v[212:215], v[140:143], v[188:191]\n"
    "v_add_f32_e32 v125, v205, v125\n"
    "v_cvt_pk_bf16_f32 v110, v204, v205\n"
    "ds_read_b128 v[212:215], v249 offset:24576\n"
    "v_mfma_f32_16x16x32_bf16 v[184:187], v[216:219], v[144:147], v[184:187]\n"
    "v_add_f32_e32 v125, v206, v125\n"
    "v_add_f32_e32 v125, v207, v125\n"
    "v_mfma_f32_16x16x32_bf16 v[188:191], v[216:219], v[148:151], v[188:191]\n"
    "v_cvt_pk_bf16_f32 v111, v206, v207\n"
    "v_add_f32_e32 v192, v124, v125\n"
    "ds_read_b128 v[216:219], v248 offset:28672\n"
    "s_waitcnt lgkmcnt(1)\n"
    "v_mfma_f32_16x16x32_bf16 v[184:187], v[220:223], v[152:155], v[184:187]\n"
    "v_cmp_nge_f32_e32 vcc, s38, v192\n"
    "s_cbranch_vccnz .Lrare_a13\n"
    ".Lback_a13:\n"
    "v_add_f32_e32 v252, v252, v124\n"
    "v_mfma_f32_16x16x32_bf16 v[188:191], v[220:223], v[156:159], v[188:191]\n"
    "v_add_f32_e32 v253, v253, v125\n"
    "ds_read_b128 v[220:223], v249 offset:28672\n"
    "v_mfma_f32_16x16x32_bf16 v[192:195], v[208:211], v[160:163], v[112:115]\n"
    "v_mbcnt_lo_u32_b32 v120, -1, 0\n"
    "v_mbcnt_hi_u32_b32 v120, -1, v120\n"
    "v_lshl_add_u32 v120, v120, 4, s34\n"
    "v_mfma_f32_16x16x32_bf16 v[196:199], v[208:211], v[164:167], v[116:119]\n"
    "ds_read_b128 v[120:123], v120 offset:1024\n"
    "ds_write_b64 v126, v[224:225]\n"
    "ds_read_b128 v[208:211], v248 offset:32768\n"
    "v_mfma_f32_16x16x32_bf16 v[192:195], v[212:215], v[168:171], v[192:195]\n"
    "v_exp_f32_e32 v240, v240\n"
    "v_exp_f32_e32 v241, v241\n"
    "v_mfma_f32_16x16x32_bf16 v[196:199], v[212:215], v[172:175], v[196:199]\n"
    "ds_write_b64 v126, v[226:227] offset:128\n"
    "ds_read_b128 v[212:215], v249 offset:32768\n"
    "s_waitcnt lgkmcnt(2)\n"
    "v_mfma_f32_16x16x32_bf16 v[192:195], v[216:219], v[128:131], v[192:195]\n"
    "v_exp_f32_e32 v242, v242\n"
    "v_mfma_f32_16x16x32_bf16 v[196:199], v[216:219], v[132:135], v[196:199]\n"
    "ds_read_b128 v[216:219], v250 offset:24576\n"
    "v_mfma_f32_16x16x32_bf16 v[192:195], v[220:223], v[136:139], v[192:195]\n"
    "s_setprio 1\n"
    "v_exp_f32_e32 v243, v243\n"
    "v_cvt_pk_bf16_f32 v238, v240, v241\n"
    "v_mfma_f32_16x16x32_bf16 v[196:199], v[220:223], v[140:143], v[196:199]\n"
    "v_add_f32_e32 v240, v240, v241\n"
    "ds_write_b64 v126, v[228:229] offset:256\n"
    "ds_read_b128 v[220:223], v251 offset:24576\n"
    "v_mfma_f32_16x16x32_bf16 v[192:195], v[208:211], v[144:147], v[192:195]\n"
    "v_cvt_pk_bf16_f32 v239, v242, v243\n"
    "v_add_f32_e32 v242, v242, v243\n"
    "v_add_f32_e32 v240, v240, v242\n"
    "v_mfma_f32_16x16x32_bf16 v[196:199], v[208:211], v[148:151], v[196:199]\n"
    "ds_write_b64 v126, v[230:231] offset:384\n"
    "ds_read_b128 v[208:211], v250 offset:28672\n"
    "s_waitcnt lgkmcnt(2)\n"
    "v_mfma_f32_16x16x32_bf16 v[192:195], v[212:215], v[152:155], v[192:195]\n"
    "v_add_u32_e32 v120, s35, v121\n"
    "v_add_u32_e32 v121, s17, v121\n"
    "v_xor_b32_e32 v123, s22, v123\n"
    "v_mfma_f32_16x16x32_bf16 v[196:199], v[212:215], v[156:159], v[196:199]\n"
    "v_xor_b32_e32 v244, 0x20, v123\n"
    "ds_write_b64 v126, v[232:233] offset:512\n"
    "ds_read_b128 v[212:215], v251 offset:28672\n"
    "v_mfma_f32_16x16x32_bf16 v[200:203], v[216:219], v[160:163], v[112:115]\n"
    "v_xor_b32_e32 v245, 0x820, v123\n"
    "v_xor_b32_e32 v246, 0x40, v123\n"
    "v_xor_b32_e32 v247, 0x840, v123\n"
    "v_xor_b32_e32 v160, 0x1020, v123\n"
    "v_mfma_f32_16x16x32_bf16 v[204:207], v[216:219], v[164:167], v[116:119]\n"
    "v_xor_b32_e32 v161, 0x1820, v123\n"
    "v_xor_b32_e32 v162, 0x1040, v123\n"
    "v_xor_b32_e32 v163, 0x1840, v123\n"
    "ds_read_b128 v[216:219], v250 offset:32768\n"
    "v_mfma_f32_16x16x32_bf16 v[200:203], v[220:223], v[168:171], v[200:203]\n"
    "s_setprio 0\n"
    "ds_write_b64 v126, v[234:235] offset:640\n"
    "v_xor_b32_e32 v164, 0x1060, v123\n"
    "v_xor_b32_e32 v165, 0x1860, v123\n"
    "v_xor_b32_e32 v166, 0x1000, v123\n"
    "v_xor_b32_e32 v167, 0x1800, v123\n"
    "v_xor_b32_e32 v248, 0x60, v123\n"
    "v_mfma_f32_16x16x32_bf16 v[204:207], v[220:223], v[172:175], v[204:207]\n"
    "v_xor_b32_e32 v249, 0x860, v123\n"
    "v_mov_b32_e32 v171, v112\n"
    "v_mov_b32_e32 v172, v116\n"
    "v_cmp_nge_f32_e32 vcc, s38, v240\n"
    "s_cbranch_vccnz .Lrare_n7\n"
    ".Lback_n7:\n"
    "v_add_f32_e32 v255, v255, v240\n"
    "ds_read_b128 v[220:223], v251 offset:32768\n"
    "s_waitcnt lgkmcnt(1)\n"
    "v_mfma_f32_16x16x32_bf16 v[200:203], v[208:211], v[128:131], v[200:203]\n"
    "ds_write_b64 v126, v[236:237] offset:768\n"
    "ds_write_b64 v126, v[238:239] offset:896\n"
    "v_mov_b32_e32 v113, v255\n"
    "s_nop 1\n"
    "v_permlane16_swap_b32_e32 v255, v113\n"
    "v_add_f32_e32 v255, v255, v113\n"
    "v_mfma_f32_16x16x32_bf16 v[204:207], v[208:211], v[132:135], v[204:207]\n"
    "v_mov_b32_e32 v113, v255\n"
    "s_nop 1\n"
    "v_permlane32_swap_b32_e32 v255, v113\n"
    "v_add_f32_e32 v255, v255, v113\n"
    "v_add_u32_e32 v114, s16, v121\n"
    "ds_write_b32 v114, v254\n"
    "v_mfma_f32_16x16x32_bf16 v[200:203], v[212:215], v[136:139], v[200:203]\n"
    "ds_write_b32 v114, v255 offset:512\n"
    "v_mbcnt_lo_u32_b32 v240, -1, 0\n"
    "v_mbcnt_hi_u32_b32 v240, -1, v240\n"
    "v_lshl_add_u32 v240, v240, 4, s34\n"
    "ds_read_b128 v[240:243], v240\n"
    "v_mfma_f32_16x16x32_bf16 v[204:207], v[212:215], v[140:143], v[204:207]\n"
    "s_and_b32 s66, s39, 4\n"
    "s_lshl_b32 s66, s66, 3\n"
    "v_mov_b32_e32 v255, v122\n"
    "s_waitcnt lgkmcnt(0)\n"
    "v_add_u32_e32 v240, s23, v240\n"
    "v_add_u32_e32 v242, s24, v242\n"
    "v_mov_b32_e32 v243, v127\n"
    "v_xor_b32_e32 v250, 0x0, v123\n"
    "v_xor_b32_e32 v251, 0x800, v123\n"
    "s_waitcnt vmcnt(15) lgkmcnt(0)\n"
    "s_barrier\n"
    "s_setprio 3\n"
    "v_mov_b32_e32 v128, 0x14400\n"
    "v_mfma_f32_16x16x32_bf16 v[200:203], v[216:219], v[144:147], v[200:203]\n"
    "ds_read_b32 v129, v128\n"
    "ds_read_b64_tr_b16 v[208:209], v244\n"
    "ds_read_b64_tr_b16 v[210:211], v245\n"
    "ds_read_b64_tr_b16 v[212:213], v246\n"
    "ds_read_b64_tr_b16 v[214:215], v247\n"
    "v_mfma_f32_16x16x32_bf16 v[204:207], v[216:219], v[148:151], v[204:207]\n"
    "ds_read_b64_tr_b16 v[216:217], v248\n"
    "v_mfma_f32_16x16x32_bf16 v[200:203], v[220:223], v[152:155], v[200:203]\n"
    "ds_read_b64_tr_b16 v[218:219], v249\n"
    "v_mfma_f32_16x16x32_bf16 v[204:207], v[220:223], v[156:159], v[204:207]\n"
    "ds_read_b64_tr_b16 v[220:221], v160\n"
    "s_waitcnt lgkmcnt(2)\n"
    "v_mfma_f32_16x16x32_bf16 v[136:139], v[208:211], v[0:3], 0\n"
    "ds_read_b64_tr_b16 v[222:223], v161\n"
    "v_mfma_f32_16x16x32_bf16 v[140:143], v[208:211], v[4:7], 0\n"
    "ds_read_b64_tr_b16 v[208:209], v162\n"
    "v_mfma_f32_16x16x32_bf16 v[144:147], v[212:215], v[0:3], 0\n"
    "s_add_u32 m0, s57, 0x4200\n"
    "s_add_u32 s61, s62, 0x80\n"
    "buffer_load_dwordx4 v243, s[48:51], s61 offen nt lds\n"
    "v_exp_f32_e32 v176, v176\n"
    "ds_read_b64_tr_b16 v[210:211], v163\n"
    "v_mfma_f32_16x16x32_bf16 v[148:151], v[212:215], v[4:7], 0\n"
    "v_exp_f32_e32 v177, v177\n"
    "ds_read_b64_tr_b16 v[212:213], v164\n"
    "s_waitcnt lgkmcnt(2)\n"
    "v_mfma_f32_16x16x32_bf16 v[152:155], v[216:219], v[0:3], 0\n"
    "v_exp_f32_e32 v178, v178\n"
    "ds_read_b64_tr_b16 v[214:215], v165\n"
    "v_mfma_f32_16x16x32_bf16 v[156:159], v[216:219], v[4:7], 0\n"
    "v_readfirstlane_b32 s66, v129\n"
    "s_nop 3\n"
    "s_cmp_lg_u32 s66, 0\n"
    "s_cbranch_scc1 .Lslow\n"
    ".Lslowback:\n"
    "v_mfma_f32_16x16x32_bf16 v[136:139], v[220:223], v[8:11], v[136:139]\n"
    "s_setprio 2\n"
    "v_exp_f32_e32 v179, v179\n"
    "ds_read_b64_tr_b16 v[216:217], v250\n"
    "ds_read_b64_tr_b16 v[218:219], v251\n"
    "ds_read_b128 v[224:227], v240\n"
    "v_mfma_f32_16x16x32_bf16 v[140:143], v[220:223], v[12:15], v[140:143]\n"
    "v_add_f32_e32 v124, v176, v177\n"
    "v_cvt_pk_bf16_f32 v112, v176, v177\n"
    "ds_read_b64_tr_b16 v[220:221], v166\n"
    "ds_read_b64_tr_b16 v[222:223], v167\n"
    "s_waitcnt lgkmcnt(5)\n"
    "v_mfma_f32_16x16x32_bf16 v[144:147], v[208:211], v[8:11], v[144:147]\n"
    "v_exp_f32_e32 v180, v180\n"
    "ds_read_b128 v[228:231], v242\n"
    "v_mfma_f32_16x16x32_bf16 v[148:151], v[208:211], v[12:15], v[148:151]\n"
    "v_add_f32_e32 v124, v178, v124\n"
    "ds_read_b64_tr_b16 v[208:209], v244 offset:8192\n"
    "v_mfma_f32_16x16x32_bf16 v[152:155], v[212:215], v[8:11], v[152:155]\n"
    "v_exp_f32_e32 v181, v181\n"
    "ds_read_b64_tr_b16 v[210:211], v245 offset:8192\n"
    "v_mfma_f32_16x16x32_bf16 v[156:159], v[212:215], v[12:15], v[156:159]\n"
    "v_add_f32_e32 v124, v179, v124\n"
    "v_cvt_pk_bf16_f32 v113, v178, v179\n"
    "ds_read_b64_tr_b16 v[212:213], v246 offset:8192\n"
    "s_waitcnt lgkmcnt(2)\n"
    "v_mfma_f32_16x16x32_bf16 v[128:131], v[216:219], v[0:3], 0\n"
    "v_exp_f32_e32 v182, v182\n"
    "ds_read_b64_tr_b16 v[214:215], v247 offset:8192\n"
    "v_mfma_f32_16x16x32_bf16 v[132:135], v[216:219], v[4:7], 0\n"
    "v_exp_f32_e32 v183, v183\n"
    "v_mfma_f32_16x16x32_bf16 v[232:235], v[216:219], v[224:227], 0\n"
    "v_add_f32_e32 v125, v180, v181\n"
    "ds_read_b64_tr_b16 v[216:217], v248 offset:8192\n"
    "v_mfma_f32_16x16x32_bf16 v[128:131], v[220:223], v[8:11], v[128:131]\n"
    "s_setprio 1\n"
    "s_add_u32 m0, s57, 0x6200\n"
    "s_add_u32 s61, s62, 0x6080\n"
    "buffer_load_dwordx4 v243, s[48:51], s61 offen nt lds\n"
    "v_cvt_pk_bf16_f32 v116, v180, v181\n"
    "ds_read_b64_tr_b16 v[218:219], v249 offset:8192\n"
    "v_mfma_f32_16x16x32_bf16 v[132:135], v[220:223], v[12:15], v[132:135]\n"
    "v_exp_f32_e32 v184, v184\n"
    "v_add_f32_e32 v125, v182, v125\n"
    "v_mfma_f32_16x16x32_bf16 v[232:235], v[220:223], v[228:231], v[232:235]\n"
    "v_exp_f32_e32 v185, v185\n"
    "ds_read_b64_tr_b16 v[220:221], v250 offset:8192\n"
    "ds_read_b64_tr_b16 v[222:223], v251 offset:8192\n"
    "s_waitcnt lgkmcnt(2)\n"
    "v_mfma_f32_16x16x32_bf16 v[136:139], v[208:211], v[16:19], v[136:139]\n"
    "v_add_f32_e32 v125, v183, v125\n"
    "ds_read_b128 v[224:227], v240 offset:128\n"
    "v_mfma_f32_16x16x32_bf16 v[140:143], v[208:211], v[20:23], v[140:143]\n"
    "v_cvt_pk_bf16_f32 v117, v182, v183\n"
    "ds_read_b64_tr_b16 v[208:209], v160 offset:8192\n"
    "v_mfma_f32_16x16x32_bf16 v[144:147], v[212:215], v[16:19], v[144:147]\n"
    "s_add_u32 m0, s57, 0x8200\n"
    "s_add_u32 s61, s62, 0xc080\n"
    "buffer_load_dwordx4 v243, s[48:51], s61 offen nt lds\n"
    "v_exp_f32_e32 v186, v186\n"
    "ds_read_b64_tr_b16 v[210:211], v161 offset:8192\n"
    "v_mfma_f32_16x16x32_bf16 v[148:151], v[212:215], v[20:23], v[148:151]\n"
    "v_add_f32_e32 v124, v184, v124\n"
    "v_exp_f32_e32 v187, v187\n"
    "ds_read_b64_tr_b16 v[212:213], v162 offset:8192\n"
    "v_mfma_f32_16x16x32_bf16 v[152:155], v[216:219], v[16:19], v[152:155]\n"
    "s_setprio 0\n"
    "v_add_f32_e32 v124, v185, v124\n"
    "ds_read_b64_tr_b16 v[214:215], v163 offset:8192\n"
    "v_mfma_f32_16x16x32_bf16 v[156:159], v[216:219], v[20:23], v[156:159]\n"
    "v_cvt_pk_bf16_f32 v114, v184, v185\n"
    "ds_read_b64_tr_b16 v[216:217], v164 offset:8192\n"
    "s_waitcnt lgkmcnt(2)\n"
    "v_mfma_f32_16x16x32_bf16 v[128:131], v[220:223], v[16:19], v[128:131]\n"
    "s_add_u32 m0, s57, 0xa200\n"
    "s_add_u32 s61, s62, 0x12080\n"
    "buffer_load_dwordx4 v243, s[48:51], s61 offen nt lds\n"
    "v_exp_f32_e32 v188, v188\n"
    "ds_read_b64_tr_b16 v[218:219], v165 offset:8192\n"
    "v_mfma_f32_16x16x32_bf16 v[132:135], v[220:223], v[20:23], v[132:135]\n"
    "v_add_f32_e32 v124, v186, v124\n"
    "v_exp_f32_e32 v189, v189\n"
    "v_mfma_f32_16x16x32_bf16 v[232:235], v[220:223], v[224:227], v[232:235]\n"
    "v_add_f32_e32 v124, v187, v124\n"
    "ds_read_b64_tr_b16 v[220:221], v166 offset:8192\n"
    "ds_read_b64_tr_b16 v[222:223], v167 offset:8192\n"
    "v_mfma_f32_16x16x32_bf16 v[136:139], v[208:211], v[24:27], v[136:139]\n"
    "v_cvt_pk_bf16_f32 v115, v186, v187\n"
    "v_exp_f32_e32 v190, v190\n"
    "ds_read_b128 v[228:231], v242 offset:128\n"
    "v_mfma_f32_16x16x32_bf16 v[140:143], v[208:211], v[28:31], v[140:143]\n"
    "s_waitcnt lgkmcnt(1)\n"
    "v_mfma_f32_16x16x32_bf16 v[144:147], v[212:215], v[24:27], v[144:147]\n"
    "v_add_f32_e32 v125, v188, v125\n"
    "v_exp_f32_e32 v191, v191\n"
    "v_mfma_f32_16x16x32_bf16 v[148:151], v[212:215], v[28:31], v[148:151]\n"
    "v_add_f32_e32 v125, v189, v125\n"
    "s_waitcnt vmcnt(11) lgkmcnt(0)\n"
    "s_barrier\n"
    "s_setprio 3\n"
    "ds_read_b64_tr_b16 v[208:209], v250 offset:16384\n"
    "v_mfma_f32_16x16x32_bf16 v[152:155], v[216:219], v[24:27], v[152:155]\n"
    "v_cvt_pk_bf16_f32 v118, v188, v189\n"
    "v_add_f32_e32 v125, v190, v125\n"
    "ds_read_b64_tr_b16 v[210:211], v251 offset:16384\n"
    "ds_read_b128 v[224:227], v240 offset:256\n"
    "ds_read_b64_tr_b16 v[212:213], v244 offset:16384\n"
    "ds_read_b64_tr_b16 v[214:215], v245 offset:16384\n"
    "v_mfma_f32_16x16x32_bf16 v[156:159], v[216:219], v[28:31], v[156:159]\n"
    "v_add_f32_e32 v125, v191, v125\n"
    "ds_read_b64_tr_b16 v[216:217], v246 offset:16384\n"
    "v_mfma_f32_16x16x32_bf16 v[128:131], v[220:223], v[24:27], v[128:131]\n"
    "v_cvt_pk_bf16_f32 v119, v190, v191\n"
    "v_add_f32_e32 v176, v124, v125\n"
    "ds_read_b64_tr_b16 v[218:219], v247 offset:16384\n"
    "v_mfma_f32_16x16x32_bf16 v[132:135], v[220:223], v[28:31], v[132:135]\n"
    "v_cmp_nge_f32_e32 vcc, s38, v176\n"
    "s_cbranch_vccnz .Lrare_a14\n"
    ".Lback_a14:\n"
    "v_add_f32_e32 v252, v252, v124\n"
    "v_mfma_f32_16x16x32_bf16 v[232:235], v[220:223], v[228:231], v[232:235]\n"
    "v_add_f32_e32 v253, v253, v125\n"
    "ds_read_b64_tr_b16 v[220:221], v248 offset:16384\n"
    "s_waitcnt lgkmcnt(1)\n"
    "v_mfma_f32_16x16x32_bf16 v[128:131], v[208:211], v[32:35], v[128:131]\n"
    "ds_read_b128 v[236:239], v120 offset:512\n"
    "s_waitcnt lgkmcnt(0)\n"
    "v_add_f32_e32 v236, v236, v237\n"
    "ds_read_b64_tr_b16 v[222:223], v249 offset:16384\n"
    "v_mfma_f32_16x16x32_bf16 v[132:135], v[208:211], v[36:39], v[132:135]\n"
    "v_add_f32_e32 v238, v238, v239\n"
    "v_mfma_f32_16x16x32_bf16 v[232:235], v[208:211], v[224:227], v[232:235]\n"
    "v_add_f32_e32 v236, v236, v238\n"
    "ds_read_b64_tr_b16 v[208:209], v166 offset:16384\n"
    "ds_read_b64_tr_b16 v[210:211], v167 offset:16384\n"
    "v_mfma_f32_16x16x32_bf16 v[136:139], v[212:215], v[32:35], v[136:139]\n"
    "s_add_u32 m0, s57, 0x18000\n"
    "s_add_u32 s61, s62, 0x18080\n"
    "buffer_load_dwordx4 v243, s[48:51], s61 offen nt lds\n"
    "v_rcp_f32_e32 v254, v236\n"
    "ds_read_b128 v[228:231], v242 offset:256\n"
    "v_mfma_f32_16x16x32_bf16 v[140:143], v[212:215], v[36:39], v[140:143]\n"
    "ds_read_b64_tr_b16 v[212:213], v160 offset:16384\n"
    "v_mfma_f32_16x16x32_bf16 v[144:147], v[216:219], v[32:35], v[144:147]\n"
    "v_exp_f32_e32 v192, v192\n"
    "ds_read_b64_tr_b16 v[214:215], v161 offset:16384\n"
    "v_mfma_f32_16x16x32_bf16 v[148:151], v[216:219], v[36:39], v[148:151]\n"
    "ds_read_b64_tr_b16 v[216:217], v162 offset:16384\n"
    "s_waitcnt lgkmcnt(2)\n"
    "v_mfma_f32_16x16x32_bf16 v[152:155], v[220:223], v[32:35], v[152:155]\n"
    "s_setprio 2\n"
    "v_exp_f32_e32 v193, v193\n"
    "ds_read_b64_tr_b16 v[218:219], v163 offset:16384\n"
    "v_mfma_f32_16x16x32_bf16 v[156:159], v[220:223], v[36:39], v[156:159]\n"
    "ds_read_b64_tr_b16 v[220:221], v164 offset:16384\n"
    "v_mfma_f32_16x16x32_bf16 v[128:131], v[208:211], v[40:43], v[128:131]\n"
    "v_exp_f32_e32 v194, v194\n"
    "ds_read_b64_tr_b16 v[222:223], v165 offset:16384\n"
    "v_mfma_f32_16x16x32_bf16 v[132:135], v[208:211], v[44:47], v[132:135]\n"
    "v_exp_f32_e32 v195, v195\n"
    "v_mfma_f32_16x16x32_bf16 v[232:235], v[208:211], v[228:231], v[232:235]\n"
    "ds_read_b64_tr_b16 v[208:209], v250 offset:24576\n"
    "ds_read_b64_tr_b16 v[210:211], v251 offset:24576\n"
    "s_waitcnt lgkmcnt(2)\n"
    "v_mfma_f32_16x16x32_bf16 v[136:139], v[212:215], v[40:43], v[136:139]\n"
    "v_add_f32_e32 v168, v192, v193\n"
    "ds_read_b128 v[224:227], v240 offset:384\n"
    "v_mfma_f32_16x16x32_bf16 v[140:143], v[212:215], v[44:47], v[140:143]\n"
    "v_cvt_pk_bf16_f32 v120, v192, v193\n"
    "ds_read_b64_tr_b16 v[212:213], v244 offset:24576\n"
    "v_mfma_f32_16x16x32_bf16 v[144:147], v[216:219], v[40:43], v[144:147]\n"
    "v_exp_f32_e32 v196, v196\n"
    "ds_read_b64_tr_b16 v[214:215], v245 offset:24576\n"
    "v_mfma_f32_16x16x32_bf16 v[148:151], v[216:219], v[44:47], v[148:151]\n"
    "v_add_f32_e32 v168, v194, v168\n"
    "ds_read_b64_tr_b16 v[216:217], v246 offset:24576\n"
    "v_mfma_f32_16x16x32_bf16 v[152:155], v[220:223], v[40:43], v[152:155]\n"
    "s_setprio 1\n"
    "s_add_u32 m0, s57, 0x1a000\n"
    "s_add_u32 s61, s62, 0x1e080\n"
    "buffer_load_dwordx4 v243, s[48:51], s61 offen nt lds\n"
    "v_exp_f32_e32 v197, v197\n"
    "ds_read_b64_tr_b16 v[218:219], v247 offset:24576\n"
    "v_mfma_f32_16x16x32_bf16 v[156:159], v[220:223], v[44:47], v[156:159]\n"
    "v_add_f32_e32 v168, v195, v168\n"
    "ds_read_b64_tr_b16 v[220:221], v248 offset:24576\n"
    "s_waitcnt lgkmcnt(2)\n"
    "v_mfma_f32_16x16x32_bf16 v[128:131], v[208:211], v[48:51], v[128:131]\n"
    "v_cvt_pk_bf16_f32 v121, v194, v195\n"
    "ds_read_b64_tr_b16 v[222:223], v249 offset:24576\n"
    "v_mfma_f32_16x16x32_bf16 v[132:135], v[208:211], v[52:55], v[132:135]\n"
    "v_exp_f32_e32 v198, v198\n"
    "v_mfma_f32_16x16x32_bf16 v[232:235], v[208:211], v[224:227], v[232:235]\n"
    "ds_read_b64_tr_b16 v[208:209], v166 offset:24576\n"
    "ds_read_b64_tr_b16 v[210:211], v167 offset:24576\n"
    "v_mfma_f32_16x16x32_bf16 v[136:139], v[212:215], v[48:51], v[136:139]\n"
    "v_exp_f32_e32 v199, v199\n"
    "ds_read_b128 v[228:231], v242 offset:384\n"
    "v_mfma_f32_16x16x32_bf16 v[140:143], v[212:215], v[52:55], v[140:143]\n"
    "ds_read_b64_tr_b16 v[212:213], v160 offset:24576\n"
    "s_waitcnt lgkmcnt(2)\n"
    "v_mfma_f32_16x16x32_bf16 v[144:147], v[216:219], v[48:51], v[144:147]\n"
    "s_setprio 0\n"
    "v_add_f32_e32 v169, v196, v197\n"
    "ds_read_b64_tr_b16 v[214:215], v161 offset:24576\n"
    "v_mfma_f32_16x16x32_bf16 v[148:151], v[216:219], v[52:55], v[148:151]\n"
    "v_cvt_pk_bf16_f32 v124, v196, v197\n"
    "ds_read_b64_tr_b16 v[216:217], v162 offset:24576\n"
    "v_mfma_f32_16x16x32_bf16 v[152:155], v[220:223], v[48:51], v[152:155]\n"
    "v_exp_f32_e32 v200, v200\n"
    "ds_read_b64_tr_b16 v[218:219], v163 offset:24576\n"
    "v_mfma_f32_16x16x32_bf16 v[156:159], v[220:223], v[52:55], v[156:159]\n"
    "v_add_f32_e32 v169, v198, v169\n"
    "ds_read_b64_tr_b16 v[220:221], v164 offset:24576\n"
    "v_mfma_f32_16x16x32_bf16 v[128:131], v[208:211], v[56:59], v[128:131]\n"
    "v_exp_f32_e32 v201, v201\n"
    "ds_read_b64_tr_b16 v[222:223], v165 offset:24576\n"
    "v_mfma_f32_16x16x32_bf16 v[132:135], v[208:211], v[60:63], v[132:135]\n"
    "v_add_f32_e32 v169, v199, v169\n"
    "s_waitcnt lgkmcnt(1)\n"
    "v_mfma_f32_16x16x32_bf16 v[232:235], v[208:211], v[228:231], v[232:235]\n"
    "v_cvt_pk_bf16_f32 v125, v198, v199\n"
    "v_mfma_f32_16x16x32_bf16 v[136:139], v[212:215], v[56:59], v[136:139]\n"
    "v_exp_f32_e32 v202, v202\n"
    "v_mfma_f32_16x16x32_bf16 v[140:143], v[212:215], v[60:63], v[140:143]\n"
    "s_waitcnt vmcnt(8) lgkmcnt(0)\n"
    "s_barrier\n"
    "s_setprio 3\n"
    "ds_read_b64_tr_b16 v[208:209], v250 offset:32768\n"
    "v_mfma_f32_16x16x32_bf16 v[144:147], v[216:219], v[56:59], v[144:147]\n"
    "v_add_f32_e32 v168, v200, v168\n"
    "v_exp_f32_e32 v203, v203\n"
    "ds_read_b64_tr_b16 v[210:211], v251 offset:32768\n"
    "ds_read_b128 v[224:227], v240 offset:512\n"
    "ds_read_b64_tr_b16 v[212:213], v244 offset:32768\n"
    "ds_read_b64_tr_b16 v[214:215], v245 offset:32768\n"
    "v_mfma_f32_16x16x32_bf16 v[148:151], v[216:219], v[60:63], v[148:151]\n"
    "ds_read_b64_tr_b16 v[216:217], v246 offset:32768\n"
    "v_mfma_f32_16x16x32_bf16 v[152:155], v[220:223], v[56:59], v[152:155]\n"
    "v_add_f32_e32 v168, v201, v168\n"
    "ds_read_b64_tr_b16 v[218:219], v247 offset:32768\n"
    "v_mfma_f32_16x16x32_bf16 v[156:159], v[220:223], v[60:63], v[156:159]\n"
    "v_cvt_pk_bf16_f32 v122, v200, v201\n"
    "ds_read_b64_tr_b16 v[220:221], v248 offset:32768\n"
    "s_waitcnt lgkmcnt(2)\n"
    "v_mfma_f32_16x16x32_bf16 v[128:131], v[208:211], v[64:67], v[128:131]\n"
    "v_exp_f32_e32 v204, v204\n"
    "ds_read_b64_tr_b16 v[222:223], v249 offset:32768\n"
    "v_mfma_f32_16x16x32_bf16 v[132:135], v[208:211], v[68:71], v[132:135]\n"
    "v_mfma_f32_16x16x32_bf16 v[232:235], v[208:211], v[224:227], v[232:235]\n"
    "v_add_f32_e32 v168, v202, v168\n"
    "ds_read_b64_tr_b16 v[208:209], v166 offset:32768\n"
    "ds_read_b64_tr_b16 v[210:211], v167 offset:32768\n"
    "v_mfma_f32_16x16x32_bf16 v[136:139], v[212:215], v[64:67], v[136:139]\n"
    "s_add_u32 m0, s57, 0x1c000\n"
    "s_add_u32 s61, s62, 0x24080\n"
    "buffer_load_dwordx4 v243, s[48:51], s61 offen nt lds\n"
    "v_exp_f32_e32 v205, v205\n"
    "ds_read_b128 v[228:231], v242 offset:512\n"
    "v_mfma_f32_16x16x32_bf16 v[140:143], v[212:215], v[68:71], v[140:143]\n"
    "v_add_f32_e32 v168, v203, v168\n"
    "ds_read_b64_tr_b16 v[212:213], v160 offset:32768\n"
    "s_waitcnt lgkmcnt(2)\n"
    "v_mfma_f32_16x16x32_bf16 v[144:147], v[216:219], v[64:67], v[144:147]\n"
    "v_cvt_pk_bf16_f32 v123, v202, v203\n"
    "v_exp_f32_e32 v206, v206\n"
    "ds_read_b64_tr_b16 v[214:215], v161 offset:32768\n"
    "v_mfma_f32_16x16x32_bf16 v[148:151], v[216:219], v[68:71], v[148:151]\n"
    "ds_read_b64_tr_b16 v[216:217], v162 offset:32768\n"
    "v_mfma_f32_16x16x32_bf16 v[152:155], v[220:223], v[64:67], v[152:155]\n"
    "s_setprio 2\n"
    "v_add_f32_e32 v169, v204, v169\n"
    "ds_read_b64_tr_b16 v[218:219], v163 offset:32768\n"
    "v_mfma_f32_16x16x32_bf16 v[156:159], v[220:223], v[68:71], v[156:159]\n"
    "ds_read_b64_tr_b16 v[220:221], v164 offset:32768\n"
    "v_mfma_f32_16x16x32_bf16 v[128:131], v[208:211], v[72:75], v[128:131]\n"
    "v_exp_f32_e32 v207, v207\n"
    "ds_read_b64_tr_b16 v[222:223], v165 offset:32768\n"
    "v_mfma_f32_16x16x32_bf16 v[132:135], v[208:211], v[76:79], v[132:135]\n"
    "v_add_f32_e32 v169, v205, v169\n"
    "s_waitcnt lgkmcnt(1)\n"
    "v_mfma_f32_16x16x32_bf16 v[232:235], v[208:211], v[228:231], v[232:235]\n"
    "v_cvt_pk_bf16_f32 v126, v204, v205\n"
    "ds_read_b64_tr_b16 v[208:209], v250 offset:40960\n"
    "ds_read_b64_tr_b16 v[210:211], v251 offset:40960\n"
    "v_mfma_f32_16x16x32_bf16 v[136:139], v[212:215], v[72:75], v[136:139]\n"
    "v_add_f32_e32 v169, v206, v169\n"
    "ds_read_b128 v[224:227], v240 offset:640\n"
    "v_mfma_f32_16x16x32_bf16 v[140:143], v[212:215], v[76:79], v[140:143]\n"
    "v_add_f32_e32 v169, v207, v169\n"
    "ds_read_b64_tr_b16 v[212:213], v244 offset:40960\n"
    "v_mfma_f32_16x16x32_bf16 v[144:147], v[216:219], v[72:75], v[144:147]\n"
    "v_cvt_pk_bf16_f32 v127, v206, v207\n"
    "v_add_f32_e32 v192, v168, v169\n"
    "ds_read_b64_tr_b16 v[214:215], v245 offset:40960\n"
    "v_mfma_f32_16x16x32_bf16 v[148:151], v[216:219], v[76:79], v[148:151]\n"
    "v_cmp_nge_f32_e32 vcc, s38, v192\n"
    "ds_read_b64_tr_b16 v[216:217], v246 offset:40960\n"
    "s_waitcnt lgkmcnt(2)\n"
    "v_mfma_f32_16x16x32_bf16 v[152:155], v[220:223], v[72:75], v[152:155]\n"
    "s_setprio 1\n"
    "s_add_u32 m0, s57, 0x1e000\n"
    "s_add_u32 s61, s62, 0x2a080\n"
    "buffer_load_dwordx4 v243, s[48:51], s61 offen nt lds\n"
    "ds_read_b64_tr_b16 v[218:219], v247 offset:40960\n"
    "v_mfma_f32_16x16x32_bf16 v[156:159], v[220:223], v[76:79], v[156:159]\n"
    "s_cbranch_vccnz .Lrare_a15\n"
    ".Lback_a15:\n"
    "v_add_f32_e32 v252, v252, v168\n"
    "ds_read_b64_tr_b16 v[220:221], v248 offset:40960\n"
    "v_mfma_f32_16x16x32_bf16 v[128:131], v[208:211], v[80:83], v[128:131]\n"
    "v_add_f32_e32 v253, v253, v169\n"
    "v_mov_b32_e32 v168, v252\n"
    "ds_read_b64_tr_b16 v[222:223], v249 offset:40960\n"
    "v_mfma_f32_16x16x32_bf16 v[132:135], v[208:211], v[84:87], v[132:135]\n"
    "v_permlane16_swap_b32_e32 v252, v168\n"
    "v_mfma_f32_16x16x32_bf16 v[232:235], v[208:211], v[224:227], v[232:235]\n"
    "v_add_f32_e32 v252, v252, v168\n"
    "ds_read_b64_tr_b16 v[208:209], v166 offset:40960\n"
    "ds_read_b64_tr_b16 v[210:211], v167 offset:40960\n"
    "s_waitcnt lgkmcnt(2)\n"
    "v_mfma_f32_16x16x32_bf16 v[136:139], v[212:215], v[80:83], v[136:139]\n"
    "v_mov_b32_e32 v168, v252\n"
    "ds_read_b128 v[228:231], v242 offset:640\n"
    "v_mfma_f32_16x16x32_bf16 v[140:143], v[212:215], v[84:87], v[140:143]\n"
    "v_permlane32_swap_b32_e32 v252, v168\n"
    "ds_read_b64_tr_b16 v[212:213], v160 offset:40960\n"
    "v_mfma_f32_16x16x32_bf16 v[144:147], v[216:219], v[80:83], v[144:147]\n"
    "s_setprio 0\n"
    "v_add_f32_e32 v252, v252, v168\n"
    "v_rcp_f32_e32 v252, v252\n"
    "ds_read_b64_tr_b16 v[214:215], v161 offset:40960\n"
    "v_mfma_f32_16x16x32_bf16 v[148:151], v[216:219], v[84:87], v[148:151]\n"
    "ds_read_b64_tr_b16 v[216:217], v162 offset:40960\n"
    "v_mfma_f32_16x16x32_bf16 v[152:155], v[220:223], v[80:83], v[152:155]\n"
    "v_mov_b32_e32 v169, v253\n"
    "ds_read_b64_tr_b16 v[218:219], v163 offset:40960\n"
    "v_mfma_f32_16x16x32_bf16 v[156:159], v[220:223], v[84:87], v[156:159]\n"
    "v_permlane16_swap_b32_e32 v253, v169\n"
    "ds_read_b64_tr_b16 v[220:221], v164 offset:40960\n"
    "s_waitcnt lgkmcnt(2)\n"
    "v_mfma_f32_16x16x32_bf16 v[128:131], v[208:211], v[88:91], v[128:131]\n"
    "v_add_f32_e32 v253, v253, v169\n"
    "v_mov_b32_e32 v169, v253\n"
    "ds_read_b64_tr_b16 v[222:223], v165 offset:40960\n"
    "v_mfma_f32_16x16x32_bf16 v[132:135], v[208:211], v[92:95], v[132:135]\n"
    "v_permlane32_swap_b32_e32 v253, v169\n"
    "v_mfma_f32_16x16x32_bf16 v[232:235], v[208:211], v[228:231], v[232:235]\n"
    "v_add_f32_e32 v253, v253, v169\n"
    "v_mfma_f32_16x16x32_bf16 v[136:139], v[212:215], v[88:91], v[136:139]\n"
    "v_rcp_f32_e32 v253, v253\n"
    "v_mfma_f32_16x16x32_bf16 v[140:143], v[212:215], v[92:95], v[140:143]\n"
    "s_waitcnt vmcnt(8) lgkmcnt(0)\n"
    "s_barrier\n"
    "s_setprio 3\n"
    "ds_read_b64_tr_b16 v[208:209], v250 offset:49152\n"
    "v_mfma_f32_16x16x32_bf16 v[144:147], v[216:219], v[88:91], v[144:147]\n"
    "ds_read_b64_tr_b16 v[210:211], v251 offset:49152\n"
    "ds_read_b128 v[224:227], v240 offset:768\n"
    "ds_read_b64_tr_b16 v[212:213], v244 offset:49152\n"
    "ds_read_b64_tr_b16 v[214:215], v245 offset:49152\n"
    "v_mfma_f32_16x16x32_bf16 v[148:151], v[216:219], v[92:95], v[148:151]\n"
    "ds_read_b64_tr_b16 v[216:217], v246 offset:49152\n"
    "v_mfma_f32_16x16x32_bf16 v[152:155], v[220:223], v[88:91], v[152:155]\n"
    "ds_read_b64_tr_b16 v[218:219], v247 offset:49152\n"
    "v_mfma_f32_16x16x32_bf16 v[156:159], v[220:223], v[92:95], v[156:159]\n"
    "ds_read_b64_tr_b16 v[220:221], v248 offset:49152\n"
    "s_waitcnt lgkmcnt(2)\n"
    "v_mfma_f32_16x16x32_bf16 v[128:131], v[208:211], v[96:99], v[128:131]\n"
    "ds_read_b64_tr_b16 v[222:223], v249 offset:49152\n"
    "v_mfma_f32_16x16x32_bf16 v[132:135], v[208:211], v[100:103], v[132:135]\n"
    "v_mfma_f32_16x16x32_bf16 v[232:235], v[208:211], v[224:227], v[232:235]\n"
    "ds_read_b64_tr_b16 v[208:209], v166 offset:49152\n"
    "ds_read_b64_tr_b16 v[210:211], v167 offset:49152\n"
    "ds_read_b128 v[228:231], v242 offset:768\n"
    "v_mfma_f32_16x16x32_bf16 v[136:139], v[212:215], v[96:99], v[136:139]\n"
    "s_add_u32 m0, s57, 0x20000\n"
    "s_add_u32 s61, s62, 0x100\n"
    "buffer_load_dwordx4 v243, s[48:51], s61 offen nt lds\n"
    "ds_read_b64_tr_b16 v[168:169], v160 offset:49152\n"
    "ds_read_b64_tr_b16 v[170:171], v161 offset:49152\n"
    "v_mfma_f32_16x16x32_bf16 v[140:143], v[212:215], v[100:103], v[140:143]\n"
    "ds_read_b64_tr_b16 v[172:173], v162 offset:49152\n"
    "s_waitcnt lgkmcnt(3)\n"
    "v_mfma_f32_16x16x32_bf16 v[144:147], v[216:219], v[96:99], v[144:147]\n"
    "ds_read_b64_tr_b16 v[174:175], v163 offset:49152\n"
    "v_mfma_f32_16x16x32_bf16 v[148:151], v[216:219], v[100:103], v[148:151]\n"
    "ds_read_b64_tr_b16 v[212:213], v164 offset:49152\n"
    "v_mfma_f32_16x16x32_bf16 v[152:155], v[220:223], v[96:99], v[152:155]\n"
    "s_setprio 2\n"
    "ds_read_b64_tr_b16 v[214:215], v165 offset:49152\n"
    "v_mfma_f32_16x16x32_bf16 v[156:159], v[220:223], v[100:103], v[156:159]\n"
    "ds_read_b64_tr_b16 v[216:217], v250 offset:57344\n"
    "v_mfma_f32_16x16x32_bf16 v[128:131], v[208:211], v[104:107], v[128:131]\n"
    "ds_read_b64_tr_b16 v[218:219], v251 offset:57344\n"
    "v_mfma_f32_16x16x32_bf16 v[132:135], v[208:211], v[108:111], v[132:135]\n"
    "ds_read_b128 v[224:227], v240 offset:896\n"
    "v_mfma_f32_16x16x32_bf16 v[232:235], v[208:211], v[228:231], v[232:235]\n"
    "ds_read_b64_tr_b16 v[220:221], v244 offset:57344\n"
    "s_waitcnt lgkmcnt(2)\n"
    "v_mfma_f32_16x16x32_bf16 v[136:139], v[168:171], v[104:107], v[136:139]\n"
    "ds_read_b64_tr_b16 v[222:223], v245 offset:57344\n"
    "v_mfma_f32_16x16x32_bf16 v[140:143], v[168:171], v[108:111], v[140:143]\n"
    "ds_read_b64_tr_b16 v[208:209], v246 offset:57344\n"
    "v_mfma_f32_16x16x32_bf16 v[144:147], v[172:175], v[104:107], v[144:147]\n"
    "ds_read_b64_tr_b16 v[210:211], v247 offset:57344\n"
    "v_mfma_f32_16x16x32_bf16 v[148:151], v[172:175], v[108:111], v[148:151]\n"
    "ds_read_b64_tr_b16 v[168:169], v248 offset:57344\n"
    "v_mfma_f32_16x16x32_bf16 v[152:155], v[212:215], v[104:107], v[152:155]\n"
    "s_setprio 1\n"
    "s_add_u32 m0, s57, 0x22000\n"
    "s_add_u32 s61, s62, 0x6100\n"
    "ds_read_b64_tr_b16 v[170:171], v249 offset:57344\n"
    "v_mfma_f32_16x16x32_bf16 v[156:159], v[212:215], v[108:111], v[156:159]\n"
    "buffer_load_dwordx4 v243, s[48:51], s61 offen nt lds\n"
    "ds_read_b64_tr_b16 v[172:173], v166 offset:57344\n"
    "v_mfma_f32_16x16x32_bf16 v[128:131], v[216:219], v[112:115], v[128:131]\n"
    "v_add_u32_e32 v250, 0xfffec200, v250\n"
    "ds_read_b64_tr_b16 v[174:175], v167 offset:57344\n"
    "v_mfma_f32_16x16x32_bf16 v[132:135], v[216:219], v[116:119], v[132:135]\n"
    "v_add_u32_e32 v251, 0xfffec200, v251\n"
    "ds_read_b128 v[228:231], v242 offset:896\n"
    "s_waitcnt lgkmcnt(2)\n"
    "v_mfma_f32_16x16x32_bf16 v[232:235], v[216:219], v[224:227], v[232:235]\n"
    "ds_read_b64_tr_b16 v[212:213], v160 offset:57344\n"
    "v_mfma_f32_16x16x32_bf16 v[136:139], v[220:223], v[112:115], v[136:139]\n"
    "v_add_u32_e32 v244, 0xfffec200, v244\n"
    "ds_read_b64_tr_b16 v[214:215], v161 offset:57344\n"
    "v_mfma_f32_16x16x32_bf16 v[140:143], v[220:223], v[116:119], v[140:143]\n"
    "v_add_u32_e32 v245, 0xfffec200, v245\n"
    "ds_read_b64_tr_b16 v[216:217], v162 offset:57344\n"
    "v_mfma_f32_16x16x32_bf16 v[144:147], v[208:211], v[112:115], v[144:147]\n"
    "s_setprio 0\n"
    "v_add_u32_e32 v246, 0xfffec200, v246\n"
    "ds_read_b64_tr_b16 v[218:219], v163 offset:57344\n"
    "v_mfma_f32_16x16x32_bf16 v[148:151], v[208:211], v[116:119], v[148:151]\n"
    "v_add_u32_e32 v247, 0xfffec200, v247\n"
    "ds_read_b64_tr_b16 v[220:221], v164 offset:57344\n"
    "v_mfma_f32_16x16x32_bf16 v[152:155], v[168:171], v[112:115], v[152:155]\n"
    "v_add_u32_e32 v248, 0xfffec200, v248\n"
    "ds_read_b64_tr_b16 v[222:223], v165 offset:57344\n"
    "v_mfma_f32_16x16x32_bf16 v[156:159], v[168:171], v[116:119], v[156:159]\n"
    "v_add_u32_e32 v249, 0xfffec200, v249\n"
    "s_waitcnt lgkmcnt(1)\n"
    "v_mfma_f32_16x16x32_bf16 v[128:131], v[172:175], v[120:123], v[128:131]\n"
    "v_add_u32_e32 v166, 0xfffec200, v166\n"
    "v_mfma_f32_16x16x32_bf16 v[132:135], v[172:175], v[124:127], v[132:135]\n"
    "v_add_u32_e32 v167, 0xfffec200, v167\n"
    "v_mfma_f32_16x16x32_bf16 v[232:235], v[172:175], v[228:231], v[232:235]\n"
    "v_mfma_f32_16x16x32_bf16 v[136:139], v[212:215], v[120:123], v[136:139]\n"
    "v_add_u32_e32 v160, 0xfffec200, v160\n"
    "v_mfma_f32_16x16x32_bf16 v[140:143], v[212:215], v[124:127], v[140:143]\n"
    "v_add_u32_e32 v161, 0xfffec200, v161\n"
    "s_waitcnt vmcnt(8) lgkmcnt(0)\n"
    "s_barrier\n"
    "s_setprio 3\n"
    "ds_read_b64_tr_b16 v[208:209], v250\n"
    "ds_read_b64_tr_b16 v[210:211], v251\n"
    "v_mfma_f32_16x16x32_bf16 v[144:147], v[216:219], v[120:123], v[144:147]\n"
    "v_add_u32_e32 v162, 0xfffec200, v162\n"
    "ds_read_b128 v[224:227], v240\n"
    "ds_read_b64_tr_b16 v[168:169], v244\n"
    "ds_read_b64_tr_b16 v[170:171], v245\n"
    "ds_read_b64_tr_b16 v[172:173], v246\n"
    "ds_read_b64_tr_b16 v[174:175], v247\n"
    "v_mfma_f32_16x16x32_bf16 v[148:151], v[216:219], v[124:127], v[148:151]\n"
    "v_add_u32_e32 v163, 0xfffec200, v163\n"
    "ds_read_b64_tr_b16 v[212:213], v248\n"
    "v_mfma_f32_16x16x32_bf16 v[152:155], v[220:223], v[120:123], v[152:155]\n"
    "v_add_u32_e32 v164, 0xfffec200, v164\n"
    "ds_read_b64_tr_b16 v[214:215], v249\n"
    "v_mfma_f32_16x16x32_bf16 v[156:159], v[220:223], v[124:127], v[156:159]\n"
    "v_add_u32_e32 v165, 0xfffec200, v165\n"
    "ds_read_b64_tr_b16 v[216:217], v166\n"
    "s_waitcnt lgkmcnt(2)\n"
    "v_mfma_f32_16x16x32_bf16 v[176:179], v[208:211], v[0:3], 0\n"
    "ds_read_b64_tr_b16 v[218:219], v167\n"
    "v_mfma_f32_16x16x32_bf16 v[180:183], v[208:211], v[4:7], 0\n"
    "ds_read_b128 v[228:231], v242\n"
    "v_mfma_f32_16x16x32_bf16 v[236:239], v[208:211], v[224:227], 0\n"
    "ds_read_b64_tr_b16 v[220:221], v160\n"
    "v_mfma_f32_16x16x32_bf16 v[184:187], v[168:171], v[0:3], 0\n"
    "s_add_u32 m0, s57, 0x24000\n"
    "s_add_u32 s61, s62, 0xc100\n"
    "buffer_load_dwordx4 v243, s[48:51], s61 offen nt lds\n"
    "ds_read_b64_tr_b16 v[222:223], v161\n"
    "v_mfma_f32_16x16x32_bf16 v[188:191], v[168:171], v[4:7], 0\n"
    "ds_read_b64_tr_b16 v[208:209], v162\n"
    "v_mfma_f32_16x16x32_bf16 v[192:195], v[172:175], v[0:3], 0\n"
    "v_mul_f32_e32 v128, v128, v252\n"
    "ds_read_b64_tr_b16 v[210:211], v163\n"
    "v_mfma_f32_16x16x32_bf16 v[196:199], v[172:175], v[4:7], 0\n"
    "v_mul_f32_e32 v129, v129, v252\n"
    "ds_read_b64_tr_b16 v[168:169], v164\n"
    "s_waitcnt lgkmcnt(2)\n"
    "v_mfma_f32_16x16x32_bf16 v[200:203], v[212:215], v[0:3], 0\n"
    "s_setprio 2\n"
    "v_mul_f32_e32 v130, v130, v252\n"
    "ds_read_b64_tr_b16 v[170:171], v165\n"
    "v_mfma_f32_16x16x32_bf16 v[204:207], v[212:215], v[4:7], 0\n"
    "v_mul_f32_e32 v131, v131, v252\n"
    "ds_read_b64_tr_b16 v[172:173], v250 offset:8192\n"
    "v_mfma_f32_16x16x32_bf16 v[176:179], v[216:219], v[8:11], v[176:179]\n"
    "v_cvt_pk_bf16_f32 v128, v128, v129\n"
    "ds_read_b64_tr_b16 v[174:175], v251 offset:8192\n"
    "v_mfma_f32_16x16x32_bf16 v[180:183], v[216:219], v[12:15], v[180:183]\n"
    "v_cvt_pk_bf16_f32 v129, v130, v131\n"
    "ds_read_b128 v[224:227], v240 offset:128\n"
    "v_mfma_f32_16x16x32_bf16 v[236:239], v[216:219], v[228:231], v[236:239]\n"
    "v_mul_f32_e32 v136, v136, v252\n"
    "ds_read_b64_tr_b16 v[212:213], v244 offset:8192\n"
    "v_mfma_f32_16x16x32_bf16 v[184:187], v[220:223], v[8:11], v[184:187]\n"
    "v_mul_f32_e32 v137, v137, v252\n"
    "ds_read_b64_tr_b16 v[214:215], v245 offset:8192\n"
    "v_mfma_f32_16x16x32_bf16 v[188:191], v[220:223], v[12:15], v[188:191]\n"
    "ds_read_b64_tr_b16 v[216:217], v246 offset:8192\n"
    "s_waitcnt lgkmcnt(2)\n"
    "v_mfma_f32_16x16x32_bf16 v[192:195], v[208:211], v[8:11], v[192:195]\n"
    "v_mul_f32_e32 v138, v138, v252\n"
    "ds_read_b64_tr_b16 v[218:219], v247 offset:8192\n"
    "v_mfma_f32_16x16x32_bf16 v[196:199], v[208:211], v[12:15], v[196:199]\n"
    "v_mul_f32_e32 v139, v139, v252\n"
    "ds_read_b64_tr_b16 v[220:221], v248 offset:8192\n"
    "v_mfma_f32_16x16x32_bf16 v[200:203], v[168:171], v[8:11], v[200:203]\n"
    "s_setprio 1\n"
    "s_add_u32 m0, s57, 0x26000\n"
    "s_add_u32 s61, s62, 0x12100\n"
    "buffer_load_dwordx4 v243, s[48:51], s61 offen nt lds\n"
    "ds_read_b64_tr_b16 v[222:223], v249 offset:8192\n"
    "v_mfma_f32_16x16x32_bf16 v[204:207], v[168:171], v[12:15], v[204:207]\n"
    "v_cvt_pk_bf16_f32 v136, v136, v137\n"
    "v_cvt_pk_bf16_f32 v137, v138, v139\n"
    "ds_read_b64_tr_b16 v[208:209], v166 offset:8192\n"
    "v_mfma_f32_16x16x32_bf16 v[176:179], v[172:175], v[16:19], v[176:179]\n"
    "v_mbcnt_lo_u32_b32 v130, -1, 0\n"
    "ds_read_b64_tr_b16 v[210:211], v167 offset:8192\n"
    "v_mfma_f32_16x16x32_bf16 v[180:183], v[172:175], v[20:23], v[180:183]\n"
    "v_mbcnt_hi_u32_b32 v130, -1, v130\n"
    "ds_read_b128 v[228:231], v242 offset:128\n"
    "v_mfma_f32_16x16x32_bf16 v[236:239], v[172:175], v[224:227], v[236:239]\n"
    "v_and_b32_e32 v131, 15, v130\n"
    "ds_read_b64_tr_b16 v[168:169], v160 offset:8192\n"
    "s_waitcnt lgkmcnt(2)\n"
    "v_mfma_f32_16x16x32_bf16 v[184:187], v[212:215], v[16:19], v[184:187]\n"
    "v_lshrrev_b32_e32 v139, 4, v130\n"
    "ds_read_b64_tr_b16 v[170:171], v161 offset:8192\n"
    "v_mfma_f32_16x16x32_bf16 v[188:191], v[212:215], v[20:23], v[188:191]\n"
    "v_lshl_add_u32 v139, v139, 3, s27\n"
    "ds_read_b64_tr_b16 v[172:173], v162 offset:8192\n"
    "v_mfma_f32_16x16x32_bf16 v[192:195], v[216:219], v[16:19], v[192:195]\n"
    "s_setprio 0\n"
    "v_mad_u32_u24 v131, v131, s28, v139\n"
    "ds_read_b64_tr_b16 v[174:175], v163 offset:8192\n"
    "v_mfma_f32_16x16x32_bf16 v[196:199], v[216:219], v[20:23], v[196:199]\n"
    "v_and_b32_e32 v139, 7, v130\n"
    "ds_read_b64_tr_b16 v[212:213], v164 offset:8192\n"
    "v_mfma_f32_16x16x32_bf16 v[200:203], v[220:223], v[16:19], v[200:203]\n"
    "v_xor_b32_e32 v139, s29, v139\n"
    "ds_read_b64_tr_b16 v[214:215], v165 offset:8192\n"
    "v_mfma_f32_16x16x32_bf16 v[204:207], v[220:223], v[20:23], v[204:207]\n"
    "v_lshl_add_u32 v139, v139, 4, s27\n"
    "v_mfma_f32_16x16x32_bf16 v[176:179], v[208:211], v[24:27], v[176:179]\n"
    "v_lshrrev_b32_e32 v138, 3, v130\n"
    "v_mfma_f32_16x16x32_bf16 v[180:183], v[208:211], v[28:31], v[180:183]\n"
    "v_mad_u32_u24 v138, v138, s28, v139\n"
    "s_waitcnt lgkmcnt(0)\n"
    "v_mfma_f32_16x16x32_bf16 v[236:239], v[208:211], v[228:231], v[236:239]\n"
    "v_mfma_f32_16x16x32_bf16 v[184:187], v[168:171], v[24:27], v[184:187]\n"
    "ds_write_b64 v131, v[128:129]\n"
    "v_mfma_f32_16x16x32_bf16 v[188:191], v[168:171], v[28:31], v[188:191]\n"
    "ds_write_b64 v131, v[136:137] offset:32\n"
    "s_waitcnt vmcnt(8) lgkmcnt(0)\n"
    "s_barrier\n"
    "s_setprio 3\n"
    "ds_read_b64_tr_b16 v[216:217], v250 offset:16384\n"
    "ds_read_b64_tr_b16 v[218:219], v251 offset:16384\n"
    "v_mfma_f32_16x16x32_bf16 v[192:195], v[172:175], v[24:27], v[192:195]\n"
    "v_mul_f32_e32 v144, v144, v252\n"
    "ds_read_b128 v[224:227], v240 offset:256\n"
    "ds_read_b64_tr_b16 v[220:221], v244 offset:16384\n"
    "ds_read_b64_tr_b16 v[222:223], v245 offset:16384\n"
    "ds_read_b64_tr_b16 v[208:209], v246 offset:16384\n"
    "ds_read_b64_tr_b16 v[210:211], v247 offset:16384\n"
    "v_mfma_f32_16x16x32_bf16 v[196:199], v[172:175], v[28:31], v[196:199]\n"
    "ds_read_b64_tr_b16 v[168:169], v248 offset:16384\n"
    "v_mfma_f32_16x16x32_bf16 v[200:203], v[212:215], v[24:27], v[200:203]\n"
    "v_mul_f32_e32 v145, v145, v252\n"
    "ds_read_b64_tr_b16 v[170:171], v249 offset:16384\n"
    "v_mfma_f32_16x16x32_bf16 v[204:207], v[212:215], v[28:31], v[204:207]\n"
    "v_mul_f32_e32 v146, v146, v252\n"
    "ds_read_b64_tr_b16 v[172:173], v166 offset:16384\n"
    "s_waitcnt lgkmcnt(2)\n"
    "v_mfma_f32_16x16x32_bf16 v[176:179], v[216:219], v[32:35], v[176:179]\n"
    "v_mul_f32_e32 v147, v147, v252\n"
    "ds_read_b64_tr_b16 v[174:175], v167 offset:16384\n"
    "v_mfma_f32_16x16x32_bf16 v[180:183], v[216:219], v[36:39], v[180:183]\n"
    "v_cvt_pk_bf16_f32 v144, v144, v145\n"
    "ds_read_b128 v[228:231], v242 offset:256\n"
    "v_mfma_f32_16x16x32_bf16 v[236:239], v[216:219], v[224:227], v[236:239]\n"
    "v_cvt_pk_bf16_f32 v145, v146, v147\n"
    "ds_read_b64_tr_b16 v[212:213], v160 offset:16384\n"
    "v_mfma_f32_16x16x32_bf16 v[184:187], v[220:223], v[32:35], v[184:187]\n"
    "s_add_u32 m0, s57, 0x4200\n"
    "s_add_u32 s61, s62, 0x18100\n"
    "buffer_load_dwordx4 v243, s[48:51], s61 offen nt lds\n"
    "ds_read_b64_tr_b16 v[214:215], v161 offset:16384\n"
    "v_mfma_f32_16x16x32_bf16 v[188:191], v[220:223], v[36:39], v[188:191]\n"
    "ds_write_b64 v131, v[144:145] offset:64\n"
    "ds_read_b64_tr_b16 v[216:217], v162 offset:16384\n"
    "v_mfma_f32_16x16x32_bf16 v[192:195], v[208:211], v[32:35], v[192:195]\n"
    "v_mul_f32_e32 v152, v152, v252\n"
    "ds_read_b64_tr_b16 v[218:219], v163 offset:16384\n"
    "v_mfma_f32_16x16x32_bf16 v[196:199], v[208:211], v[36:39], v[196:199]\n"
    "v_mul_f32_e32 v153, v153, v252\n"
    "ds_read_b64_tr_b16 v[220:221], v164 offset:16384\n"
    "s_waitcnt lgkmcnt(2)\n"
    "v_mfma_f32_16x16x32_bf16 v[200:203], v[168:171], v[32:35], v[200:203]\n"
    "s_setprio 2\n"
    "v_mul_f32_e32 v154, v154, v252\n"
    "ds_read_b64_tr_b16 v[222:223], v165 offset:16384\n"
    "v_mfma_f32_16x16x32_bf16 v[204:207], v[168:171], v[36:39], v[204:207]\n"
    "v_mul_f32_e32 v155, v155, v252\n"
    "ds_read_b64_tr_b16 v[208:209], v250 offset:24576\n"
    "v_mfma_f32_16x16x32_bf16 v[176:179], v[172:175], v[40:43], v[176:179]\n"
    "v_cvt_pk_bf16_f32 v152, v152, v153\n"
    "v_cvt_pk_bf16_f32 v153, v154, v155\n"
    "ds_read_b64_tr_b16 v[210:211], v251 offset:24576\n"
    "v_mfma_f32_16x16x32_bf16 v[180:183], v[172:175], v[44:47], v[180:183]\n"
    "ds_write_b64 v131, v[152:153] offset:96\n"
    "ds_read_b128 v[224:227], v240 offset:384\n"
    "v_mfma_f32_16x16x32_bf16 v[236:239], v[172:175], v[228:231], v[236:239]\n"
    "ds_read_b64_tr_b16 v[168:169], v244 offset:24576\n"
    "v_mfma_f32_16x16x32_bf16 v[184:187], v[212:215], v[40:43], v[184:187]\n"
    "ds_read_b128 v[144:147], v138\n"
    "ds_read_b64_tr_b16 v[170:171], v245 offset:24576\n"
    "v_mfma_f32_16x16x32_bf16 v[188:191], v[212:215], v[44:47], v[188:191]\n"
    "ds_read_b128 v[152:155], v138 offset:1152\n"
    "ds_read_b64_tr_b16 v[172:173], v246 offset:24576\n"
    "s_waitcnt lgkmcnt(4)\n"
    "v_mfma_f32_16x16x32_bf16 v[192:195], v[216:219], v[40:43], v[192:195]\n"
    "v_mul_f32_e32 v132, v132, v253\n"
    "ds_read_b64_tr_b16 v[174:175], v247 offset:24576\n"
    "v_mfma_f32_16x16x32_bf16 v[196:199], v[216:219], v[44:47], v[196:199]\n"
    "ds_read_b64_tr_b16 v[212:213], v248 offset:24576\n"
    "v_mfma_f32_16x16x32_bf16 v[200:203], v[220:223], v[40:43], v[200:203]\n"
    "s_setprio 1\n"
    "s_add_u32 m0, s57, 0x6200\n"
    "s_add_u32 s61, s62, 0x1e100\n"
    "buffer_load_dwordx4 v243, s[48:51], s61 offen nt lds\n"
    "ds_read_b64_tr_b16 v[214:215], v249 offset:24576\n"
    "v_mfma_f32_16x16x32_bf16 v[204:207], v[220:223], v[44:47], v[204:207]\n"
    "v_mul_f32_e32 v133, v133, v253\n"
    "v_mul_f32_e32 v134, v134, v253\n"
    "ds_read_b64_tr_b16 v[216:217], v166 offset:24576\n"
    "v_mfma_f32_16x16x32_bf16 v[176:179], v[208:211], v[48:51], v[176:179]\n"
    "v_add_u32_e32 v250, 0x13e00, v250\n"
    "v_add_u32_e32 v251, 0x13e00, v251\n"
    "ds_read_b64_tr_b16 v[218:219], v167 offset:24576\n"
    "v_mfma_f32_16x16x32_bf16 v[180:183], v[208:211], v[52:55], v[180:183]\n"
    "v_mul_f32_e32 v135, v135, v253\n"
    "ds_read_b128 v[228:231], v242 offset:384\n"
    "v_mfma_f32_16x16x32_bf16 v[236:239], v[208:211], v[224:227], v[236:239]\n"
    "v_cvt_pk_bf16_f32 v132, v132, v133\n"
    "ds_read_b64_tr_b16 v[220:221], v160 offset:24576\n"
    "s_waitcnt lgkmcnt(2)\n"
    "v_mfma_f32_16x16x32_bf16 v[184:187], v[168:171], v[48:51], v[184:187]\n"
    "v_add_u32_e32 v244, 0x13e00, v244\n"
    "v_add_u32_e32 v245, 0x13e00, v245\n"
    "ds_read_b64_tr_b16 v[222:223], v161 offset:24576\n"
    "v_mfma_f32_16x16x32_bf16 v[188:191], v[168:171], v[52:55], v[188:191]\n"
    "v_cvt_pk_bf16_f32 v133, v134, v135\n"
    "ds_read_b64_tr_b16 v[208:209], v162 offset:24576\n"
    "v_mfma_f32_16x16x32_bf16 v[192:195], v[172:175], v[48:51], v[192:195]\n"
    "s_setprio 0\n"
    "v_add_u32_e32 v246, 0x13e00, v246\n"
    "v_add_u32_e32 v247, 0x13e00, v247\n"
    "ds_read_b64_tr_b16 v[210:211], v163 offset:24576\n"
    "v_mfma_f32_16x16x32_bf16 v[196:199], v[172:175], v[52:55], v[196:199]\n"
    "ds_write_b64 v131, v[132:133]\n"
    "ds_read_b64_tr_b16 v[168:169], v164 offset:24576\n"
    "v_mfma_f32_16x16x32_bf16 v[200:203], v[212:215], v[48:51], v[200:203]\n"
    "v_add_u32_e32 v248, 0x13e00, v248\n"
    "v_add_u32_e32 v249, 0x13e00, v249\n"
    "ds_read_b64_tr_b16 v[170:171], v165 offset:24576\n"
    "v_mfma_f32_16x16x32_bf16 v[204:207], v[212:215], v[52:55], v[204:207]\n"
    "v_mul_f32_e32 v140, v140, v253\n"
    "v_mul_f32_e32 v141, v141, v253\n"
    "v_mfma_f32_16x16x32_bf16 v[176:179], v[216:219], v[56:59], v[176:179]\n"
    "v_add_u32_e32 v166, 0x13e00, v166\n"
    "v_add_u32_e32 v167, 0x13e00, v167\n"
    "v_mfma_f32_16x16x32_bf16 v[180:183], v[216:219], v[60:63], v[180:183]\n"
    "v_mul_f32_e32 v142, v142, v253\n"
    "s_waitcnt lgkmcnt(0)\n"
    "v_mfma_f32_16x16x32_bf16 v[236:239], v[216:219], v[228:231], v[236:239]\n"
    "v_mul_f32_e32 v143, v143, v253\n"
    "v_mfma_f32_16x16x32_bf16 v[184:187], v[220:223], v[56:59], v[184:187]\n"
    "v_add_u32_e32 v160, 0x13e00, v160\n"
    "v_add_u32_e32 v161, 0x13e00, v161\n"
    "v_mfma_f32_16x16x32_bf16 v[188:191], v[220:223], v[60:63], v[188:191]\n"
    "v_cvt_pk_bf16_f32 v140, v140, v141\n"
    "s_waitcnt vmcnt(8) lgkmcnt(0)\n"
    "s_barrier\n"
    "s_setprio 3\n"
    "ds_read_b64_tr_b16 v[172:173], v250\n"
    "ds_read_b64_tr_b16 v[174:175], v251\n"
    "v_mfma_f32_16x16x32_bf16 v[192:195], v[208:211], v[56:59], v[192:195]\n"
    "v_add_u32_e32 v162, 0x13e00, v162\n"
    "v_add_u32_e32 v163, 0x13e00, v163\n"
    "ds_read_b128 v[224:227], v240 offset:512\n"
    "ds_read_b64_tr_b16 v[212:213], v244\n"
    "ds_read_b64_tr_b16 v[214:215], v245\n"
    "ds_read_b64_tr_b16 v[216:217], v246\n"
    "ds_read_b64_tr_b16 v[218:219], v247\n"
    "v_mfma_f32_16x16x32_bf16 v[196:199], v[208:211], v[60:63], v[196:199]\n"
    "v_cvt_pk_bf16_f32 v141, v142, v143\n"
    "ds_read_b64_tr_b16 v[220:221], v248\n"
    "v_mfma_f32_16x16x32_bf16 v[200:203], v[168:171], v[56:59], v[200:203]\n"
    "v_add_u32_e32 v164, 0x13e00, v164\n"
    "v_add_u32_e32 v165, 0x13e00, v165\n"
    "ds_read_b64_tr_b16 v[222:223], v249\n"
    "v_mfma_f32_16x16x32_bf16 v[204:207], v[168:171], v[60:63], v[204:207]\n"
    "ds_write_b64 v131, v[140:141] offset:32\n"
    "ds_read_b64_tr_b16 v[208:209], v166\n"
    "s_waitcnt lgkmcnt(3)\n"
    "v_mfma_f32_16x16x32_bf16 v[176:179], v[172:175], v[64:67], v[176:179]\n"
    "v_mul_f32_e32 v148, v148, v253\n"
    "ds_read_b64_tr_b16 v[210:211], v167\n"
    "v_mfma_f32_16x16x32_bf16 v[180:183], v[172:175], v[68:71], v[180:183]\n"
    "v_mul_f32_e32 v149, v149, v253\n"
    "ds_read_b128 v[228:231], v242 offset:512\n"
    "v_mfma_f32_16x16x32_bf16 v[236:239], v[172:175], v[224:227], v[236:239]\n"
    "ds_read_b64_tr_b16 v[168:169], v160\n"
    "v_mfma_f32_16x16x32_bf16 v[184:187], v[212:215], v[64:67], v[184:187]\n"
    "s_add_u32 m0, s57, 0x8200\n"
    "s_add_u32 s61, s62, 0x24100\n"
    "buffer_load_dwordx4 v243, s[48:51], s61 offen nt lds\n"
    "v_mul_f32_e32 v150, v150, v253\n"
    "ds_read_b64_tr_b16 v[170:171], v161\n"
    "v_mfma_f32_16x16x32_bf16 v[188:191], v[212:215], v[68:71], v[188:191]\n"
    "v_mul_f32_e32 v151, v151, v253\n"
    "ds_read_b64_tr_b16 v[172:173], v162\n"
    "v_mfma_f32_16x16x32_bf16 v[192:195], v[216:219], v[64:67], v[192:195]\n"
    "v_cvt_pk_bf16_f32 v148, v148, v149\n"
    "ds_read_b64_tr_b16 v[174:175], v163\n"
    "v_mfma_f32_16x16x32_bf16 v[196:199], v[216:219], v[68:71], v[196:199]\n"
    "ds_read_b64_tr_b16 v[212:213], v164\n"
    "s_waitcnt lgkmcnt(2)\n"
    "v_mfma_f32_16x16x32_bf16 v[200:203], v[220:223], v[64:67], v[200:203]\n"
    "s_setprio 2\n"
    "v_cvt_pk_bf16_f32 v149, v150, v151\n"
    "ds_read_b64_tr_b16 v[214:215], v165\n"
    "v_mfma_f32_16x16x32_bf16 v[204:207], v[220:223], v[68:71], v[204:207]\n"
    "ds_read_b64_tr_b16 v[216:217], v250 offset:8192\n"
    "v_mfma_f32_16x16x32_bf16 v[176:179], v[208:211], v[72:75], v[176:179]\n"
    "ds_write_b64 v131, v[148:149] offset:64\n"
    "ds_read_b64_tr_b16 v[218:219], v251 offset:8192\n"
    "v_mfma_f32_16x16x32_bf16 v[180:183], v[208:211], v[76:79], v[180:183]\n"
    "v_mul_f32_e32 v156, v156, v253\n"
    "ds_read_b128 v[224:227], v240 offset:640\n"
    "v_mfma_f32_16x16x32_bf16 v[236:239], v[208:211], v[228:231], v[236:239]\n"
    "ds_read_b64_tr_b16 v[220:221], v244 offset:8192\n"
    "v_mfma_f32_16x16x32_bf16 v[184:187], v[168:171], v[72:75], v[184:187]\n"
    "v_mul_f32_e32 v157, v157, v253\n"
    "ds_read_b64_tr_b16 v[222:223], v245 offset:8192\n"
    "v_mfma_f32_16x16x32_bf16 v[188:191], v[168:171], v[76:79], v[188:191]\n"
    "v_mul_f32_e32 v158, v158, v253\n"
    "ds_read_b64_tr_b16 v[208:209], v246 offset:8192\n"
    "s_waitcnt lgkmcnt(2)\n"
    "v_mfma_f32_16x16x32_bf16 v[192:195], v[172:175], v[72:75], v[192:195]\n"
    "v_mul_f32_e32 v159, v159, v253\n"
    "ds_read_b64_tr_b16 v[210:211], v247 offset:8192\n"
    "v_mfma_f32_16x16x32_bf16 v[196:199], v[172:175], v[76:79], v[196:199]\n"
    "ds_read_b64_tr_b16 v[168:169], v248 offset:8192\n"
    "v_mfma_f32_16x16x32_bf16 v[200:203], v[212:215], v[72:75], v[200:203]\n"
    "s_setprio 1\n"
    "s_add_u32 m0, s57, 0xa200\n"
    "s_add_u32 s61, s62, 0x2a100\n"
    "buffer_load_dwordx4 v243, s[48:51], s61 offen nt lds\n"
    "ds_read_b64_tr_b16 v[170:171], v249 offset:8192\n"
    "v_mfma_f32_16x16x32_bf16 v[204:207], v[212:215], v[76:79], v[204:207]\n"
    "v_cvt_pk_bf16_f32 v156, v156, v157\n"
    "ds_read_b64_tr_b16 v[172:173], v166 offset:8192\n"
    "v_mfma_f32_16x16x32_bf16 v[176:179], v[216:219], v[80:83], v[176:179]\n"
    "v_cvt_pk_bf16_f32 v157, v158, v159\n"
    "ds_read_b64_tr_b16 v[174:175], v167 offset:8192\n"
    "v_mfma_f32_16x16x32_bf16 v[180:183], v[216:219], v[84:87], v[180:183]\n"
    "ds_write_b64 v131, v[156:157] offset:96\n"
    "ds_read_b128 v[228:231], v242 offset:640\n"
    "v_mfma_f32_16x16x32_bf16 v[236:239], v[216:219], v[224:227], v[236:239]\n"
    "ds_read_b64_tr_b16 v[212:213], v160 offset:8192\n"
    "s_waitcnt lgkmcnt(3)\n"
    "v_mfma_f32_16x16x32_bf16 v[184:187], v[220:223], v[80:83], v[184:187]\n"
    "ds_read_b128 v[148:151], v138\n"
    "ds_read_b64_tr_b16 v[214:215], v161 offset:8192\n"
    "v_mfma_f32_16x16x32_bf16 v[188:191], v[220:223], v[84:87], v[188:191]\n"
    "ds_read_b128 v[156:159], v138 offset:1152\n"
    "ds_read_b64_tr_b16 v[216:217], v162 offset:8192\n"
    "v_mfma_f32_16x16x32_bf16 v[192:195], v[208:211], v[80:83], v[192:195]\n"
    "s_setprio 0\n"
    "ds_read_b64_tr_b16 v[218:219], v163 offset:8192\n"
    "v_mfma_f32_16x16x32_bf16 v[196:199], v[208:211], v[84:87], v[196:199]\n"
    "ds_read_b64_tr_b16 v[220:221], v164 offset:8192\n"
    "v_mfma_f32_16x16x32_bf16 v[200:203], v[168:171], v[80:83], v[200:203]\n"
    "ds_read_b64_tr_b16 v[222:223], v165 offset:8192\n"
    "v_mfma_f32_16x16x32_bf16 v[204:207], v[168:171], v[84:87], v[204:207]\n"
    "v_mfma_f32_16x16x32_bf16 v[176:179], v[172:175], v[88:91], v[176:179]\n"
    "v_mfma_f32_16x16x32_bf16 v[180:183], v[172:175], v[92:95], v[180:183]\n"
    "s_waitcnt lgkmcnt(0)\n"
    "v_mfma_f32_16x16x32_bf16 v[236:239], v[172:175], v[228:231], v[236:239]\n"
    "v_mfma_f32_16x16x32_bf16 v[184:187], v[212:215], v[88:91], v[184:187]\n"
    "v_mfma_f32_16x16x32_bf16 v[188:191], v[212:215], v[92:95], v[188:191]\n"
    "s_waitcnt vmcnt(8) lgkmcnt(0)\n"
    "s_barrier\n"
    "s_setprio 3\n"
    "ds_read_b64_tr_b16 v[208:209], v250 offset:16384\n"
    "ds_read_b64_tr_b16 v[210:211], v251 offset:16384\n"
    "v_mfma_f32_16x16x32_bf16 v[192:195], v[216:219], v[88:91], v[192:195]\n"
    "ds_read_b128 v[224:227], v240 offset:768\n"
    "ds_read_b64_tr_b16 v[168:169], v244 offset:16384\n"
    "ds_read_b64_tr_b16 v[170:171], v245 offset:16384\n"
    "ds_read_b64_tr_b16 v[172:173], v246 offset:16384\n"
    "ds_read_b64_tr_b16 v[174:175], v247 offset:16384\n"
    "v_mfma_f32_16x16x32_bf16 v[196:199], v[216:219], v[92:95], v[196:199]\n"
    "ds_read_b64_tr_b16 v[212:213], v248 offset:16384\n"
    "v_mfma_f32_16x16x32_bf16 v[200:203], v[220:223], v[88:91], v[200:203]\n"
    "ds_read_b64_tr_b16 v[214:215], v249 offset:16384\n"
    "v_mfma_f32_16x16x32_bf16 v[204:207], v[220:223], v[92:95], v[204:207]\n"
    "ds_read_b64_tr_b16 v[216:217], v166 offset:16384\n"
    "s_waitcnt lgkmcnt(2)\n"
    "v_mfma_f32_16x16x32_bf16 v[176:179], v[208:211], v[96:99], v[176:179]\n"
    "buffer_store_dwordx4 v[144:147], v241, s[52:55], s58 offen nt\n"
    "ds_read_b64_tr_b16 v[218:219], v167 offset:16384\n"
    "v_mfma_f32_16x16x32_bf16 v[180:183], v[208:211], v[100:103], v[180:183]\n"
    "ds_read_b128 v[228:231], v242 offset:768\n"
    "v_mfma_f32_16x16x32_bf16 v[236:239], v[208:211], v[224:227], v[236:239]\n"
    "ds_read_b64_tr_b16 v[220:221], v160 offset:16384\n"
    "v_mfma_f32_16x16x32_bf16 v[184:187], v[168:171], v[96:99], v[184:187]\n"
    "ds_read_b64_tr_b16 v[222:223], v161 offset:16384\n"
    "v_mfma_f32_16x16x32_bf16 v[188:191], v[168:171], v[100:103], v[188:191]\n"
    "ds_read_b64_tr_b16 v[208:209], v162 offset:16384\n"
    "v_mfma_f32_16x16x32_bf16 v[192:195], v[172:175], v[96:99], v[192:195]\n"
    "ds_read_b64_tr_b16 v[210:211], v163 offset:16384\n"
    "v_mfma_f32_16x16x32_bf16 v[196:199], v[172:175], v[100:103], v[196:199]\n"
    "ds_read_b64_tr_b16 v[168:169], v164 offset:16384\n"
    "s_waitcnt lgkmcnt(2)\n"
    "v_mfma_f32_16x16x32_bf16 v[200:203], v[212:215], v[96:99], v[200:203]\n"
    "s_setprio 2\n"
    "ds_read_b64_tr_b16 v[170:171], v165 offset:16384\n"
    "v_mfma_f32_16x16x32_bf16 v[204:207], v[212:215], v[100:103], v[204:207]\n"
    "ds_read_b64_tr_b16 v[172:173], v250 offset:24576\n"
    "v_mfma_f32_16x16x32_bf16 v[176:179], v[216:219], v[104:107], v[176:179]\n"
    "buffer_store_dwordx4 v[152:155], v241, s[52:55], s59 offen nt\n"
    "ds_read_b64_tr_b16 v[174:175], v251 offset:24576\n"
    "v_mfma_f32_16x16x32_bf16 v[180:183], v[216:219], v[108:111], v[180:183]\n"
    "ds_read_b128 v[224:227], v240 offset:896\n"
    "v_mfma_f32_16x16x32_bf16 v[236:239], v[216:219], v[228:231], v[236:239]\n"
    "ds_read_b64_tr_b16 v[212:213], v244 offset:24576\n"
    "v_mfma_f32_16x16x32_bf16 v[184:187], v[220:223], v[104:107], v[184:187]\n"
    "ds_read_b64_tr_b16 v[214:215], v245 offset:24576\n"
    "v_mfma_f32_16x16x32_bf16 v[188:191], v[220:223], v[108:111], v[188:191]\n"
    "ds_read_b64_tr_b16 v[216:217], v246 offset:24576\n"
    "s_waitcnt lgkmcnt(2)\n"
    "v_mfma_f32_16x16x32_bf16 v[192:195], v[208:211], v[104:107], v[192:195]\n"
    "ds_read_b64_tr_b16 v[218:219], v247 offset:24576\n"
    "v_mfma_f32_16x16x32_bf16 v[196:199], v[208:211], v[108:111], v[196:199]\n"
    "ds_read_b64_tr_b16 v[220:221], v248 offset:24576\n"
    "v_mfma_f32_16x16x32_bf16 v[200:203], v[168:171], v[104:107], v[200:203]\n"
    "s_setprio 1\n"
    "ds_read_b64_tr_b16 v[222:223], v249 offset:24576\n"
    "v_mfma_f32_16x16x32_bf16 v[204:207], v[168:171], v[108:111], v[204:207]\n"
    "ds_read_b64_tr_b16 v[208:209], v166 offset:24576\n"
    "v_mfma_f32_16x16x32_bf16 v[176:179], v[172:175], v[112:115], v[176:179]\n"
    "buffer_store_dwordx4 v[148:151], v241, s[52:55], s60 offen nt\n"
    "ds_read_b64_tr_b16 v[210:211], v167 offset:24576\n"
    "v_mfma_f32_16x16x32_bf16 v[180:183], v[172:175], v[116:119], v[180:183]\n"
    "ds_read_b128 v[228:231], v242 offset:896\n"
    "v_mfma_f32_16x16x32_bf16 v[236:239], v[172:175], v[224:227], v[236:239]\n"
    "ds_read_b64_tr_b16 v[168:169], v160 offset:24576\n"
    "s_waitcnt lgkmcnt(2)\n"
    "v_mfma_f32_16x16x32_bf16 v[184:187], v[212:215], v[112:115], v[184:187]\n"
    "ds_read_b64_tr_b16 v[170:171], v161 offset:24576\n"
    "v_mfma_f32_16x16x32_bf16 v[188:191], v[212:215], v[116:119], v[188:191]\n"
    "ds_read_b64_tr_b16 v[172:173], v162 offset:24576\n"
    "v_mfma_f32_16x16x32_bf16 v[192:195], v[216:219], v[112:115], v[192:195]\n"
    "s_setprio 0\n"
    "v_mul_f32_e32 v232, v232, v254\n"
    "ds_read_b64_tr_b16 v[174:175], v163 offset:24576\n"
    "v_mfma_f32_16x16x32_bf16 v[196:199], v[216:219], v[116:119], v[196:199]\n"
    "ds_read_b64_tr_b16 v[212:213], v164 offset:24576\n"
    "v_mfma_f32_16x16x32_bf16 v[200:203], v[220:223], v[112:115], v[200:203]\n"
    "buffer_store_dwordx4 v[156:159], v241, s[52:55], s64 offen nt\n"
    "v_mul_f32_e32 v233, v233, v254\n"
    "ds_read_b64_tr_b16 v[214:215], v165 offset:24576\n"
    "v_mfma_f32_16x16x32_bf16 v[204:207], v[220:223], v[116:119], v[204:207]\n"
    "v_mul_f32_e32 v234, v234, v254\n"
    "v_mfma_f32_16x16x32_bf16 v[176:179], v[208:211], v[120:123], v[176:179]\n"
    "v_mul_f32_e32 v235, v235, v254\n"
    "v_mfma_f32_16x16x32_bf16 v[180:183], v[208:211], v[124:127], v[180:183]\n"
    "v_cvt_pk_bf16_f32 v232, v232, v233\n"
    "s_waitcnt lgkmcnt(0)\n"
    "v_mfma_f32_16x16x32_bf16 v[236:239], v[208:211], v[228:231], v[236:239]\n"
    "v_mfma_f32_16x16x32_bf16 v[184:187], v[168:171], v[120:123], v[184:187]\n"
    "v_cvt_pk_bf16_f32 v233, v234, v235\n"
    "v_mfma_f32_16x16x32_bf16 v[188:191], v[168:171], v[124:127], v[188:191]\n"
    "s_waitcnt vmcnt(4) lgkmcnt(0)\n"
    "s_barrier\n"
    "s_setprio 3\n"
    "ds_read_b64_tr_b16 v[216:217], v250 offset:32768\n"
    "ds_read_b64_tr_b16 v[218:219], v251 offset:32768\n"
    "ds_read_b128 v[224:227], v240\n"
    "v_mfma_f32_16x16x32_bf16 v[192:195], v[172:175], v[120:123], v[192:195]\n"
    "buffer_store_dwordx2 v[232:233], v255, s[52:55], s65 offen nt\n"
    "ds_read_b64_tr_b16 v[220:221], v244 offset:32768\n"
    "ds_read_b64_tr_b16 v[222:223], v245 offset:32768\n"
    "ds_read_b64_tr_b16 v[208:209], v166 offset:32768\n"
    "ds_read_b64_tr_b16 v[210:211], v167 offset:32768\n"
    "ds_read_b128 v[228:231], v242\n"
    "v_mfma_f32_16x16x32_bf16 v[196:199], v[172:175], v[124:127], v[196:199]\n"
    "ds_read_b64_tr_b16 v[168:169], v160 offset:32768\n"
    "v_mfma_f32_16x16x32_bf16 v[200:203], v[212:215], v[120:123], v[200:203]\n"
    "ds_read_b64_tr_b16 v[170:171], v161 offset:32768\n"
    "v_mfma_f32_16x16x32_bf16 v[204:207], v[212:215], v[124:127], v[204:207]\n"
    "s_waitcnt lgkmcnt(1)\n"
    "v_mfma_f32_16x16x32_bf16 v[128:131], v[216:219], v[0:3], 0\n"
    "v_mfma_f32_16x16x32_bf16 v[132:135], v[216:219], v[4:7], 0\n"
    "v_mfma_f32_16x16x32_bf16 v[232:235], v[216:219], v[224:227], 0\n"
    "ds_read_b64_tr_b16 v[172:173], v250 offset:40960\n"
    "ds_read_b64_tr_b16 v[174:175], v251 offset:40960\n"
    "ds_read_b128 v[224:227], v240 offset:128\n"
    "v_mfma_f32_16x16x32_bf16 v[136:139], v[220:223], v[0:3], 0\n"
    "v_mul_f32_e32 v176, v176, v252\n"
    "v_mul_f32_e32 v177, v177, v252\n"
    "ds_read_b64_tr_b16 v[212:213], v244 offset:40960\n"
    "ds_read_b64_tr_b16 v[214:215], v245 offset:40960\n"
    "v_mfma_f32_16x16x32_bf16 v[140:143], v[220:223], v[4:7], 0\n"
    "v_mul_f32_e32 v178, v178, v252\n"
    "v_mul_f32_e32 v179, v179, v252\n"
    "v_mfma_f32_16x16x32_bf16 v[128:131], v[208:211], v[8:11], v[128:131]\n"
    "v_cvt_pk_bf16_f32 v176, v176, v177\n"
    "v_cvt_pk_bf16_f32 v177, v178, v179\n"
    "v_mul_f32_e32 v184, v184, v252\n"
    "v_mfma_f32_16x16x32_bf16 v[132:135], v[208:211], v[12:15], v[132:135]\n"
    "v_mul_f32_e32 v185, v185, v252\n"
    "v_mul_f32_e32 v186, v186, v252\n"
    "v_mfma_f32_16x16x32_bf16 v[232:235], v[208:211], v[228:231], v[232:235]\n"
    "v_mul_f32_e32 v187, v187, v252\n"
    "v_cvt_pk_bf16_f32 v184, v184, v185\n"
    "ds_read_b64_tr_b16 v[216:217], v166 offset:40960\n"
    "ds_read_b64_tr_b16 v[218:219], v167 offset:40960\n"
    "ds_read_b128 v[228:231], v242 offset:128\n"
    "s_waitcnt lgkmcnt(3)\n"
    "v_mfma_f32_16x16x32_bf16 v[136:139], v[168:171], v[8:11], v[136:139]\n"
    "v_cvt_pk_bf16_f32 v185, v186, v187\n"
    "v_mbcnt_lo_u32_b32 v178, -1, 0\n"
    "ds_read_b64_tr_b16 v[220:221], v160 offset:40960\n"
    "ds_read_b64_tr_b16 v[222:223], v161 offset:40960\n"
    "v_mfma_f32_16x16x32_bf16 v[140:143], v[168:171], v[12:15], v[140:143]\n"
    "v_mbcnt_hi_u32_b32 v178, -1, v178\n"
    "v_and_b32_e32 v179, 15, v178\n"
    "v_mfma_f32_16x16x32_bf16 v[128:131], v[172:175], v[16:19], v[128:131]\n"
    "v_lshrrev_b32_e32 v187, 4, v178\n"
    "v_lshl_add_u32 v187, v187, 3, s27\n"
    "v_mfma_f32_16x16x32_bf16 v[132:135], v[172:175], v[20:23], v[132:135]\n"
    "v_mad_u32_u24 v179, v179, s28, v187\n"
    "v_and_b32_e32 v187, 7, v178\n"
    "v_mfma_f32_16x16x32_bf16 v[232:235], v[172:175], v[224:227], v[232:235]\n"
    "v_xor_b32_e32 v187, s29, v187\n"
    "v_lshl_add_u32 v187, v187, 4, s27\n"
    "ds_read_b64_tr_b16 v[208:209], v250 offset:49152\n"
    "ds_read_b64_tr_b16 v[210:211], v251 offset:49152\n"
    "ds_read_b128 v[224:227], v240 offset:256\n"
    "v_mfma_f32_16x16x32_bf16 v[136:139], v[212:215], v[16:19], v[136:139]\n"
    "v_lshrrev_b32_e32 v186, 3, v178\n"
    "v_mad_u32_u24 v186, v186, s28, v187\n"
    "ds_write_b64 v179, v[176:177]\n"
    "ds_read_b64_tr_b16 v[168:169], v244 offset:49152\n"
    "ds_read_b64_tr_b16 v[170:171], v245 offset:49152\n"
    "v_mfma_f32_16x16x32_bf16 v[140:143], v[212:215], v[20:23], v[140:143]\n"
    "ds_write_b64 v179, v[184:185] offset:32\n"
    "s_waitcnt lgkmcnt(4)\n"
    "v_mfma_f32_16x16x32_bf16 v[128:131], v[216:219], v[24:27], v[128:131]\n"
    "v_mul_f32_e32 v192, v192, v252\n"
    "v_mul_f32_e32 v193, v193, v252\n"
    "v_mfma_f32_16x16x32_bf16 v[132:135], v[216:219], v[28:31], v[132:135]\n"
    "v_mul_f32_e32 v194, v194, v252\n"
    "v_mul_f32_e32 v195, v195, v252\n"
    "v_mfma_f32_16x16x32_bf16 v[232:235], v[216:219], v[228:231], v[232:235]\n"
    "v_cvt_pk_bf16_f32 v192, v192, v193\n"
    "v_cvt_pk_bf16_f32 v193, v194, v195\n"
    "ds_read_b64_tr_b16 v[172:173], v166 offset:49152\n"
    "ds_read_b64_tr_b16 v[174:175], v167 offset:49152\n"
    "ds_read_b128 v[228:231], v242 offset:256\n"
    "v_mfma_f32_16x16x32_bf16 v[136:139], v[220:223], v[24:27], v[136:139]\n"
    "ds_write_b64 v179, v[192:193] offset:64\n"
    "v_mul_f32_e32 v200, v200, v252\n"
    "ds_read_b64_tr_b16 v[212:213], v160 offset:49152\n"
    "ds_read_b64_tr_b16 v[214:215], v161 offset:49152\n"
    "v_mfma_f32_16x16x32_bf16 v[140:143], v[220:223], v[28:31], v[140:143]\n"
    "v_mul_f32_e32 v201, v201, v252\n"
    "v_mul_f32_e32 v202, v202, v252\n"
    "v_mfma_f32_16x16x32_bf16 v[128:131], v[208:211], v[32:35], v[128:131]\n"
    "v_mul_f32_e32 v203, v203, v252\n"
    "v_cvt_pk_bf16_f32 v200, v200, v201\n"
    "v_mfma_f32_16x16x32_bf16 v[132:135], v[208:211], v[36:39], v[132:135]\n"
    "v_cvt_pk_bf16_f32 v201, v202, v203\n"
    "ds_write_b64 v179, v[200:201] offset:96\n"
    "v_mfma_f32_16x16x32_bf16 v[232:235], v[208:211], v[224:227], v[232:235]\n"
    "ds_read_b128 v[192:195], v186\n"
    "ds_read_b128 v[200:203], v186 offset:1152\n"
    "ds_read_b64_tr_b16 v[216:217], v250 offset:57344\n"
    "ds_read_b64_tr_b16 v[218:219], v251 offset:57344\n"
    "ds_read_b128 v[224:227], v240 offset:384\n"
    "s_waitcnt lgkmcnt(6)\n"
    "v_mfma_f32_16x16x32_bf16 v[136:139], v[168:171], v[32:35], v[136:139]\n"
    "v_mul_f32_e32 v180, v180, v253\n"
    "v_mul_f32_e32 v181, v181, v253\n"
    "ds_read_b64_tr_b16 v[220:221], v244 offset:57344\n"
    "ds_read_b64_tr_b16 v[222:223], v245 offset:57344\n"
    "v_mfma_f32_16x16x32_bf16 v[140:143], v[168:171], v[36:39], v[140:143]\n"
    "v_mul_f32_e32 v182, v182, v253\n"
    "v_mfma_f32_16x16x32_bf16 v[128:131], v[172:175], v[40:43], v[128:131]\n"
    "v_mul_f32_e32 v183, v183, v253\n"
    "v_cvt_pk_bf16_f32 v180, v180, v181\n"
    "v_mfma_f32_16x16x32_bf16 v[132:135], v[172:175], v[44:47], v[132:135]\n"
    "v_cvt_pk_bf16_f32 v181, v182, v183\n"
    "v_mfma_f32_16x16x32_bf16 v[232:235], v[172:175], v[228:231], v[232:235]\n"
    "ds_write_b64 v179, v[180:181] offset:32768\n"
    "ds_read_b64_tr_b16 v[208:209], v166 offset:57344\n"
    "ds_read_b64_tr_b16 v[210:211], v167 offset:57344\n"
    "ds_read_b128 v[228:231], v242 offset:384\n"
    "v_mfma_f32_16x16x32_bf16 v[136:139], v[212:215], v[40:43], v[136:139]\n"
    "v_mul_f32_e32 v188, v188, v253\n"
    "v_mul_f32_e32 v189, v189, v253\n"
    "v_mul_f32_e32 v190, v190, v253\n"
    "ds_read_b64_tr_b16 v[168:169], v160 offset:57344\n"
    "ds_read_b64_tr_b16 v[170:171], v161 offset:57344\n"
    "v_mfma_f32_16x16x32_bf16 v[140:143], v[212:215], v[44:47], v[140:143]\n"
    "s_waitcnt lgkmcnt(0)\n"
    "buffer_store_dwordx4 v[192:195], v241, s[52:55], s58 offen offset:128 nt\n"
    "v_mfma_f32_16x16x32_bf16 v[128:131], v[216:219], v[48:51], v[128:131]\n"
    "v_mul_f32_e32 v191, v191, v253\n"
    "v_cvt_pk_bf16_f32 v188, v188, v189\n"
    "v_mfma_f32_16x16x32_bf16 v[132:135], v[216:219], v[52:55], v[132:135]\n"
    "v_cvt_pk_bf16_f32 v189, v190, v191\n"
    "ds_write_b64 v179, v[188:189] offset:32800\n"
    "v_mfma_f32_16x16x32_bf16 v[232:235], v[216:219], v[224:227], v[232:235]\n"
    "v_mul_f32_e32 v196, v196, v253\n"
    "v_add_u32_e32 v250, 0xfffec200, v250\n"
    "v_add_u32_e32 v251, 0xfffec200, v251\n"
    "ds_read_b64_tr_b16 v[172:173], v250\n"
    "ds_read_b64_tr_b16 v[174:175], v251\n"
    "ds_read_b128 v[224:227], v240 offset:512\n"
    "v_mfma_f32_16x16x32_bf16 v[136:139], v[220:223], v[48:51], v[136:139]\n"
    "v_mul_f32_e32 v197, v197, v253\n"
    "v_mul_f32_e32 v198, v198, v253\n"
    "v_mul_f32_e32 v199, v199, v253\n"
    "v_add_u32_e32 v244, 0xfffec200, v244\n"
    "v_add_u32_e32 v245, 0xfffec200, v245\n"
    "ds_read_b64_tr_b16 v[212:213], v244\n"
    "ds_read_b64_tr_b16 v[214:215], v245\n"
    "v_mfma_f32_16x16x32_bf16 v[140:143], v[220:223], v[52:55], v[140:143]\n"
    "buffer_store_dwordx4 v[200:203], v241, s[52:55], s59 offen offset:128 nt\n"
    "v_mfma_f32_16x16x32_bf16 v[128:131], v[208:211], v[56:59], v[128:131]\n"
    "v_cvt_pk_bf16_f32 v196, v196, v197\n"
    "v_cvt_pk_bf16_f32 v197, v198, v199\n"
    "v_mfma_f32_16x16x32_bf16 v[132:135], v[208:211], v[60:63], v[132:135]\n"
    "ds_write_b64 v179, v[196:197] offset:32832\n"
    "v_mfma_f32_16x16x32_bf16 v[232:235], v[208:211], v[228:231], v[232:235]\n"
    "v_mul_f32_e32 v204, v204, v253\n"
    "v_add_u32_e32 v166, 0xfffec200, v166\n"
    "v_add_u32_e32 v167, 0xfffec200, v167\n"
    "ds_read_b64_tr_b16 v[216:217], v166\n"
    "ds_read_b64_tr_b16 v[218:219], v167\n"
    "ds_read_b128 v[228:231], v242 offset:512\n"
    "v_mfma_f32_16x16x32_bf16 v[136:139], v[168:171], v[56:59], v[136:139]\n"
    "v_mul_f32_e32 v205, v205, v253\n"
    "v_mul_f32_e32 v206, v206, v253\n"
    "v_add_u32_e32 v160, 0xfffec200, v160\n"
    "v_add_u32_e32 v161, 0xfffec200, v161\n"
    "ds_read_b64_tr_b16 v[220:221], v160\n"
    "ds_read_b64_tr_b16 v[222:223], v161\n"
    "v_mfma_f32_16x16x32_bf16 v[140:143], v[168:171], v[60:63], v[140:143]\n"
    "v_mul_f32_e32 v207, v207, v253\n"
    "s_waitcnt lgkmcnt(2)\n"
    "v_mfma_f32_16x16x32_bf16 v[128:131], v[172:175], v[64:67], v[128:131]\n"
    "v_cvt_pk_bf16_f32 v204, v204, v205\n"
    "v_cvt_pk_bf16_f32 v205, v206, v207\n"
    "v_mfma_f32_16x16x32_bf16 v[132:135], v[172:175], v[68:71], v[132:135]\n"
    "ds_write_b64 v179, v[204:205] offset:32864\n"
    "v_mfma_f32_16x16x32_bf16 v[232:235], v[172:175], v[224:227], v[232:235]\n"
    "ds_read_b128 v[196:199], v186 offset:32768\n"
    "ds_read_b128 v[204:207], v186 offset:33920\n"
    "ds_read_b64_tr_b16 v[208:209], v250 offset:8192\n"
    "ds_read_b64_tr_b16 v[210:211], v251 offset:8192\n"
    "ds_read_b128 v[224:227], v240 offset:640\n"
    "v_mfma_f32_16x16x32_bf16 v[136:139], v[212:215], v[64:67], v[136:139]\n"
    "ds_read_b64_tr_b16 v[168:169], v244 offset:8192\n"
    "ds_read_b64_tr_b16 v[170:171], v245 offset:8192\n"
    "v_mfma_f32_16x16x32_bf16 v[140:143], v[212:215], v[68:71], v[140:143]\n"
    "v_mfma_f32_16x16x32_bf16 v[128:131], v[216:219], v[72:75], v[128:131]\n"
    "v_mfma_f32_16x16x32_bf16 v[132:135], v[216:219], v[76:79], v[132:135]\n"
    "v_mfma_f32_16x16x32_bf16 v[232:235], v[216:219], v[228:231], v[232:235]\n"
    "ds_read_b64_tr_b16 v[172:173], v166 offset:8192\n"
    "ds_read_b64_tr_b16 v[174:175], v167 offset:8192\n"
    "ds_read_b128 v[228:231], v242 offset:640\n"
    "s_waitcnt lgkmcnt(3)\n"
    "v_mfma_f32_16x16x32_bf16 v[136:139], v[220:223], v[72:75], v[136:139]\n"
    "buffer_store_dwordx4 v[196:199], v241, s[52:55], s60 offen offset:128 nt\n"
    "ds_read_b64_tr_b16 v[212:213], v160 offset:8192\n"
    "ds_read_b64_tr_b16 v[214:215], v161 offset:8192\n"
    "v_mfma_f32_16x16x32_bf16 v[140:143], v[220:223], v[76:79], v[140:143]\n"
    "v_mfma_f32_16x16x32_bf16 v[128:131], v[208:211], v[80:83], v[128:131]\n"
    "v_mul_f32_e32 v236, v236, v254\n"
    "v_mfma_f32_16x16x32_bf16 v[132:135], v[208:211], v[84:87], v[132:135]\n"
    "v_mul_f32_e32 v237, v237, v254\n"
    "v_mfma_f32_16x16x32_bf16 v[232:235], v[208:211], v[224:227], v[232:235]\n"
    "ds_read_b64_tr_b16 v[216:217], v250 offset:16384\n"
    "ds_read_b64_tr_b16 v[218:219], v251 offset:16384\n"
    "ds_read_b128 v[224:227], v240 offset:768\n"
    "v_mfma_f32_16x16x32_bf16 v[136:139], v[168:171], v[80:83], v[136:139]\n"
    "buffer_store_dwordx4 v[204:207], v241, s[52:55], s64 offen offset:128 nt\n"
    "ds_read_b64_tr_b16 v[220:221], v244 offset:16384\n"
    "ds_read_b64_tr_b16 v[222:223], v245 offset:16384\n"
    "v_mfma_f32_16x16x32_bf16 v[140:143], v[168:171], v[84:87], v[140:143]\n"
    "v_mul_f32_e32 v238, v238, v254\n"
    "s_waitcnt lgkmcnt(2)\n"
    "v_mfma_f32_16x16x32_bf16 v[128:131], v[172:175], v[88:91], v[128:131]\n"
    "v_mul_f32_e32 v239, v239, v254\n"
    "v_mfma_f32_16x16x32_bf16 v[132:135], v[172:175], v[92:95], v[132:135]\n"
    "v_cvt_pk_bf16_f32 v236, v236, v237\n"
    "v_mfma_f32_16x16x32_bf16 v[232:235], v[172:175], v[228:231], v[232:235]\n"
    "ds_read_b64_tr_b16 v[208:209], v166 offset:16384\n"
    "ds_read_b64_tr_b16 v[210:211], v167 offset:16384\n"
    "ds_read_b128 v[228:231], v242 offset:768\n"
    "v_mfma_f32_16x16x32_bf16 v[136:139], v[212:215], v[88:91], v[136:139]\n"
    "v_cvt_pk_bf16_f32 v237, v238, v239\n"
    "ds_read_b64_tr_b16 v[168:169], v160 offset:16384\n"
    "ds_read_b64_tr_b16 v[170:171], v161 offset:16384\n"
    "v_mfma_f32_16x16x32_bf16 v[140:143], v[212:215], v[92:95], v[140:143]\n"
    "v_mfma_f32_16x16x32_bf16 v[128:131], v[216:219], v[96:99], v[128:131]\n"
    "s_setprio 2\n"
    "buffer_store_dwordx2 v[236:237], v255, s[52:55], s65 offen offset:128 nt\n"
    "v_mfma_f32_16x16x32_bf16 v[132:135], v[216:219], v[100:103], v[132:135]\n"
    "v_mfma_f32_16x16x32_bf16 v[232:235], v[216:219], v[224:227], v[232:235]\n"
    "ds_read_b64_tr_b16 v[172:173], v250 offset:24576\n"
    "ds_read_b64_tr_b16 v[174:175], v251 offset:24576\n"
    "ds_read_b128 v[224:227], v240 offset:896\n"
    "s_waitcnt lgkmcnt(3)\n"
    "v_mfma_f32_16x16x32_bf16 v[136:139], v[220:223], v[96:99], v[136:139]\n"
    "ds_read_b64_tr_b16 v[212:213], v244 offset:24576\n"
    "ds_read_b64_tr_b16 v[214:215], v245 offset:24576\n"
    "v_mfma_f32_16x16x32_bf16 v[140:143], v[220:223], v[100:103], v[140:143]\n"
    "v_mfma_f32_16x16x32_bf16 v[128:131], v[208:211], v[104:107], v[128:131]\n"
    "v_mfma_f32_16x16x32_bf16 v[132:135], v[208:211], v[108:111], v[132:135]\n"
    "v_mfma_f32_16x16x32_bf16 v[232:235], v[208:211], v[228:231], v[232:235]\n"
    "ds_read_b64_tr_b16 v[216:217], v166 offset:24576\n"
    "ds_read_b64_tr_b16 v[218:219], v167 offset:24576\n"
    "ds_read_b128 v[228:231], v242 offset:896\n"
    "v_mfma_f32_16x16x32_bf16 v[136:139], v[168:171], v[104:107], v[136:139]\n"
    "ds_read_b64_tr_b16 v[220:221], v160 offset:24576\n"
    "ds_read_b64_tr_b16 v[222:223], v161 offset:24576\n"
    "v_mfma_f32_16x16x32_bf16 v[140:143], v[168:171], v[108:111], v[140:143]\n"
    "ds_read_b64_tr_b16 v[208:209], v246 offset:32768\n"
    "s_waitcnt lgkmcnt(3)\n"
    "v_mfma_f32_16x16x32_bf16 v[128:131], v[172:175], v[112:115], v[128:131]\n"
    "ds_read_b64_tr_b16 v[210:211], v247 offset:32768\n"
    "v_mfma_f32_16x16x32_bf16 v[132:135], v[172:175], v[116:119], v[132:135]\n"
    "v_mfma_f32_16x16x32_bf16 v[232:235], v[172:175], v[224:227], v[232:235]\n"
    "ds_read_b64_tr_b16 v[168:169], v248 offset:32768\n"
    "v_mfma_f32_16x16x32_bf16 v[136:139], v[212:215], v[112:115], v[136:139]\n"
    "ds_read_b64_tr_b16 v[170:171], v249 offset:32768\n"
    "v_mfma_f32_16x16x32_bf16 v[140:143], v[212:215], v[116:119], v[140:143]\n"
    "ds_read_b64_tr_b16 v[172:173], v162 offset:32768\n"
    "v_mfma_f32_16x16x32_bf16 v[128:131], v[216:219], v[120:123], v[128:131]\n"
    "ds_read_b64_tr_b16 v[174:175], v163 offset:32768\n"
    "v_mfma_f32_16x16x32_bf16 v[132:135], v[216:219], v[124:127], v[132:135]\n"
    "v_mfma_f32_16x16x32_bf16 v[232:235], v[216:219], v[228:231], v[232:235]\n"
    "ds_read_b64_tr_b16 v[212:213], v164 offset:32768\n"
    "s_waitcnt lgkmcnt(1)\n"
    "v_mfma_f32_16x16x32_bf16 v[136:139], v[220:223], v[120:123], v[136:139]\n"
    "ds_read_b64_tr_b16 v[214:215], v165 offset:32768\n"
    "v_mfma_f32_16x16x32_bf16 v[140:143], v[220:223], v[124:127], v[140:143]\n"
    "ds_read_b64_tr_b16 v[216:217], v246 offset:40960\n"
    "v_mfma_f32_16x16x32_bf16 v[144:147], v[208:211], v[0:3], 0\n"
    "ds_read_b64_tr_b16 v[218:219], v247 offset:40960\n"
    "v_mfma_f32_16x16x32_bf16 v[148:151], v[208:211], v[4:7], 0\n"
    "ds_read_b64_tr_b16 v[220:221], v248 offset:40960\n"
    "v_mfma_f32_16x16x32_bf16 v[152:155], v[168:171], v[0:3], 0\n"
    "ds_read_b64_tr_b16 v[222:223], v249 offset:40960\n"
    "v_mfma_f32_16x16x32_bf16 v[156:159], v[168:171], v[4:7], 0\n"
    "ds_read_b64_tr_b16 v[208:209], v162 offset:40960\n"
    "v_mfma_f32_16x16x32_bf16 v[144:147], v[172:175], v[8:11], v[144:147]\n"
    "v_mul_f32_e32 v128, v128, v252\n"
    "v_mul_f32_e32 v129, v129, v252\n"
    "ds_read_b64_tr_b16 v[210:211], v163 offset:40960\n"
    "v_mfma_f32_16x16x32_bf16 v[148:151], v[172:175], v[12:15], v[148:151]\n"
    "v_mul_f32_e32 v130, v130, v252\n"
    "ds_read_b64_tr_b16 v[168:169], v164 offset:40960\n"
    "s_waitcnt lgkmcnt(2)\n"
    "v_mfma_f32_16x16x32_bf16 v[152:155], v[212:215], v[8:11], v[152:155]\n"
    "v_mul_f32_e32 v131, v131, v252\n"
    "v_cvt_pk_bf16_f32 v128, v128, v129\n"
    "ds_read_b64_tr_b16 v[170:171], v165 offset:40960\n"
    "v_mfma_f32_16x16x32_bf16 v[156:159], v[212:215], v[12:15], v[156:159]\n"
    "v_cvt_pk_bf16_f32 v129, v130, v131\n"
    "ds_read_b64_tr_b16 v[172:173], v246 offset:49152\n"
    "v_mfma_f32_16x16x32_bf16 v[144:147], v[216:219], v[16:19], v[144:147]\n"
    "v_mul_f32_e32 v136, v136, v252\n"
    "v_mul_f32_e32 v137, v137, v252\n"
    "ds_read_b64_tr_b16 v[174:175], v247 offset:49152\n"
    "v_mfma_f32_16x16x32_bf16 v[148:151], v[216:219], v[20:23], v[148:151]\n"
    "v_mul_f32_e32 v138, v138, v252\n"
    "ds_read_b64_tr_b16 v[212:213], v248 offset:49152\n"
    "v_mfma_f32_16x16x32_bf16 v[152:155], v[220:223], v[16:19], v[152:155]\n"
    "v_mul_f32_e32 v139, v139, v252\n"
    "v_cvt_pk_bf16_f32 v136, v136, v137\n"
    "ds_read_b64_tr_b16 v[214:215], v249 offset:49152\n"
    "v_mfma_f32_16x16x32_bf16 v[156:159], v[220:223], v[20:23], v[156:159]\n"
    "v_cvt_pk_bf16_f32 v137, v138, v139\n"
    "ds_read_b64_tr_b16 v[216:217], v162 offset:49152\n"
    "s_waitcnt lgkmcnt(2)\n"
    "v_mfma_f32_16x16x32_bf16 v[144:147], v[208:211], v[24:27], v[144:147]\n"
    "v_mbcnt_lo_u32_b32 v130, -1, 0\n"
    "v_mbcnt_hi_u32_b32 v130, -1, v130\n"
    "ds_read_b64_tr_b16 v[218:219], v163 offset:49152\n"
    "v_mfma_f32_16x16x32_bf16 v[148:151], v[208:211], v[28:31], v[148:151]\n"
    "v_and_b32_e32 v131, 15, v130\n"
    "ds_read_b64_tr_b16 v[220:221], v164 offset:49152\n"
    "v_mfma_f32_16x16x32_bf16 v[152:155], v[168:171], v[24:27], v[152:155]\n"
    "v_lshrrev_b32_e32 v139, 4, v130\n"
    "v_lshl_add_u32 v139, v139, 3, s27\n"
    "ds_read_b64_tr_b16 v[222:223], v165 offset:49152\n"
    "v_mfma_f32_16x16x32_bf16 v[156:159], v[168:171], v[28:31], v[156:159]\n"
    "v_mad_u32_u24 v131, v131, s28, v139\n"
    "ds_read_b64_tr_b16 v[208:209], v246 offset:57344\n"
    "v_mfma_f32_16x16x32_bf16 v[144:147], v[172:175], v[32:35], v[144:147]\n"
    "v_and_b32_e32 v139, 7, v130\n"
    "v_xor_b32_e32 v139, s29, v139\n"
    "ds_read_b64_tr_b16 v[210:211], v247 offset:57344\n"
    "v_mfma_f32_16x16x32_bf16 v[148:151], v[172:175], v[36:39], v[148:151]\n"
    "v_lshl_add_u32 v139, v139, 4, s27\n"
    "ds_read_b64_tr_b16 v[168:169], v248 offset:57344\n"
    "s_waitcnt lgkmcnt(2)\n"
    "v_mfma_f32_16x16x32_bf16 v[152:155], v[212:215], v[32:35], v[152:155]\n"
    "v_lshrrev_b32_e32 v138, 3, v130\n"
    "v_mad_u32_u24 v138, v138, s28, v139\n"
    "ds_read_b64_tr_b16 v[170:171], v249 offset:57344\n"
    "v_mfma_f32_16x16x32_bf16 v[156:159], v[212:215], v[36:39], v[156:159]\n"
    "ds_write_b64 v131, v[128:129]\n"
    "ds_read_b64_tr_b16 v[172:173], v162 offset:57344\n"
    "v_mfma_f32_16x16x32_bf16 v[144:147], v[216:219], v[40:43], v[144:147]\n"
    "ds_write_b64 v131, v[136:137] offset:32\n"
    "ds_read_b64_tr_b16 v[174:175], v163 offset:57344\n"
    "v_mfma_f32_16x16x32_bf16 v[148:151], v[216:219], v[44:47], v[148:151]\n"
    "v_mul_f32_e32 v132, v132, v253\n"
    "ds_read_b64_tr_b16 v[212:213], v164 offset:57344\n"
    "v_mfma_f32_16x16x32_bf16 v[152:155], v[220:223], v[40:43], v[152:155]\n"
    "v_mul_f32_e32 v133, v133, v253\n"
    "v_mul_f32_e32 v134, v134, v253\n"
    "ds_read_b64_tr_b16 v[214:215], v165 offset:57344\n"
    "v_mfma_f32_16x16x32_bf16 v[156:159], v[220:223], v[44:47], v[156:159]\n"
    "v_mul_f32_e32 v135, v135, v253\n"
    "v_add_u32_e32 v246, 0xfffec200, v246\n"
    "v_add_u32_e32 v247, 0xfffec200, v247\n"
    "s_waitcnt lgkmcnt(1)\n"
    "v_mfma_f32_16x16x32_bf16 v[144:147], v[208:211], v[48:51], v[144:147]\n"
    "v_cvt_pk_bf16_f32 v132, v132, v133\n"
    "v_cvt_pk_bf16_f32 v133, v134, v135\n"
    "ds_read_b64_tr_b16 v[216:217], v246\n"
    "ds_read_b64_tr_b16 v[218:219], v247\n"
    "v_mfma_f32_16x16x32_bf16 v[148:151], v[208:211], v[52:55], v[148:151]\n"
    "ds_write_b64 v131, v[132:133] offset:32768\n"
    "v_add_u32_e32 v248, 0xfffec200, v248\n"
    "v_add_u32_e32 v249, 0xfffec200, v249\n"
    "v_mfma_f32_16x16x32_bf16 v[152:155], v[168:171], v[48:51], v[152:155]\n"
    "v_mul_f32_e32 v140, v140, v253\n"
    "v_mul_f32_e32 v141, v141, v253\n"
    "ds_read_b64_tr_b16 v[220:221], v248\n"
    "ds_read_b64_tr_b16 v[222:223], v249\n"
    "v_mfma_f32_16x16x32_bf16 v[156:159], v[168:171], v[52:55], v[156:159]\n"
    "v_mul_f32_e32 v142, v142, v253\n"
    "v_add_u32_e32 v162, 0xfffec200, v162\n"
    "v_add_u32_e32 v163, 0xfffec200, v163\n"
    "v_mfma_f32_16x16x32_bf16 v[144:147], v[172:175], v[56:59], v[144:147]\n"
    "v_mul_f32_e32 v143, v143, v253\n"
    "v_cvt_pk_bf16_f32 v140, v140, v141\n"
    "ds_read_b64_tr_b16 v[208:209], v162\n"
    "ds_read_b64_tr_b16 v[210:211], v163\n"
    "v_mfma_f32_16x16x32_bf16 v[148:151], v[172:175], v[60:63], v[148:151]\n"
    "v_cvt_pk_bf16_f32 v141, v142, v143\n"
    "v_add_u32_e32 v164, 0xfffec200, v164\n"
    "v_add_u32_e32 v165, 0xfffec200, v165\n"
    "s_waitcnt lgkmcnt(2)\n"
    "v_mfma_f32_16x16x32_bf16 v[152:155], v[212:215], v[56:59], v[152:155]\n"
    "ds_write_b64 v131, v[140:141] offset:32800\n"
    "ds_read_b64_tr_b16 v[168:169], v164\n"
    "ds_read_b64_tr_b16 v[170:171], v165\n"
    "v_mfma_f32_16x16x32_bf16 v[156:159], v[212:215], v[60:63], v[156:159]\n"
    "v_mul_f32_e32 v232, v232, v254\n"
    "ds_read_b64_tr_b16 v[172:173], v246 offset:8192\n"
    "v_mfma_f32_16x16x32_bf16 v[144:147], v[216:219], v[64:67], v[144:147]\n"
    "s_setprio 1\n"
    "v_mul_f32_e32 v233, v233, v254\n"
    "v_mul_f32_e32 v234, v234, v254\n"
    "ds_read_b64_tr_b16 v[174:175], v247 offset:8192\n"
    "v_mfma_f32_16x16x32_bf16 v[148:151], v[216:219], v[68:71], v[148:151]\n"
    "v_mul_f32_e32 v235, v235, v254\n"
    "ds_read_b64_tr_b16 v[212:213], v248 offset:8192\n"
    "v_mfma_f32_16x16x32_bf16 v[152:155], v[220:223], v[64:67], v[152:155]\n"
    "v_cvt_pk_bf16_f32 v232, v232, v233\n"
    "v_cvt_pk_bf16_f32 v233, v234, v235\n"
    "ds_read_b64_tr_b16 v[214:215], v249 offset:8192\n"
    "v_mfma_f32_16x16x32_bf16 v[156:159], v[220:223], v[68:71], v[156:159]\n"
    "buffer_store_dwordx2 v[232:233], v255, s[52:55], s65 offen offset:256 nt\n"
    "ds_read_b64_tr_b16 v[216:217], v162 offset:8192\n"
    "s_waitcnt lgkmcnt(2)\n"
    "v_mfma_f32_16x16x32_bf16 v[144:147], v[208:211], v[72:75], v[144:147]\n"
    "ds_read_b64_tr_b16 v[218:219], v163 offset:8192\n"
    "v_mfma_f32_16x16x32_bf16 v[148:151], v[208:211], v[76:79], v[148:151]\n"
    "ds_read_b64_tr_b16 v[220:221], v164 offset:8192\n"
    "v_mfma_f32_16x16x32_bf16 v[152:155], v[168:171], v[72:75], v[152:155]\n"
    "ds_read_b64_tr_b16 v[222:223], v165 offset:8192\n"
    "v_mfma_f32_16x16x32_bf16 v[156:159], v[168:171], v[76:79], v[156:159]\n"
    "ds_read_b64_tr_b16 v[208:209], v246 offset:16384\n"
    "v_mfma_f32_16x16x32_bf16 v[144:147], v[172:175], v[80:83], v[144:147]\n"
    "ds_read_b64_tr_b16 v[210:211], v247 offset:16384\n"
    "v_mfma_f32_16x16x32_bf16 v[148:151], v[172:175], v[84:87], v[148:151]\n"
    "ds_read_b64_tr_b16 v[168:169], v248 offset:16384\n"
    "s_waitcnt lgkmcnt(2)\n"
    "v_mfma_f32_16x16x32_bf16 v[152:155], v[212:215], v[80:83], v[152:155]\n"
    "ds_read_b64_tr_b16 v[170:171], v249 offset:16384\n"
    "v_mfma_f32_16x16x32_bf16 v[156:159], v[212:215], v[84:87], v[156:159]\n"
    "ds_read_b64_tr_b16 v[172:173], v162 offset:16384\n"
    "v_mfma_f32_16x16x32_bf16 v[144:147], v[216:219], v[88:91], v[144:147]\n"
    "ds_read_b64_tr_b16 v[174:175], v163 offset:16384\n"
    "v_mfma_f32_16x16x32_bf16 v[148:151], v[216:219], v[92:95], v[148:151]\n"
    "ds_read_b64_tr_b16 v[212:213], v164 offset:16384\n"
    "v_mfma_f32_16x16x32_bf16 v[152:155], v[220:223], v[88:91], v[152:155]\n"
    "ds_read_b64_tr_b16 v[214:215], v165 offset:16384\n"
    "v_mfma_f32_16x16x32_bf16 v[156:159], v[220:223], v[92:95], v[156:159]\n"
    "ds_read_b64_tr_b16 v[216:217], v246 offset:24576\n"
    "s_waitcnt lgkmcnt(2)\n"
    "v_mfma_f32_16x16x32_bf16 v[144:147], v[208:211], v[96:99], v[144:147]\n"
    "ds_read_b64_tr_b16 v[218:219], v247 offset:24576\n"
    "v_mfma_f32_16x16x32_bf16 v[148:151], v[208:211], v[100:103], v[148:151]\n"
    "ds_read_b64_tr_b16 v[220:221], v248 offset:24576\n"
    "v_mfma_f32_16x16x32_bf16 v[152:155], v[168:171], v[96:99], v[152:155]\n"
    "ds_read_b64_tr_b16 v[222:223], v249 offset:24576\n"
    "v_mfma_f32_16x16x32_bf16 v[156:159], v[168:171], v[100:103], v[156:159]\n"
    "ds_read_b64_tr_b16 v[208:209], v162 offset:24576\n"
    "v_mfma_f32_16x16x32_bf16 v[144:147], v[172:175], v[104:107], v[144:147]\n"
    "ds_read_b64_tr_b16 v[210:211], v163 offset:24576\n"
    "v_mfma_f32_16x16x32_bf16 v[148:151], v[172:175], v[108:111], v[148:151]\n"
    "ds_read_b64_tr_b16 v[168:169], v164 offset:24576\n"
    "s_waitcnt lgkmcnt(2)\n"
    "v_mfma_f32_16x16x32_bf16 v[152:155], v[212:215], v[104:107], v[152:155]\n"
    "ds_read_b64_tr_b16 v[170:171], v165 offset:24576\n"
    "v_mfma_f32_16x16x32_bf16 v[156:159], v[212:215], v[108:111], v[156:159]\n"
    "v_mfma_f32_16x16x32_bf16 v[144:147], v[216:219], v[112:115], v[144:147]\n"
    "s_setprio 0\n"
    "v_mfma_f32_16x16x32_bf16 v[148:151], v[216:219], v[116:119], v[148:151]\n"
    "v_mfma_f32_16x16x32_bf16 v[152:155], v[220:223], v[112:115], v[152:155]\n"
    "v_mfma_f32_16x16x32_bf16 v[156:159], v[220:223], v[116:119], v[156:159]\n"
    "s_waitcnt lgkmcnt(0)\n"
    "v_mfma_f32_16x16x32_bf16 v[144:147], v[208:211], v[120:123], v[144:147]\n"
    "v_mfma_f32_16x16x32_bf16 v[148:151], v[208:211], v[124:127], v[148:151]\n"
    "v_mfma_f32_16x16x32_bf16 v[152:155], v[168:171], v[120:123], v[152:155]\n"
    "s_nop 5\n"
    "v_mul_f32_e32 v144, v144, v252\n"
    "v_mul_f32_e32 v145, v145, v252\n"
    "v_mul_f32_e32 v146, v146, v252\n"
    "v_mul_f32_e32 v147, v147, v252\n"
    "v_cvt_pk_bf16_f32 v144, v144, v145\n"
    "v_cvt_pk_bf16_f32 v145, v146, v147\n"
    "ds_write_b64 v131, v[144:145] offset:64\n"
    "v_mfma_f32_16x16x32_bf16 v[156:159], v[168:171], v[124:127], v[156:159]\n"
    "v_mul_f32_e32 v148, v148, v253\n"
    "v_mul_f32_e32 v149, v149, v253\n"
    "v_mul_f32_e32 v150, v150, v253\n"
    "v_mul_f32_e32 v151, v151, v253\n"
    "v_cvt_pk_bf16_f32 v148, v148, v149\n"
    "v_cvt_pk_bf16_f32 v149, v150, v151\n"
    "ds_write_b64 v131, v[148:149] offset:32832\n"
    "v_mul_f32_e32 v152, v152, v252\n"
    "v_mul_f32_e32 v153, v153, v252\n"
    "v_mul_f32_e32 v154, v154, v252\n"
    "v_mul_f32_e32 v155, v155, v252\n"
    "v_cvt_pk_bf16_f32 v152, v152, v153\n"
    "v_cvt_pk_bf16_f32 v153, v154, v155\n"
    "ds_write_b64 v131, v[152:153] offset:96\n"
    "ds_read_b128 v[132:135], v138\n"
    "ds_read_b128 v[140:143], v138 offset:1152\n"
    "v_mul_f32_e32 v156, v156, v253\n"
    "v_mul_f32_e32 v157, v157, v253\n"
    "v_mul_f32_e32 v158, v158, v253\n"
    "v_mul_f32_e32 v159, v159, v253\n"
    "v_cvt_pk_bf16_f32 v156, v156, v157\n"
    "v_cvt_pk_bf16_f32 v157, v158, v159\n"
    "ds_write_b64 v131, v[156:157] offset:32864\n"
    "ds_read_b128 v[144:147], v138 offset:32768\n"
    "ds_read_b128 v[152:155], v138 offset:33920\n"
    "s_waitcnt lgkmcnt(4)\n"
    "buffer_store_dwordx4 v[132:135], v241, s[52:55], s58 offen offset:256 nt\n"
    "s_waitcnt lgkmcnt(3)\n"
    "buffer_store_dwordx4 v[140:143], v241, s[52:55], s59 offen offset:256 nt\n"
    "s_waitcnt lgkmcnt(1)\n"
    "buffer_store_dwordx4 v[144:147], v241, s[52:55], s60 offen offset:256 nt\n"
    "s_waitcnt lgkmcnt(0)\n"
    "buffer_store_dwordx4 v[152:155], v241, s[52:55], s64 offen offset:256 nt\n"
    "s_endpgm\n"
    ".Lcodeend:\n"
    ".Lrare_a1:\n"
    "s_nop 7\n"
    "s_nop 7\n"
    "v_mbcnt_lo_u32_b32 v124, -1, 0\n"
    "v_mbcnt_hi_u32_b32 v124, -1, v124\n"
    "v_and_b32_e32 v125, 15, v124\n"
    "v_mul_u32_u24_e32 v125, 0x180, v125\n"
    "v_lshrrev_b32_e32 v124, 4, v124\n"
    "v_lshl_add_u32 v12, v124, 4, v125\n"
    "s_xor_b32 s100, s18, 2\n"
    "s_mul_i32 s100, s100, 6144\n"
    "s_add_u32 s100, s100, 0x0\n"
    "buffer_load_dwordx4 v[8:11], v12, s[44:47], s100 offen\n"
    "s_waitcnt vmcnt(0)\n"
    "s_nop 1\n"
    "v_mfma_f32_16x16x32_bf16 v[192:195], v[8:11], v[160:163], 0\n"
    "v_mfma_f32_16x16x32_bf16 v[196:199], v[8:11], v[164:167], 0\n"
    "s_nop 7\n"
    "buffer_load_dwordx4 v[8:11], v12, s[44:47], s100 offen offset:64\n"
    "s_waitcnt vmcnt(0)\n"
    "s_nop 1\n"
    "v_mfma_f32_16x16x32_bf16 v[192:195], v[8:11], v[168:171], v[192:195]\n"
    "v_mfma_f32_16x16x32_bf16 v[196:199], v[8:11], v[172:175], v[196:199]\n"
    "s_nop 7\n"
    "buffer_load_dwordx4 v[8:11], v12, s[44:47], s100 offen offset:128\n"
    "s_waitcnt vmcnt(0)\n"
    "s_nop 1\n"
    "v_mfma_f32_16x16x32_bf16 v[192:195], v[8:11], v[128:131], v[192:195]\n"
    "v_mfma_f32_16x16x32_bf16 v[196:199], v[8:11], v[132:135], v[196:199]\n"
    "s_nop 7\n"
    "buffer_load_dwordx4 v[8:11], v12, s[44:47], s100 offen offset:192\n"
    "s_waitcnt vmcnt(0)\n"
    "s_nop 1\n"
    "v_mfma_f32_16x16x32_bf16 v[192:195], v[8:11], v[136:139], v[192:195]\n"
    "v_mfma_f32_16x16x32_bf16 v[196:199], v[8:11], v[140:143], v[196:199]\n"
    "s_nop 7\n"
    "buffer_load_dwordx4 v[8:11], v12, s[44:47], s100 offen offset:256\n"
    "s_waitcnt vmcnt(0)\n"
    "s_nop 1\n"
    "v_mfma_f32_16x16x32_bf16 v[192:195], v[8:11], v[144:147], v[192:195]\n"
    "v_mfma_f32_16x16x32_bf16 v[196:199], v[8:11], v[148:151], v[196:199]\n"
    "s_nop 7\n"
    "buffer_load_dwordx4 v[8:11], v12, s[44:47], s100 offen offset:320\n"
    "s_waitcnt vmcnt(0)\n"
    "s_nop 1\n"
    "v_mfma_f32_16x16x32_bf16 v[192:195], v[8:11], v[152:155], v[192:195]\n"
    "v_mfma_f32_16x16x32_bf16 v[196:199], v[8:11], v[156:159], v[196:199]\n"
    "s_nop 7\n"
    "s_xor_b32 s100, s18, 3\n"
    "s_mul_i32 s100, s100, 6144\n"
    "s_add_u32 s100, s100, 0x0\n"
    "buffer_load_dwordx4 v[8:11], v12, s[44:47], s100 offen\n"
    "s_waitcnt vmcnt(0)\n"
    "s_nop 1\n"
    "v_mfma_f32_16x16x32_bf16 v[200:203], v[8:11], v[160:163], 0\n"
    "v_mfma_f32_16x16x32_bf16 v[204:207], v[8:11], v[164:167], 0\n"
    "s_nop 7\n"
    "buffer_load_dwordx4 v[8:11], v12, s[44:47], s100 offen offset:64\n"
    "s_waitcnt vmcnt(0)\n"
    "s_nop 1\n"
    "v_mfma_f32_16x16x32_bf16 v[200:203], v[8:11], v[168:171], v[200:203]\n"
    "v_mfma_f32_16x16x32_bf16 v[204:207], v[8:11], v[172:175], v[204:207]\n"
    "s_nop 7\n"
    "buffer_load_dwordx4 v[8:11], v12, s[44:47], s100 offen offset:128\n"
    "s_waitcnt vmcnt(0)\n"
    "s_nop 1\n"
    "v_mfma_f32_16x16x32_bf16 v[200:203], v[8:11], v[128:131], v[200:203]\n"
    "v_mfma_f32_16x16x32_bf16 v[204:207], v[8:11], v[132:135], v[204:207]\n"
    "s_nop 7\n"
    "buffer_load_dwordx4 v[8:11], v12, s[44:47], s100 offen offset:192\n"
    "s_waitcnt vmcnt(0)\n"
    "s_nop 1\n"
    "v_mfma_f32_16x16x32_bf16 v[200:203], v[8:11], v[136:139], v[200:203]\n"
    "v_mfma_f32_16x16x32_bf16 v[204:207], v[8:11], v[140:143], v[204:207]\n"
    "s_nop 7\n"
    "buffer_load_dwordx4 v[8:11], v12, s[44:47], s100 offen offset:256\n"
    "s_waitcnt vmcnt(0)\n"
    "s_nop 1\n"
    "v_mfma_f32_16x16x32_bf16 v[200:203], v[8:11], v[144:147], v[200:203]\n"
    "v_mfma_f32_16x16x32_bf16 v[204:207], v[8:11], v[148:151], v[204:207]\n"
    "s_nop 7\n"
    "buffer_load_dwordx4 v[8:11], v12, s[44:47], s100 offen offset:320\n"
    "s_waitcnt vmcnt(0)\n"
    "s_nop 1\n"
    "v_mfma_f32_16x16x32_bf16 v[200:203], v[8:11], v[152:155], v[200:203]\n"
    "v_mfma_f32_16x16x32_bf16 v[204:207], v[8:11], v[156:159], v[204:207]\n"
    "s_nop 7\n"
    "s_nop 7\n"
    "s_nop 7\n"
    "v_max3_f32 v124, v192, v193, v194\n"
    "v_max3_f32 v124, v124, v195, v200\n"
    "v_max3_f32 v124, v124, v201, v202\n"
    "v_max_f32_e32 v124, v124, v203\n"
    "v_max3_f32 v125, v196, v197, v198\n"
    "v_max3_f32 v125, v125, v199, v204\n"
    "v_max3_f32 v125, v125, v205, v206\n"
    "v_max_f32_e32 v125, v125, v207\n"
    "v_mov_b32_e32 v8, v124\n"
    "s_nop 1\n"
    "v_permlane16_swap_b32_e32 v124, v8\n"
    "v_max_f32_e32 v124, v124, v8\n"
    "v_mov_b32_e32 v8, v124\n"
    "s_nop 1\n"
    "v_permlane32_swap_b32_e32 v124, v8\n"
    "v_max_f32_e32 v124, v124, v8\n"
    "v_mov_b32_e32 v8, v125\n"
    "s_nop 1\n"
    "v_permlane16_swap_b32_e32 v125, v8\n"
    "v_max_f32_e32 v125, v125, v8\n"
    "v_mov_b32_e32 v8, v125\n"
    "s_nop 1\n"
    "v_permlane32_swap_b32_e32 v125, v8\n"
    "v_max_f32_e32 v125, v125, v8\n"
    "v_floor_f32_e64 v124, -v124\n"
    "v_min_f32_e32 v124, v124, v112\n"
    "v_sub_f32_e32 v8, v112, v124\n"
    "v_mov_b32_e32 v112, v124\n"
    "v_sub_f32_e32 v176, v176, v8\n"
    "v_sub_f32_e32 v177, v177, v8\n"
    "v_sub_f32_e32 v178, v178, v8\n"
    "v_sub_f32_e32 v179, v179, v8\n"
    "v_sub_f32_e32 v184, v184, v8\n"
    "v_sub_f32_e32 v185, v185, v8\n"
    "v_sub_f32_e32 v186, v186, v8\n"
    "v_sub_f32_e32 v187, v187, v8\n"
    "v_min_f32_e32 v8, 0x437f0000, v8\n"
    "v_cvt_u32_f32_e32 v8, v8\n"
    "v_sub_u32_e32 v124, 0, v8\n"
    "v_ldexp_f32 v252, v252, v124\n"
    "v_lshlrev_b32_e32 v8, 7, v8\n"
    "v_lshl_or_b32 v8, v8, 16, v8\n"
    "v_pk_sub_u16 v0, v0, v8 clamp\n"
    "v_pk_sub_u16 v1, v1, v8 clamp\n"
    "v_pk_sub_u16 v2, v2, v8 clamp\n"
    "v_pk_sub_u16 v3, v3, v8 clamp\n"
    "v_floor_f32_e64 v125, -v125\n"
    "v_min_f32_e32 v125, v125, v116\n"
    "v_sub_f32_e32 v8, v116, v125\n"
    "v_mov_b32_e32 v116, v125\n"
    "v_sub_f32_e32 v180, v180, v8\n"
    "v_sub_f32_e32 v181, v181, v8\n"
    "v_sub_f32_e32 v182, v182, v8\n"
    "v_sub_f32_e32 v183, v183, v8\n"
    "v_sub_f32_e32 v188, v188, v8\n"
    "v_sub_f32_e32 v189, v189, v8\n"
    "v_sub_f32_e32 v190, v190, v8\n"
    "v_sub_f32_e32 v191, v191, v8\n"
    "v_min_f32_e32 v8, 0x437f0000, v8\n"
    "v_cvt_u32_f32_e32 v8, v8\n"
    "v_sub_u32_e32 v125, 0, v8\n"
    "v_ldexp_f32 v253, v253, v125\n"
    "v_lshlrev_b32_e32 v8, 7, v8\n"
    "v_lshl_or_b32 v8, v8, 16, v8\n"
    "v_pk_sub_u16 v4, v4, v8 clamp\n"
    "v_pk_sub_u16 v5, v5, v8 clamp\n"
    "v_pk_sub_u16 v6, v6, v8 clamp\n"
    "v_pk_sub_u16 v7, v7, v8 clamp\n"
    "v_mov_b32_e32 v113, v112\n"
    "v_mov_b32_e32 v114, v112\n"
    "v_mov_b32_e32 v115, v112\n"
    "v_mov_b32_e32 v117, v116\n"
    "v_mov_b32_e32 v118, v116\n"
    "v_mov_b32_e32 v119, v116\n"
    "v_add_f32_e32 v192, v112, v192\n"
    "v_add_f32_e32 v193, v112, v193\n"
    "v_add_f32_e32 v194, v112, v194\n"
    "v_exp_f32_e32 v192, v192\n"
    "v_add_f32_e32 v195, v112, v195\n"
    "v_exp_f32_e32 v193, v193\n"
    "v_add_f32_e32 v196, v116, v196\n"
    "v_exp_f32_e32 v194, v194\n"
    "v_add_f32_e32 v197, v116, v197\n"
    "v_exp_f32_e32 v195, v195\n"
    "v_add_f32_e32 v124, v192, v193\n"
    "v_cvt_pk_bf16_f32 v8, v192, v193\n"
    "v_add_f32_e32 v198, v116, v198\n"
    "v_exp_f32_e32 v196, v196\n"
    "v_add_f32_e32 v124, v194, v124\n"
    "v_add_f32_e32 v199, v116, v199\n"
    "v_exp_f32_e32 v197, v197\n"
    "v_add_f32_e32 v124, v195, v124\n"
    "v_cvt_pk_bf16_f32 v9, v194, v195\n"
    "v_add_f32_e32 v200, v112, v200\n"
    "v_exp_f32_e32 v198, v198\n"
    "v_add_f32_e32 v201, v112, v201\n"
    "v_exp_f32_e32 v199, v199\n"
    "v_add_f32_e32 v125, v196, v197\n"
    "v_cvt_pk_bf16_f32 v12, v196, v197\n"
    "v_add_f32_e32 v202, v112, v202\n"
    "v_exp_f32_e32 v200, v200\n"
    "v_add_f32_e32 v125, v198, v125\n"
    "v_add_f32_e32 v203, v112, v203\n"
    "v_exp_f32_e32 v201, v201\n"
    "v_add_f32_e32 v125, v199, v125\n"
    "v_cvt_pk_bf16_f32 v13, v198, v199\n"
    "v_add_f32_e32 v204, v116, v204\n"
    "v_exp_f32_e32 v202, v202\n"
    "v_add_f32_e32 v124, v200, v124\n"
    "v_add_f32_e32 v205, v116, v205\n"
    "v_exp_f32_e32 v203, v203\n"
    "v_add_f32_e32 v124, v201, v124\n"
    "v_cvt_pk_bf16_f32 v10, v200, v201\n"
    "v_add_f32_e32 v206, v116, v206\n"
    "v_exp_f32_e32 v204, v204\n"
    "v_add_f32_e32 v124, v202, v124\n"
    "v_add_f32_e32 v207, v116, v207\n"
    "v_exp_f32_e32 v205, v205\n"
    "v_add_f32_e32 v124, v203, v124\n"
    "v_cvt_pk_bf16_f32 v11, v202, v203\n"
    "v_exp_f32_e32 v206, v206\n"
    "v_add_f32_e32 v125, v204, v125\n"
    "v_exp_f32_e32 v207, v207\n"
    "v_add_f32_e32 v125, v205, v125\n"
    "v_cvt_pk_bf16_f32 v14, v204, v205\n"
    "v_add_f32_e32 v125, v206, v125\n"
    "v_add_f32_e32 v125, v207, v125\n"
    "v_cvt_pk_bf16_f32 v15, v206, v207\n"
    "s_nop 7\n"
    "s_nop 7\n"
    "s_branch .Lback_a1\n"
    ".Lrare_a2:\n"
    "s_nop 7\n"
    "s_nop 7\n"
    "v_mbcnt_lo_u32_b32 v124, -1, 0\n"
    "v_mbcnt_hi_u32_b32 v124, -1, v124\n"
    "v_and_b32_e32 v125, 15, v124\n"
    "v_mul_u32_u24_e32 v125, 0x180, v125\n"
    "v_lshrrev_b32_e32 v124, 4, v124\n"
    "v_lshl_add_u32 v20, v124, 4, v125\n"
    "s_xor_b32 s100, s18, 0\n"
    "s_mul_i32 s100, s100, 6144\n"
    "s_add_u32 s100, s100, 0x6000\n"
    "buffer_load_dwordx4 v[16:19], v20, s[44:47], s100 offen\n"
    "s_waitcnt vmcnt(0)\n"
    "s_nop 1\n"
    "v_mfma_f32_16x16x32_bf16 v[176:179], v[16:19], v[160:163], 0\n"
    "v_mfma_f32_16x16x32_bf16 v[180:183], v[16:19], v[164:167], 0\n"
    "s_nop 7\n"
    "buffer_load_dwordx4 v[16:19], v20, s[44:47], s100 offen offset:64\n"
    "s_waitcnt vmcnt(0)\n"
    "s_nop 1\n"
    "v_mfma_f32_16x16x32_bf16 v[176:179], v[16:19], v[168:171], v[176:179]\n"
    "v_mfma_f32_16x16x32_bf16 v[180:183], v[16:19], v[172:175], v[180:183]\n"
    "s_nop 7\n"
    "buffer_load_dwordx4 v[16:19], v20, s[44:47], s100 offen offset:128\n"
    "s_waitcnt vmcnt(0)\n"
    "s_nop 1\n"
    "v_mfma_f32_16x16x32_bf16 v[176:179], v[16:19], v[128:131], v[176:179]\n"
    "v_mfma_f32_16x16x32_bf16 v[180:183], v[16:19], v[132:135], v[180:183]\n"
    "s_nop 7\n"
    "buffer_load_dwordx4 v[16:19], v20, s[44:47], s100 offen offset:192\n"
    "s_waitcnt vmcnt(0)\n"
    "s_nop 1\n"
    "v_mfma_f32_16x16x32_bf16 v[176:179], v[16:19], v[136:139], v[176:179]\n"
    "v_mfma_f32_16x16x32_bf16 v[180:183], v[16:19], v[140:143], v[180:183]\n"
    "s_nop 7\n"
    "buffer_load_dwordx4 v[16:19], v20, s[44:47], s100 offen offset:256\n"
    "s_waitcnt vmcnt(0)\n"
    "s_nop 1\n"
    "v_mfma_f32_16x16x32_bf16 v[176:179], v[16:19], v[144:147], v[176:179]\n"
    "v_mfma_f32_16x16x32_bf16 v[180:183], v[16:19], v[148:151], v[180:183]\n"
    "s_nop 7\n"
    "buffer_load_dwordx4 v[16:19], v20, s[44:47], s100 offen offset:320\n"
    "s_waitcnt vmcnt(0)\n"
    "s_nop 1\n"
    "v_mfma_f32_16x16x32_bf16 v[176:179], v[16:19], v[152:155], v[176:179]\n"
    "v_mfma_f32_16x16x32_bf16 v[180:183], v[16:19], v[156:159], v[180:183]\n"
    "s_nop 7\n"
    "s_xor_b32 s100, s18, 1\n"
    "s_mul_i32 s100, s100, 6144\n"
    "s_add_u32 s100, s100, 0x6000\n"
    "buffer_load_dwordx4 v[16:19], v20, s[44:47], s100 offen\n"
    "s_waitcnt vmcnt(0)\n"
    "s_nop 1\n"
    "v_mfma_f32_16x16x32_bf16 v[184:187], v[16:19], v[160:163], 0\n"
    "v_mfma_f32_16x16x32_bf16 v[188:191], v[16:19], v[164:167], 0\n"
    "s_nop 7\n"
    "buffer_load_dwordx4 v[16:19], v20, s[44:47], s100 offen offset:64\n"
    "s_waitcnt vmcnt(0)\n"
    "s_nop 1\n"
    "v_mfma_f32_16x16x32_bf16 v[184:187], v[16:19], v[168:171], v[184:187]\n"
    "v_mfma_f32_16x16x32_bf16 v[188:191], v[16:19], v[172:175], v[188:191]\n"
    "s_nop 7\n"
    "buffer_load_dwordx4 v[16:19], v20, s[44:47], s100 offen offset:128\n"
    "s_waitcnt vmcnt(0)\n"
    "s_nop 1\n"
    "v_mfma_f32_16x16x32_bf16 v[184:187], v[16:19], v[128:131], v[184:187]\n"
    "v_mfma_f32_16x16x32_bf16 v[188:191], v[16:19], v[132:135], v[188:191]\n"
    "s_nop 7\n"
    "buffer_load_dwordx4 v[16:19], v20, s[44:47], s100 offen offset:192\n"
    "s_waitcnt vmcnt(0)\n"
    "s_nop 1\n"
    "v_mfma_f32_16x16x32_bf16 v[184:187], v[16:19], v[136:139], v[184:187]\n"
    "v_mfma_f32_16x16x32_bf16 v[188:191], v[16:19], v[140:143], v[188:191]\n"
    "s_nop 7\n"
    "buffer_load_dwordx4 v[16:19], v20, s[44:47], s100 offen offset:256\n"
    "s_waitcnt vmcnt(0)\n"
    "s_nop 1\n"
    "v_mfma_f32_16x16x32_bf16 v[184:187], v[16:19], v[144:147], v[184:187]\n"
    "v_mfma_f32_16x16x32_bf16 v[188:191], v[16:19], v[148:151], v[188:191]\n"
    "s_nop 7\n"
    "buffer_load_dwordx4 v[16:19], v20, s[44:47], s100 offen offset:320\n"
    "s_waitcnt vmcnt(0)\n"
    "s_nop 1\n"
    "v_mfma_f32_16x16x32_bf16 v[184:187], v[16:19], v[152:155], v[184:187]\n"
    "v_mfma_f32_16x16x32_bf16 v[188:191], v[16:19], v[156:159], v[188:191]\n"
    "s_nop 7\n"
    "s_nop 7\n"
    "s_nop 7\n"
    "v_max3_f32 v124, v176, v177, v178\n"
    "v_max3_f32 v124, v124, v179, v184\n"
    "v_max3_f32 v124, v124, v185, v186\n"
    "v_max_f32_e32 v124, v124, v187\n"
    "v_max3_f32 v125, v180, v181, v182\n"
    "v_max3_f32 v125, v125, v183, v188\n"
    "v_max3_f32 v125, v125, v189, v190\n"
    "v_max_f32_e32 v125, v125, v191\n"
    "v_mov_b32_e32 v16, v124\n"
    "s_nop 1\n"
    "v_permlane16_swap_b32_e32 v124, v16\n"
    "v_max_f32_e32 v124, v124, v16\n"
    "v_mov_b32_e32 v16, v124\n"
    "s_nop 1\n"
    "v_permlane32_swap_b32_e32 v124, v16\n"
    "v_max_f32_e32 v124, v124, v16\n"
    "v_mov_b32_e32 v16, v125\n"
    "s_nop 1\n"
    "v_permlane16_swap_b32_e32 v125, v16\n"
    "v_max_f32_e32 v125, v125, v16\n"
    "v_mov_b32_e32 v16, v125\n"
    "s_nop 1\n"
    "v_permlane32_swap_b32_e32 v125, v16\n"
    "v_max_f32_e32 v125, v125, v16\n"
    "v_floor_f32_e64 v124, -v124\n"
    "v_min_f32_e32 v124, v124, v112\n"
    "v_sub_f32_e32 v16, v112, v124\n"
    "v_mov_b32_e32 v112, v124\n"
    "v_sub_f32_e32 v192, v192, v16\n"
    "v_sub_f32_e32 v193, v193, v16\n"
    "v_sub_f32_e32 v194, v194, v16\n"
    "v_sub_f32_e32 v195, v195, v16\n"
    "v_sub_f32_e32 v200, v200, v16\n"
    "v_sub_f32_e32 v201, v201, v16\n"
    "v_sub_f32_e32 v202, v202, v16\n"
    "v_sub_f32_e32 v203, v203, v16\n"
    "v_min_f32_e32 v16, 0x437f0000, v16\n"
    "v_cvt_u32_f32_e32 v16, v16\n"
    "v_sub_u32_e32 v124, 0, v16\n"
    "v_ldexp_f32 v252, v252, v124\n"
    "v_lshlrev_b32_e32 v16, 7, v16\n"
    "v_lshl_or_b32 v16, v16, 16, v16\n"
    "v_pk_sub_u16 v0, v0, v16 clamp\n"
    "v_pk_sub_u16 v1, v1, v16 clamp\n"
    "v_pk_sub_u16 v2, v2, v16 clamp\n"
    "v_pk_sub_u16 v3, v3, v16 clamp\n"
    "v_pk_sub_u16 v8, v8, v16 clamp\n"
    "v_pk_sub_u16 v9, v9, v16 clamp\n"
    "v_pk_sub_u16 v10, v10, v16 clamp\n"
    "v_pk_sub_u16 v11, v11, v16 clamp\n"
    "v_floor_f32_e64 v125, -v125\n"
    "v_min_f32_e32 v125, v125, v116\n"
    "v_sub_f32_e32 v16, v116, v125\n"
    "v_mov_b32_e32 v116, v125\n"
    "v_sub_f32_e32 v196, v196, v16\n"
    "v_sub_f32_e32 v197, v197, v16\n"
    "v_sub_f32_e32 v198, v198, v16\n"
    "v_sub_f32_e32 v199, v199, v16\n"
    "v_sub_f32_e32 v204, v204, v16\n"
    "v_sub_f32_e32 v205, v205, v16\n"
    "v_sub_f32_e32 v206, v206, v16\n"
    "v_sub_f32_e32 v207, v207, v16\n"
    "v_min_f32_e32 v16, 0x437f0000, v16\n"
    "v_cvt_u32_f32_e32 v16, v16\n"
    "v_sub_u32_e32 v125, 0, v16\n"
    "v_ldexp_f32 v253, v253, v125\n"
    "v_lshlrev_b32_e32 v16, 7, v16\n"
    "v_lshl_or_b32 v16, v16, 16, v16\n"
    "v_pk_sub_u16 v4, v4, v16 clamp\n"
    "v_pk_sub_u16 v5, v5, v16 clamp\n"
    "v_pk_sub_u16 v6, v6, v16 clamp\n"
    "v_pk_sub_u16 v7, v7, v16 clamp\n"
    "v_pk_sub_u16 v12, v12, v16 clamp\n"
    "v_pk_sub_u16 v13, v13, v16 clamp\n"
    "v_pk_sub_u16 v14, v14, v16 clamp\n"
    "v_pk_sub_u16 v15, v15, v16 clamp\n"
    "v_mov_b32_e32 v113, v112\n"
    "v_mov_b32_e32 v114, v112\n"
    "v_mov_b32_e32 v115, v112\n"
    "v_mov_b32_e32 v117, v116\n"
    "v_mov_b32_e32 v118, v116\n"
    "v_mov_b32_e32 v119, v116\n"
    "v_add_f32_e32 v176, v112, v176\n"
    "v_add_f32_e32 v177, v112, v177\n"
    "v_add_f32_e32 v178, v112, v178\n"
    "v_exp_f32_e32 v176, v176\n"
    "v_add_f32_e32 v179, v112, v179\n"
    "v_exp_f32_e32 v177, v177\n"
    "v_add_f32_e32 v180, v116, v180\n"
    "v_exp_f32_e32 v178, v178\n"
    "v_add_f32_e32 v181, v116, v181\n"
    "v_exp_f32_e32 v179, v179\n"
    "v_add_f32_e32 v124, v176, v177\n"
    "v_cvt_pk_bf16_f32 v16, v176, v177\n"
    "v_add_f32_e32 v182, v116, v182\n"
    "v_exp_f32_e32 v180, v180\n"
    "v_add_f32_e32 v124, v178, v124\n"
    "v_add_f32_e32 v183, v116, v183\n"
    "v_exp_f32_e32 v181, v181\n"
    "v_add_f32_e32 v124, v179, v124\n"
    "v_cvt_pk_bf16_f32 v17, v178, v179\n"
    "v_add_f32_e32 v184, v112, v184\n"
    "v_exp_f32_e32 v182, v182\n"
    "v_add_f32_e32 v185, v112, v185\n"
    "v_exp_f32_e32 v183, v183\n"
    "v_add_f32_e32 v125, v180, v181\n"
    "v_cvt_pk_bf16_f32 v20, v180, v181\n"
    "v_add_f32_e32 v186, v112, v186\n"
    "v_exp_f32_e32 v184, v184\n"
    "v_add_f32_e32 v125, v182, v125\n"
    "v_add_f32_e32 v187, v112, v187\n"
    "v_exp_f32_e32 v185, v185\n"
    "v_add_f32_e32 v125, v183, v125\n"
    "v_cvt_pk_bf16_f32 v21, v182, v183\n"
    "v_add_f32_e32 v188, v116, v188\n"
    "v_exp_f32_e32 v186, v186\n"
    "v_add_f32_e32 v124, v184, v124\n"
    "v_add_f32_e32 v189, v116, v189\n"
    "v_exp_f32_e32 v187, v187\n"
    "v_add_f32_e32 v124, v185, v124\n"
    "v_cvt_pk_bf16_f32 v18, v184, v185\n"
    "v_add_f32_e32 v190, v116, v190\n"
    "v_exp_f32_e32 v188, v188\n"
    "v_add_f32_e32 v124, v186, v124\n"
    "v_add_f32_e32 v191, v116, v191\n"
    "v_exp_f32_e32 v189, v189\n"
    "v_add_f32_e32 v124, v187, v124\n"
    "v_cvt_pk_bf16_f32 v19, v186, v187\n"
    "v_exp_f32_e32 v190, v190\n"
    "v_add_f32_e32 v125, v188, v125\n"
    "v_exp_f32_e32 v191, v191\n"
    "v_add_f32_e32 v125, v189, v125\n"
    "v_cvt_pk_bf16_f32 v22, v188, v189\n"
    "v_add_f32_e32 v125, v190, v125\n"
    "v_add_f32_e32 v125, v191, v125\n"
    "v_cvt_pk_bf16_f32 v23, v190, v191\n"
    "s_nop 7\n"
    "s_nop 7\n"
    "s_branch .Lback_a2\n"
    ".Lrare_n1:\n"
    "s_nop 7\n"
    "s_nop 7\n"
    "v_mbcnt_lo_u32_b32 v188, -1, 0\n"
    "v_mbcnt_hi_u32_b32 v188, -1, v188\n"
    "v_and_b32_e32 v189, 15, v188\n"
    "v_lshrrev_b32_e32 v188, 4, v188\n"
    "v_lshrrev_b32_e32 v185, 1, v189\n"
    "v_xor_b32_e32 v185, v185, v188\n"
    "v_lshlrev_b32_e32 v185, 4, v185\n"
    "v_lshl_add_u32 v185, v189, 7, v185\n"
    "s_lshl_b32 s100, s17, 3\n"
    "s_add_u32 s100, s100, 0x15000\n"
    "v_add_u32_e32 v185, s100, v185\n"
    "v_xor_b32_e32 v186, 64, v185\n"
    "v_mul_u32_u24_e32 v189, 0x180, v189\n"
    "v_lshl_add_u32 v184, v188, 4, v189\n"
    "s_mul_i32 s100, s16, 1536\n"
    "s_add_u32 s100, s100, 0x6000\n"
    "buffer_load_dwordx4 v[176:179], v184, s[44:47], s100 offen\n"
    "ds_read_b128 v[180:183], v185\n"
    "s_waitcnt vmcnt(0) lgkmcnt(0)\n"
    "v_mfma_f32_16x16x32_bf16 v[240:243], v[176:179], v[180:183], 0\n"
    "s_nop 7\n"
    "buffer_load_dwordx4 v[176:179], v184, s[44:47], s100 offen offset:64\n"
    "ds_read_b128 v[180:183], v186\n"
    "s_waitcnt vmcnt(0) lgkmcnt(0)\n"
    "v_mfma_f32_16x16x32_bf16 v[240:243], v[176:179], v[180:183], v[240:243]\n"
    "s_nop 7\n"
    "buffer_load_dwordx4 v[176:179], v184, s[44:47], s100 offen offset:128\n"
    "ds_read_b128 v[180:183], v185 offset:4096\n"
    "s_waitcnt vmcnt(0) lgkmcnt(0)\n"
    "v_mfma_f32_16x16x32_bf16 v[240:243], v[176:179], v[180:183], v[240:243]\n"
    "s_nop 7\n"
    "buffer_load_dwordx4 v[176:179], v184, s[44:47], s100 offen offset:192\n"
    "ds_read_b128 v[180:183], v186 offset:4096\n"
    "s_waitcnt vmcnt(0) lgkmcnt(0)\n"
    "v_mfma_f32_16x16x32_bf16 v[240:243], v[176:179], v[180:183], v[240:243]\n"
    "s_nop 7\n"
    "buffer_load_dwordx4 v[176:179], v184, s[44:47], s100 offen offset:256\n"
    "ds_read_b128 v[180:183], v185 offset:8192\n"
    "s_waitcnt vmcnt(0) lgkmcnt(0)\n"
    "v_mfma_f32_16x16x32_bf16 v[240:243], v[176:179], v[180:183], v[240:243]\n"
    "s_nop 7\n"
    "buffer_load_dwordx4 v[176:179], v184, s[44:47], s100 offen offset:320\n"
    "ds_read_b128 v[180:183], v186 offset:8192\n"
    "s_waitcnt vmcnt(0) lgkmcnt(0)\n"
    "v_mfma_f32_16x16x32_bf16 v[240:243], v[176:179], v[180:183], v[240:243]\n"
    "s_nop 7\n"
    "s_nop 7\n"
    "v_max3_f32 v188, v240, v241, v242\n"
    "v_max_f32_e32 v188, v188, v243\n"
    "v_mov_b32_e32 v189, v188\n"
    "s_nop 1\n"
    "v_permlane16_swap_b32_e32 v188, v189\n"
    "v_max_f32_e32 v188, v188, v189\n"
    "v_mov_b32_e32 v189, v188\n"
    "s_nop 1\n"
    "v_permlane32_swap_b32_e32 v188, v189\n"
    "v_max_f32_e32 v188, v188, v189\n"
    "v_floor_f32_e64 v188, -v188\n"
    "v_min_f32_e32 v188, v188, v254\n"
    "v_sub_f32_e32 v189, v254, v188\n"
    "v_mov_b32_e32 v254, v188\n"
    "v_min_f32_e32 v189, 0x437f0000, v189\n"
    "v_cvt_u32_f32_e32 v189, v189\n"
    "v_sub_u32_e32 v188, 0, v189\n"
    "v_ldexp_f32 v255, v255, v188\n"
    "v_lshlrev_b32_e32 v189, 7, v189\n"
    "v_lshl_or_b32 v189, v189, 16, v189\n"
    "v_pk_sub_u16 v224, v224, v189 clamp\n"
    "v_pk_sub_u16 v225, v225, v189 clamp\n"
    "v_mov_b32_e32 v120, v254\n"
    "v_mov_b32_e32 v121, v254\n"
    "v_mov_b32_e32 v122, v254\n"
    "v_mov_b32_e32 v123, v254\n"
    "v_mov_b32_e32 v176, 0x14400\n"
    "v_mov_b32_e32 v177, 1\n"
    "ds_write_b32 v176, v177\n"
    "s_waitcnt lgkmcnt(0)\n"
    "v_add_f32_e32 v240, v254, v240\n"
    "v_add_f32_e32 v241, v254, v241\n"
    "v_add_f32_e32 v242, v254, v242\n"
    "v_add_f32_e32 v243, v254, v243\n"
    "v_exp_f32_e32 v240, v240\n"
    "v_exp_f32_e32 v241, v241\n"
    "v_exp_f32_e32 v242, v242\n"
    "v_exp_f32_e32 v243, v243\n"
    "v_cvt_pk_bf16_f32 v226, v240, v241\n"
    "v_add_f32_e32 v240, v240, v241\n"
    "v_cvt_pk_bf16_f32 v227, v242, v243\n"
    "v_add_f32_e32 v242, v242, v243\n"
    "v_add_f32_e32 v240, v240, v242\n"
    "s_nop 7\n"
    "s_nop 7\n"
    "s_branch .Lback_n1\n"
    ".Lrare_a3:\n"
    "s_nop 7\n"
    "s_nop 7\n"
    "v_mbcnt_lo_u32_b32 v124, -1, 0\n"
    "v_mbcnt_hi_u32_b32 v124, -1, v124\n"
    "v_and_b32_e32 v125, 15, v124\n"
    "v_mul_u32_u24_e32 v125, 0x180, v125\n"
    "v_lshrrev_b32_e32 v124, 4, v124\n"
    "v_lshl_add_u32 v28, v124, 4, v125\n"
    "s_xor_b32 s100, s18, 2\n"
    "s_mul_i32 s100, s100, 6144\n"
    "s_add_u32 s100, s100, 0x6000\n"
    "buffer_load_dwordx4 v[24:27], v28, s[44:47], s100 offen\n"
    "s_waitcnt vmcnt(0)\n"
    "s_nop 1\n"
    "v_mfma_f32_16x16x32_bf16 v[192:195], v[24:27], v[160:163], 0\n"
    "v_mfma_f32_16x16x32_bf16 v[196:199], v[24:27], v[164:167], 0\n"
    "s_nop 7\n"
    "buffer_load_dwordx4 v[24:27], v28, s[44:47], s100 offen offset:64\n"
    "s_waitcnt vmcnt(0)\n"
    "s_nop 1\n"
    "v_mfma_f32_16x16x32_bf16 v[192:195], v[24:27], v[168:171], v[192:195]\n"
    "v_mfma_f32_16x16x32_bf16 v[196:199], v[24:27], v[172:175], v[196:199]\n"
    "s_nop 7\n"
    "buffer_load_dwordx4 v[24:27], v28, s[44:47], s100 offen offset:128\n"
    "s_waitcnt vmcnt(0)\n"
    "s_nop 1\n"
    "v_mfma_f32_16x16x32_bf16 v[192:195], v[24:27], v[128:131], v[192:195]\n"
    "v_mfma_f32_16x16x32_bf16 v[196:199], v[24:27], v[132:135], v[196:199]\n"
    "s_nop 7\n"
    "buffer_load_dwordx4 v[24:27], v28, s[44:47], s100 offen offset:192\n"
    "s_waitcnt vmcnt(0)\n"
    "s_nop 1\n"
    "v_mfma_f32_16x16x32_bf16 v[192:195], v[24:27], v[136:139], v[192:195]\n"
    "v_mfma_f32_16x16x32_bf16 v[196:199], v[24:27], v[140:143], v[196:199]\n"
    "s_nop 7\n"
    "buffer_load_dwordx4 v[24:27], v28, s[44:47], s100 offen offset:256\n"
    "s_waitcnt vmcnt(0)\n"
    "s_nop 1\n"
    "v_mfma_f32_16x16x32_bf16 v[192:195], v[24:27], v[144:147], v[192:195]\n"
    "v_mfma_f32_16x16x32_bf16 v[196:199], v[24:27], v[148:151], v[196:199]\n"
    "s_nop 7\n"
    "buffer_load_dwordx4 v[24:27], v28, s[44:47], s100 offen offset:320\n"
    "s_waitcnt vmcnt(0)\n"
    "s_nop 1\n"
    "v_mfma_f32_16x16x32_bf16 v[192:195], v[24:27], v[152:155], v[192:195]\n"
    "v_mfma_f32_16x16x32_bf16 v[196:199], v[24:27], v[156:159], v[196:199]\n"
    "s_nop 7\n"
    "s_xor_b32 s100, s18, 3\n"
    "s_mul_i32 s100, s100, 6144\n"
    "s_add_u32 s100, s100, 0x6000\n"
    "buffer_load_dwordx4 v[24:27], v28, s[44:47], s100 offen\n"
    "s_waitcnt vmcnt(0)\n"
    "s_nop 1\n"
    "v_mfma_f32_16x16x32_bf16 v[200:203], v[24:27], v[160:163], 0\n"
    "v_mfma_f32_16x16x32_bf16 v[204:207], v[24:27], v[164:167], 0\n"
    "s_nop 7\n"
    "buffer_load_dwordx4 v[24:27], v28, s[44:47], s100 offen offset:64\n"
    "s_waitcnt vmcnt(0)\n"
    "s_nop 1\n"
    "v_mfma_f32_16x16x32_bf16 v[200:203], v[24:27], v[168:171], v[200:203]\n"
    "v_mfma_f32_16x16x32_bf16 v[204:207], v[24:27], v[172:175], v[204:207]\n"
    "s_nop 7\n"
    "buffer_load_dwordx4 v[24:27], v28, s[44:47], s100 offen offset:128\n"
    "s_waitcnt vmcnt(0)\n"
    "s_nop 1\n"
    "v_mfma_f32_16x16x32_bf16 v[200:203], v[24:27], v[128:131], v[200:203]\n"
    "v_mfma_f32_16x16x32_bf16 v[204:207], v[24:27], v[132:135], v[204:207]\n"
    "s_nop 7\n"
    "buffer_load_dwordx4 v[24:27], v28, s[44:47], s100 offen offset:192\n"
    "s_waitcnt vmcnt(0)\n"
    "s_nop 1\n"
    "v_mfma_f32_16x16x32_bf16 v[200:203], v[24:27], v[136:139], v[200:203]\n"
    "v_mfma_f32_16x16x32_bf16 v[204:207], v[24:27], v[140:143], v[204:207]\n"
    "s_nop 7\n"
    "buffer_load_dwordx4 v[24:27], v28, s[44:47], s100 offen offset:256\n"
    "s_waitcnt vmcnt(0)\n"
    "s_nop 1\n"
    "v_mfma_f32_16x16x32_bf16 v[200:203], v[24:27], v[144:147], v[200:203]\n"
    "v_mfma_f32_16x16x32_bf16 v[204:207], v[24:27], v[148:151], v[204:207]\n"
    "s_nop 7\n"
    "buffer_load_dwordx4 v[24:27], v28, s[44:47], s100 offen offset:320\n"
    "s_waitcnt vmcnt(0)\n"
    "s_nop 1\n"
    "v_mfma_f32_16x16x32_bf16 v[200:203], v[24:27], v[152:155], v[200:203]\n"
    "v_mfma_f32_16x16x32_bf16 v[204:207], v[24:27], v[156:159], v[204:207]\n"
    "s_nop 7\n"
    "s_nop 7\n"
    "s_nop 7\n"
    "v_max3_f32 v124, v192, v193, v194\n"
    "v_max3_f32 v124, v124, v195, v200\n"
    "v_max3_f32 v124, v124, v201, v202\n"
    "v_max_f32_e32 v124, v124, v203\n"
    "v_max3_f32 v125, v196, v197, v198\n"
    "v_max3_f32 v125, v125, v199, v204\n"
    "v_max3_f32 v125, v125, v205, v206\n"
    "v_max_f32_e32 v125, v125, v207\n"
    "v_mov_b32_e32 v24, v124\n"
    "s_nop 1\n"
    "v_permlane16_swap_b32_e32 v124, v24\n"
    "v_max_f32_e32 v124, v124, v24\n"
    "v_mov_b32_e32 v24, v124\n"
    "s_nop 1\n"
    "v_permlane32_swap_b32_e32 v124, v24\n"
    "v_max_f32_e32 v124, v124, v24\n"
    "v_mov_b32_e32 v24, v125\n"
    "s_nop 1\n"
    "v_permlane16_swap_b32_e32 v125, v24\n"
    "v_max_f32_e32 v125, v125, v24\n"
    "v_mov_b32_e32 v24, v125\n"
    "s_nop 1\n"
    "v_permlane32_swap_b32_e32 v125, v24\n"
    "v_max_f32_e32 v125, v125, v24\n"
    "v_floor_f32_e64 v124, -v124\n"
    "v_min_f32_e32 v124, v124, v112\n"
    "v_sub_f32_e32 v24, v112, v124\n"
    "v_mov_b32_e32 v112, v124\n"
    "v_sub_f32_e32 v176, v176, v24\n"
    "v_sub_f32_e32 v177, v177, v24\n"
    "v_sub_f32_e32 v178, v178, v24\n"
    "v_sub_f32_e32 v179, v179, v24\n"
    "v_sub_f32_e32 v184, v184, v24\n"
    "v_sub_f32_e32 v185, v185, v24\n"
    "v_sub_f32_e32 v186, v186, v24\n"
    "v_sub_f32_e32 v187, v187, v24\n"
    "v_min_f32_e32 v24, 0x437f0000, v24\n"
    "v_cvt_u32_f32_e32 v24, v24\n"
    "v_sub_u32_e32 v124, 0, v24\n"
    "v_ldexp_f32 v252, v252, v124\n"
    "v_lshlrev_b32_e32 v24, 7, v24\n"
    "v_lshl_or_b32 v24, v24, 16, v24\n"
    "v_pk_sub_u16 v0, v0, v24 clamp\n"
    "v_pk_sub_u16 v1, v1, v24 clamp\n"
    "v_pk_sub_u16 v2, v2, v24 clamp\n"
    "v_pk_sub_u16 v3, v3, v24 clamp\n"
    "v_pk_sub_u16 v8, v8, v24 clamp\n"
    "v_pk_sub_u16 v9, v9, v24 clamp\n"
    "v_pk_sub_u16 v10, v10, v24 clamp\n"
    "v_pk_sub_u16 v11, v11, v24 clamp\n"
    "v_pk_sub_u16 v16, v16, v24 clamp\n"
    "v_pk_sub_u16 v17, v17, v24 clamp\n"
    "v_pk_sub_u16 v18, v18, v24 clamp\n"
    "v_pk_sub_u16 v19, v19, v24 clamp\n"
    "v_floor_f32_e64 v125, -v125\n"
    "v_min_f32_e32 v125, v125, v116\n"
    "v_sub_f32_e32 v24, v116, v125\n"
    "v_mov_b32_e32 v116, v125\n"
    "v_sub_f32_e32 v180, v180, v24\n"
    "v_sub_f32_e32 v181, v181, v24\n"
    "v_sub_f32_e32 v182, v182, v24\n"
    "v_sub_f32_e32 v183, v183, v24\n"
    "v_sub_f32_e32 v188, v188, v24\n"
    "v_sub_f32_e32 v189, v189, v24\n"
    "v_sub_f32_e32 v190, v190, v24\n"
    "v_sub_f32_e32 v191, v191, v24\n"
    "v_min_f32_e32 v24, 0x437f0000, v24\n"
    "v_cvt_u32_f32_e32 v24, v24\n"
    "v_sub_u32_e32 v125, 0, v24\n"
    "v_ldexp_f32 v253, v253, v125\n"
    "v_lshlrev_b32_e32 v24, 7, v24\n"
    "v_lshl_or_b32 v24, v24, 16, v24\n"
    "v_pk_sub_u16 v4, v4, v24 clamp\n"
    "v_pk_sub_u16 v5, v5, v24 clamp\n"
    "v_pk_sub_u16 v6, v6, v24 clamp\n"
    "v_pk_sub_u16 v7, v7, v24 clamp\n"
    "v_pk_sub_u16 v12, v12, v24 clamp\n"
    "v_pk_sub_u16 v13, v13, v24 clamp\n"
    "v_pk_sub_u16 v14, v14, v24 clamp\n"
    "v_pk_sub_u16 v15, v15, v24 clamp\n"
    "v_pk_sub_u16 v20, v20, v24 clamp\n"
    "v_pk_sub_u16 v21, v21, v24 clamp\n"
    "v_pk_sub_u16 v22, v22, v24 clamp\n"
    "v_pk_sub_u16 v23, v23, v24 clamp\n"
    "v_mov_b32_e32 v113, v112\n"
    "v_mov_b32_e32 v114, v112\n"
    "v_mov_b32_e32 v115, v112\n"
    "v_mov_b32_e32 v117, v116\n"
    "v_mov_b32_e32 v118, v116\n"
    "v_mov_b32_e32 v119, v116\n"
    "v_add_f32_e32 v192, v112, v192\n"
    "v_add_f32_e32 v193, v112, v193\n"
    "v_add_f32_e32 v194, v112, v194\n"
    "v_exp_f32_e32 v192, v192\n"
    "v_add_f32_e32 v195, v112, v195\n"
    "v_exp_f32_e32 v193, v193\n"
    "v_add_f32_e32 v196, v116, v196\n"
    "v_exp_f32_e32 v194, v194\n"
    "v_add_f32_e32 v197, v116, v197\n"
    "v_exp_f32_e32 v195, v195\n"
    "v_add_f32_e32 v124, v192, v193\n"
    "v_cvt_pk_bf16_f32 v24, v192, v193\n"
    "v_add_f32_e32 v198, v116, v198\n"
    "v_exp_f32_e32 v196, v196\n"
    "v_add_f32_e32 v124, v194, v124\n"
    "v_add_f32_e32 v199, v116, v199\n"
    "v_exp_f32_e32 v197, v197\n"
    "v_add_f32_e32 v124, v195, v124\n"
    "v_cvt_pk_bf16_f32 v25, v194, v195\n"
    "v_add_f32_e32 v200, v112, v200\n"
    "v_exp_f32_e32 v198, v198\n"
    "v_add_f32_e32 v201, v112, v201\n"
    "v_exp_f32_e32 v199, v199\n"
    "v_add_f32_e32 v125, v196, v197\n"
    "v_cvt_pk_bf16_f32 v28, v196, v197\n"
    "v_add_f32_e32 v202, v112, v202\n"
    "v_exp_f32_e32 v200, v200\n"
    "v_add_f32_e32 v125, v198, v125\n"
    "v_add_f32_e32 v203, v112, v203\n"
    "v_exp_f32_e32 v201, v201\n"
    "v_add_f32_e32 v125, v199, v125\n"
    "v_cvt_pk_bf16_f32 v29, v198, v199\n"
    "v_add_f32_e32 v204, v116, v204\n"
    "v_exp_f32_e32 v202, v202\n"
    "v_add_f32_e32 v124, v200, v124\n"
    "v_add_f32_e32 v205, v116, v205\n"
    "v_exp_f32_e32 v203, v203\n"
    "v_add_f32_e32 v124, v201, v124\n"
    "v_cvt_pk_bf16_f32 v26, v200, v201\n"
    "v_add_f32_e32 v206, v116, v206\n"
    "v_exp_f32_e32 v204, v204\n"
    "v_add_f32_e32 v124, v202, v124\n"
    "v_add_f32_e32 v207, v116, v207\n"
    "v_exp_f32_e32 v205, v205\n"
    "v_add_f32_e32 v124, v203, v124\n"
    "v_cvt_pk_bf16_f32 v27, v202, v203\n"
    "v_exp_f32_e32 v206, v206\n"
    "v_add_f32_e32 v125, v204, v125\n"
    "v_exp_f32_e32 v207, v207\n"
    "v_add_f32_e32 v125, v205, v125\n"
    "v_cvt_pk_bf16_f32 v30, v204, v205\n"
    "v_add_f32_e32 v125, v206, v125\n"
    "v_add_f32_e32 v125, v207, v125\n"
    "v_cvt_pk_bf16_f32 v31, v206, v207\n"
    "s_nop 7\n"
    "s_nop 7\n"
    "s_branch .Lback_a3\n"
    ".Lrare_n2:\n"
    "s_nop 7\n"
    "s_nop 7\n"
    "v_mbcnt_lo_u32_b32 v204, -1, 0\n"
    "v_mbcnt_hi_u32_b32 v204, -1, v204\n"
    "v_and_b32_e32 v205, 15, v204\n"
    "v_lshrrev_b32_e32 v204, 4, v204\n"
    "v_lshrrev_b32_e32 v201, 1, v205\n"
    "v_xor_b32_e32 v201, v201, v204\n"
    "v_lshlrev_b32_e32 v201, 4, v201\n"
    "v_lshl_add_u32 v201, v205, 7, v201\n"
    "s_lshl_b32 s100, s17, 3\n"
    "s_add_u32 s100, s100, 0x15000\n"
    "v_add_u32_e32 v201, s100, v201\n"
    "v_xor_b32_e32 v202, 64, v201\n"
    "v_mul_u32_u24_e32 v205, 0x180, v205\n"
    "v_lshl_add_u32 v200, v204, 4, v205\n"
    "s_mul_i32 s100, s16, 1536\n"
    "s_add_u32 s100, s100, 0xc000\n"
    "buffer_load_dwordx4 v[192:195], v200, s[44:47], s100 offen\n"
    "ds_read_b128 v[196:199], v201\n"
    "s_waitcnt vmcnt(0) lgkmcnt(0)\n"
    "v_mfma_f32_16x16x32_bf16 v[240:243], v[192:195], v[196:199], 0\n"
    "s_nop 7\n"
    "buffer_load_dwordx4 v[192:195], v200, s[44:47], s100 offen offset:64\n"
    "ds_read_b128 v[196:199], v202\n"
    "s_waitcnt vmcnt(0) lgkmcnt(0)\n"
    "v_mfma_f32_16x16x32_bf16 v[240:243], v[192:195], v[196:199], v[240:243]\n"
    "s_nop 7\n"
    "buffer_load_dwordx4 v[192:195], v200, s[44:47], s100 offen offset:128\n"
    "ds_read_b128 v[196:199], v201 offset:4096\n"
    "s_waitcnt vmcnt(0) lgkmcnt(0)\n"
    "v_mfma_f32_16x16x32_bf16 v[240:243], v[192:195], v[196:199], v[240:243]\n"
    "s_nop 7\n"
    "buffer_load_dwordx4 v[192:195], v200, s[44:47], s100 offen offset:192\n"
    "ds_read_b128 v[196:199], v202 offset:4096\n"
    "s_waitcnt vmcnt(0) lgkmcnt(0)\n"
    "v_mfma_f32_16x16x32_bf16 v[240:243], v[192:195], v[196:199], v[240:243]\n"
    "s_nop 7\n"
    "buffer_load_dwordx4 v[192:195], v200, s[44:47], s100 offen offset:256\n"
    "ds_read_b128 v[196:199], v201 offset:8192\n"
    "s_waitcnt vmcnt(0) lgkmcnt(0)\n"
    "v_mfma_f32_16x16x32_bf16 v[240:243], v[192:195], v[196:199], v[240:243]\n"
    "s_nop 7\n"
    "buffer_load_dwordx4 v[192:195], v200, s[44:47], s100 offen offset:320\n"
    "ds_read_b128 v[196:199], v202 offset:8192\n"
    "s_waitcnt vmcnt(0) lgkmcnt(0)\n"
    "v_mfma_f32_16x16x32_bf16 v[240:243], v[192:195], v[196:199], v[240:243]\n"
    "s_nop 7\n"
    "s_nop 7\n"
    "v_max3_f32 v204, v240, v241, v242\n"
    "v_max_f32_e32 v204, v204, v243\n"
    "v_mov_b32_e32 v205, v204\n"
    "s_nop 1\n"
    "v_permlane16_swap_b32_e32 v204, v205\n"
    "v_max_f32_e32 v204, v204, v205\n"
    "v_mov_b32_e32 v205, v204\n"
    "s_nop 1\n"
    "v_permlane32_swap_b32_e32 v204, v205\n"
    "v_max_f32_e32 v204, v204, v205\n"
    "v_floor_f32_e64 v204, -v204\n"
    "v_min_f32_e32 v204, v204, v254\n"
    "v_sub_f32_e32 v205, v254, v204\n"
    "v_mov_b32_e32 v254, v204\n"
    "v_min_f32_e32 v205, 0x437f0000, v205\n"
    "v_cvt_u32_f32_e32 v205, v205\n"
    "v_sub_u32_e32 v204, 0, v205\n"
    "v_ldexp_f32 v255, v255, v204\n"
    "v_lshlrev_b32_e32 v205, 7, v205\n"
    "v_lshl_or_b32 v205, v205, 16, v205\n"
    "v_pk_sub_u16 v224, v224, v205 clamp\n"
    "v_pk_sub_u16 v225, v225, v205 clamp\n"
    "v_pk_sub_u16 v226, v226, v205 clamp\n"
    "v_pk_sub_u16 v227, v227, v205 clamp\n"
    "v_mov_b32_e32 v120, v254\n"
    "v_mov_b32_e32 v121, v254\n"
    "v_mov_b32_e32 v122, v254\n"
    "v_mov_b32_e32 v123, v254\n"
    "v_mov_b32_e32 v192, 0x14400\n"
    "v_mov_b32_e32 v193, 1\n"
    "ds_write_b32 v192, v193\n"
    "s_waitcnt lgkmcnt(0)\n"
    "v_add_f32_e32 v240, v254, v240\n"
    "v_add_f32_e32 v241, v254, v241\n"
    "v_add_f32_e32 v242, v254, v242\n"
    "v_add_f32_e32 v243, v254, v243\n"
    "v_exp_f32_e32 v240, v240\n"
    "v_exp_f32_e32 v241, v241\n"
    "v_exp_f32_e32 v242, v242\n"
    "v_exp_f32_e32 v243, v243\n"
    "v_cvt_pk_bf16_f32 v228, v240, v241\n"
    "v_add_f32_e32 v240, v240, v241\n"
    "v_cvt_pk_bf16_f32 v229, v242, v243\n"
    "v_add_f32_e32 v242, v242, v243\n"
    "v_add_f32_e32 v240, v240, v242\n"
    "s_nop 7\n"
    "s_nop 7\n"
    "s_branch .Lback_n2\n"
    ".Lrare_a4:\n"
    "s_nop 7\n"
    "s_nop 7\n"
    "v_mbcnt_lo_u32_b32 v124, -1, 0\n"
    "v_mbcnt_hi_u32_b32 v124, -1, v124\n"
    "v_and_b32_e32 v125, 15, v124\n"
    "v_mul_u32_u24_e32 v125, 0x180, v125\n"
    "v_lshrrev_b32_e32 v124, 4, v124\n"
    "v_lshl_add_u32 v36, v124, 4, v125\n"
    "s_xor_b32 s100, s18, 0\n"
    "s_mul_i32 s100, s100, 6144\n"
    "s_add_u32 s100, s100, 0xc000\n"
    "buffer_load_dwordx4 v[32:35], v36, s[44:47], s100 offen\n"
    "s_waitcnt vmcnt(0)\n"
    "s_nop 1\n"
    "v_mfma_f32_16x16x32_bf16 v[176:179], v[32:35], v[160:163], 0\n"
    "v_mfma_f32_16x16x32_bf16 v[180:183], v[32:35], v[164:167], 0\n"
    "s_nop 7\n"
    "buffer_load_dwordx4 v[32:35], v36, s[44:47], s100 offen offset:64\n"
    "s_waitcnt vmcnt(0)\n"
    "s_nop 1\n"
    "v_mfma_f32_16x16x32_bf16 v[176:179], v[32:35], v[168:171], v[176:179]\n"
    "v_mfma_f32_16x16x32_bf16 v[180:183], v[32:35], v[172:175], v[180:183]\n"
    "s_nop 7\n"
    "buffer_load_dwordx4 v[32:35], v36, s[44:47], s100 offen offset:128\n"
    "s_waitcnt vmcnt(0)\n"
    "s_nop 1\n"
    "v_mfma_f32_16x16x32_bf16 v[176:179], v[32:35], v[128:131], v[176:179]\n"
    "v_mfma_f32_16x16x32_bf16 v[180:183], v[32:35], v[132:135], v[180:183]\n"
    "s_nop 7\n"
    "buffer_load_dwordx4 v[32:35], v36, s[44:47], s100 offen offset:192\n"
    "s_waitcnt vmcnt(0)\n"
    "s_nop 1\n"
    "v_mfma_f32_16x16x32_bf16 v[176:179], v[32:35], v[136:139], v[176:179]\n"
    "v_mfma_f32_16x16x32_bf16 v[180:183], v[32:35], v[140:143], v[180:183]\n"
    "s_nop 7\n"
    "buffer_load_dwordx4 v[32:35], v36, s[44:47], s100 offen offset:256\n"
    "s_waitcnt vmcnt(0)\n"
    "s_nop 1\n"
    "v_mfma_f32_16x16x32_bf16 v[176:179], v[32:35], v[144:147], v[176:179]\n"
    "v_mfma_f32_16x16x32_bf16 v[180:183], v[32:35], v[148:151], v[180:183]\n"
    "s_nop 7\n"
    "buffer_load_dwordx4 v[32:35], v36, s[44:47], s100 offen offset:320\n"
    "s_waitcnt vmcnt(0)\n"
    "s_nop 1\n"
    "v_mfma_f32_16x16x32_bf16 v[176:179], v[32:35], v[152:155], v[176:179]\n"
    "v_mfma_f32_16x16x32_bf16 v[180:183], v[32:35], v[156:159], v[180:183]\n"
    "s_nop 7\n"
    "s_xor_b32 s100, s18, 1\n"
    "s_mul_i32 s100, s100, 6144\n"
    "s_add_u32 s100, s100, 0xc000\n"
    "buffer_load_dwordx4 v[32:35], v36, s[44:47], s100 offen\n"
    "s_waitcnt vmcnt(0)\n"
    "s_nop 1\n"
    "v_mfma_f32_16x16x32_bf16 v[184:187], v[32:35], v[160:163], 0\n"
    "v_mfma_f32_16x16x32_bf16 v[188:191], v[32:35], v[164:167], 0\n"
    "s_nop 7\n"
    "buffer_load_dwordx4 v[32:35], v36, s[44:47], s100 offen offset:64\n"
    "s_waitcnt vmcnt(0)\n"
    "s_nop 1\n"
    "v_mfma_f32_16x16x32_bf16 v[184:187], v[32:35], v[168:171], v[184:187]\n"
    "v_mfma_f32_16x16x32_bf16 v[188:191], v[32:35], v[172:175], v[188:191]\n"
    "s_nop 7\n"
    "buffer_load_dwordx4 v[32:35], v36, s[44:47], s100 offen offset:128\n"
    "s_waitcnt vmcnt(0)\n"
    "s_nop 1\n"
    "v_mfma_f32_16x16x32_bf16 v[184:187], v[32:35], v[128:131], v[184:187]\n"
    "v_mfma_f32_16x16x32_bf16 v[188:191], v[32:35], v[132:135], v[188:191]\n"
    "s_nop 7\n"
    "buffer_load_dwordx4 v[32:35], v36, s[44:47], s100 offen offset:192\n"
    "s_waitcnt vmcnt(0)\n"
    "s_nop 1\n"
    "v_mfma_f32_16x16x32_bf16 v[184:187], v[32:35], v[136:139], v[184:187]\n"
    "v_mfma_f32_16x16x32_bf16 v[188:191], v[32:35], v[140:143], v[188:191]\n"
    "s_nop 7\n"
    "buffer_load_dwordx4 v[32:35], v36, s[44:47], s100 offen offset:256\n"
    "s_waitcnt vmcnt(0)\n"
    "s_nop 1\n"
    "v_mfma_f32_16x16x32_bf16 v[184:187], v[32:35], v[144:147], v[184:187]\n"
    "v_mfma_f32_16x16x32_bf16 v[188:191], v[32:35], v[148:151], v[188:191]\n"
    "s_nop 7\n"
    "buffer_load_dwordx4 v[32:35], v36, s[44:47], s100 offen offset:320\n"
    "s_waitcnt vmcnt(0)\n"
    "s_nop 1\n"
    "v_mfma_f32_16x16x32_bf16 v[184:187], v[32:35], v[152:155], v[184:187]\n"
    "v_mfma_f32_16x16x32_bf16 v[188:191], v[32:35], v[156:159], v[188:191]\n"
    "s_nop 7\n"
    "s_nop 7\n"
    "s_nop 7\n"
    "v_max3_f32 v124, v176, v177, v178\n"
    "v_max3_f32 v124, v124, v179, v184\n"
    "v_max3_f32 v124, v124, v185, v186\n"
    "v_max_f32_e32 v124, v124, v187\n"
    "v_max3_f32 v125, v180, v181, v182\n"
    "v_max3_f32 v125, v125, v183, v188\n"
    "v_max3_f32 v125, v125, v189, v190\n"
    "v_max_f32_e32 v125, v125, v191\n"
    "v_mov_b32_e32 v32, v124\n"
    "s_nop 1\n"
    "v_permlane16_swap_b32_e32 v124, v32\n"
    "v_max_f32_e32 v124, v124, v32\n"
    "v_mov_b32_e32 v32, v124\n"
    "s_nop 1\n"
    "v_permlane32_swap_b32_e32 v124, v32\n"
    "v_max_f32_e32 v124, v124, v32\n"
    "v_mov_b32_e32 v32, v125\n"
    "s_nop 1\n"
    "v_permlane16_swap_b32_e32 v125, v32\n"
    "v_max_f32_e32 v125, v125, v32\n"
    "v_mov_b32_e32 v32, v125\n"
    "s_nop 1\n"
    "v_permlane32_swap_b32_e32 v125, v32\n"
    "v_max_f32_e32 v125, v125, v32\n"
    "v_floor_f32_e64 v124, -v124\n"
    "v_min_f32_e32 v124, v124, v112\n"
    "v_sub_f32_e32 v32, v112, v124\n"
    "v_mov_b32_e32 v112, v124\n"
    "v_sub_f32_e32 v192, v192, v32\n"
    "v_sub_f32_e32 v193, v193, v32\n"
    "v_sub_f32_e32 v194, v194, v32\n"
    "v_sub_f32_e32 v195, v195, v32\n"
    "v_sub_f32_e32 v200, v200, v32\n"
    "v_sub_f32_e32 v201, v201, v32\n"
    "v_sub_f32_e32 v202, v202, v32\n"
    "v_sub_f32_e32 v203, v203, v32\n"
    "v_min_f32_e32 v32, 0x437f0000, v32\n"
    "v_cvt_u32_f32_e32 v32, v32\n"
    "v_sub_u32_e32 v124, 0, v32\n"
    "v_ldexp_f32 v252, v252, v124\n"
    "v_lshlrev_b32_e32 v32, 7, v32\n"
    "v_lshl_or_b32 v32, v32, 16, v32\n"
    "v_pk_sub_u16 v0, v0, v32 clamp\n"
    "v_pk_sub_u16 v1, v1, v32 clamp\n"
    "v_pk_sub_u16 v2, v2, v32 clamp\n"
    "v_pk_sub_u16 v3, v3, v32 clamp\n"
    "v_pk_sub_u16 v8, v8, v32 clamp\n"
    "v_pk_sub_u16 v9, v9, v32 clamp\n"
    "v_pk_sub_u16 v10, v10, v32 clamp\n"
    "v_pk_sub_u16 v11, v11, v32 clamp\n"
    "v_pk_sub_u16 v16, v16, v32 clamp\n"
    "v_pk_sub_u16 v17, v17, v32 clamp\n"
    "v_pk_sub_u16 v18, v18, v32 clamp\n"
    "v_pk_sub_u16 v19, v19, v32 clamp\n"
    "v_pk_sub_u16 v24, v24, v32 clamp\n"
    "v_pk_sub_u16 v25, v25, v32 clamp\n"
    "v_pk_sub_u16 v26, v26, v32 clamp\n"
    "v_pk_sub_u16 v27, v27, v32 clamp\n"
    "v_floor_f32_e64 v125, -v125\n"
    "v_min_f32_e32 v125, v125, v116\n"
    "v_sub_f32_e32 v32, v116, v125\n"
    "v_mov_b32_e32 v116, v125\n"
    "v_sub_f32_e32 v196, v196, v32\n"
    "v_sub_f32_e32 v197, v197, v32\n"
    "v_sub_f32_e32 v198, v198, v32\n"
    "v_sub_f32_e32 v199, v199, v32\n"
    "v_sub_f32_e32 v204, v204, v32\n"
    "v_sub_f32_e32 v205, v205, v32\n"
    "v_sub_f32_e32 v206, v206, v32\n"
    "v_sub_f32_e32 v207, v207, v32\n"
    "v_min_f32_e32 v32, 0x437f0000, v32\n"
    "v_cvt_u32_f32_e32 v32, v32\n"
    "v_sub_u32_e32 v125, 0, v32\n"
    "v_ldexp_f32 v253, v253, v125\n"
    "v_lshlrev_b32_e32 v32, 7, v32\n"
    "v_lshl_or_b32 v32, v32, 16, v32\n"
    "v_pk_sub_u16 v4, v4, v32 clamp\n"
    "v_pk_sub_u16 v5, v5, v32 clamp\n"
    "v_pk_sub_u16 v6, v6, v32 clamp\n"
    "v_pk_sub_u16 v7, v7, v32 clamp\n"
    "v_pk_sub_u16 v12, v12, v32 clamp\n"
    "v_pk_sub_u16 v13, v13, v32 clamp\n"
    "v_pk_sub_u16 v14, v14, v32 clamp\n"
    "v_pk_sub_u16 v15, v15, v32 clamp\n"
    "v_pk_sub_u16 v20, v20, v32 clamp\n"
    "v_pk_sub_u16 v21, v21, v32 clamp\n"
    "v_pk_sub_u16 v22, v22, v32 clamp\n"
    "v_pk_sub_u16 v23, v23, v32 clamp\n"
    "v_pk_sub_u16 v28, v28, v32 clamp\n"
    "v_pk_sub_u16 v29, v29, v32 clamp\n"
    "v_pk_sub_u16 v30, v30, v32 clamp\n"
    "v_pk_sub_u16 v31, v31, v32 clamp\n"
    "v_mov_b32_e32 v113, v112\n"
    "v_mov_b32_e32 v114, v112\n"
    "v_mov_b32_e32 v115, v112\n"
    "v_mov_b32_e32 v117, v116\n"
    "v_mov_b32_e32 v118, v116\n"
    "v_mov_b32_e32 v119, v116\n"
    "v_add_f32_e32 v176, v112, v176\n"
    "v_add_f32_e32 v177, v112, v177\n"
    "v_add_f32_e32 v178, v112, v178\n"
    "v_exp_f32_e32 v176, v176\n"
    "v_add_f32_e32 v179, v112, v179\n"
    "v_exp_f32_e32 v177, v177\n"
    "v_add_f32_e32 v180, v116, v180\n"
    "v_exp_f32_e32 v178, v178\n"
    "v_add_f32_e32 v181, v116, v181\n"
    "v_exp_f32_e32 v179, v179\n"
    "v_add_f32_e32 v124, v176, v177\n"
    "v_cvt_pk_bf16_f32 v32, v176, v177\n"
    "v_add_f32_e32 v182, v116, v182\n"
    "v_exp_f32_e32 v180, v180\n"
    "v_add_f32_e32 v124, v178, v124\n"
    "v_add_f32_e32 v183, v116, v183\n"
    "v_exp_f32_e32 v181, v181\n"
    "v_add_f32_e32 v124, v179, v124\n"
    "v_cvt_pk_bf16_f32 v33, v178, v179\n"
    "v_add_f32_e32 v184, v112, v184\n"
    "v_exp_f32_e32 v182, v182\n"
    "v_add_f32_e32 v185, v112, v185\n"
    "v_exp_f32_e32 v183, v183\n"
    "v_add_f32_e32 v125, v180, v181\n"
    "v_cvt_pk_bf16_f32 v36, v180, v181\n"
    "v_add_f32_e32 v186, v112, v186\n"
    "v_exp_f32_e32 v184, v184\n"
    "v_add_f32_e32 v125, v182, v125\n"
    "v_add_f32_e32 v187, v112, v187\n"
    "v_exp_f32_e32 v185, v185\n"
    "v_add_f32_e32 v125, v183, v125\n"
    "v_cvt_pk_bf16_f32 v37, v182, v183\n"
    "v_add_f32_e32 v188, v116, v188\n"
    "v_exp_f32_e32 v186, v186\n"
    "v_add_f32_e32 v124, v184, v124\n"
    "v_add_f32_e32 v189, v116, v189\n"
    "v_exp_f32_e32 v187, v187\n"
    "v_add_f32_e32 v124, v185, v124\n"
    "v_cvt_pk_bf16_f32 v34, v184, v185\n"
    "v_add_f32_e32 v190, v116, v190\n"
    "v_exp_f32_e32 v188, v188\n"
    "v_add_f32_e32 v124, v186, v124\n"
    "v_add_f32_e32 v191, v116, v191\n"
    "v_exp_f32_e32 v189, v189\n"
    "v_add_f32_e32 v124, v187, v124\n"
    "v_cvt_pk_bf16_f32 v35, v186, v187\n"
    "v_exp_f32_e32 v190, v190\n"
    "v_add_f32_e32 v125, v188, v125\n"
    "v_exp_f32_e32 v191, v191\n"
    "v_add_f32_e32 v125, v189, v125\n"
    "v_cvt_pk_bf16_f32 v38, v188, v189\n"
    "v_add_f32_e32 v125, v190, v125\n"
    "v_add_f32_e32 v125, v191, v125\n"
    "v_cvt_pk_bf16_f32 v39, v190, v191\n"
    "s_nop 7\n"
    "s_nop 7\n"
    "s_branch .Lback_a4\n"
    ".Lrare_a5:\n"
    "s_nop 7\n"
    "s_nop 7\n"
    "v_mbcnt_lo_u32_b32 v124, -1, 0\n"
    "v_mbcnt_hi_u32_b32 v124, -1, v124\n"
    "v_and_b32_e32 v125, 15, v124\n"
    "v_mul_u32_u24_e32 v125, 0x180, v125\n"
    "v_lshrrev_b32_e32 v124, 4, v124\n"
    "v_lshl_add_u32 v44, v124, 4, v125\n"
    "s_xor_b32 s100, s18, 2\n"
    "s_mul_i32 s100, s100, 6144\n"
    "s_add_u32 s100, s100, 0xc000\n"
    "buffer_load_dwordx4 v[40:43], v44, s[44:47], s100 offen\n"
    "s_waitcnt vmcnt(0)\n"
    "s_nop 1\n"
    "v_mfma_f32_16x16x32_bf16 v[192:195], v[40:43], v[160:163], 0\n"
    "v_mfma_f32_16x16x32_bf16 v[196:199], v[40:43], v[164:167], 0\n"
    "s_nop 7\n"
    "buffer_load_dwordx4 v[40:43], v44, s[44:47], s100 offen offset:64\n"
    "s_waitcnt vmcnt(0)\n"
    "s_nop 1\n"
    "v_mfma_f32_16x16x32_bf16 v[192:195], v[40:43], v[168:171], v[192:195]\n"
    "v_mfma_f32_16x16x32_bf16 v[196:199], v[40:43], v[172:175], v[196:199]\n"
    "s_nop 7\n"
    "buffer_load_dwordx4 v[40:43], v44, s[44:47], s100 offen offset:128\n"
    "s_waitcnt vmcnt(0)\n"
    "s_nop 1\n"
    "v_mfma_f32_16x16x32_bf16 v[192:195], v[40:43], v[128:131], v[192:195]\n"
    "v_mfma_f32_16x16x32_bf16 v[196:199], v[40:43], v[132:135], v[196:199]\n"
    "s_nop 7\n"
    "buffer_load_dwordx4 v[40:43], v44, s[44:47], s100 offen offset:192\n"
    "s_waitcnt vmcnt(0)\n"
    "s_nop 1\n"
    "v_mfma_f32_16x16x32_bf16 v[192:195], v[40:43], v[136:139], v[192:195]\n"
    "v_mfma_f32_16x16x32_bf16 v[196:199], v[40:43], v[140:143], v[196:199]\n"
    "s_nop 7\n"
    "buffer_load_dwordx4 v[40:43], v44, s[44:47], s100 offen offset:256\n"
    "s_waitcnt vmcnt(0)\n"
    "s_nop 1\n"
    "v_mfma_f32_16x16x32_bf16 v[192:195], v[40:43], v[144:147], v[192:195]\n"
    "v_mfma_f32_16x16x32_bf16 v[196:199], v[40:43], v[148:151], v[196:199]\n"
    "s_nop 7\n"
    "buffer_load_dwordx4 v[40:43], v44, s[44:47], s100 offen offset:320\n"
    "s_waitcnt vmcnt(0)\n"
    "s_nop 1\n"
    "v_mfma_f32_16x16x32_bf16 v[192:195], v[40:43], v[152:155], v[192:195]\n"
    "v_mfma_f32_16x16x32_bf16 v[196:199], v[40:43], v[156:159], v[196:199]\n"
    "s_nop 7\n"
    "s_xor_b32 s100, s18, 3\n"
    "s_mul_i32 s100, s100, 6144\n"
    "s_add_u32 s100, s100, 0xc000\n"
    "buffer_load_dwordx4 v[40:43], v44, s[44:47], s100 offen\n"
    "s_waitcnt vmcnt(0)\n"
    "s_nop 1\n"
    "v_mfma_f32_16x16x32_bf16 v[200:203], v[40:43], v[160:163], 0\n"
    "v_mfma_f32_16x16x32_bf16 v[204:207], v[40:43], v[164:167], 0\n"
    "s_nop 7\n"
    "buffer_load_dwordx4 v[40:43], v44, s[44:47], s100 offen offset:64\n"
    "s_waitcnt vmcnt(0)\n"
    "s_nop 1\n"
    "v_mfma_f32_16x16x32_bf16 v[200:203], v[40:43], v[168:171], v[200:203]\n"
    "v_mfma_f32_16x16x32_bf16 v[204:207], v[40:43], v[172:175], v[204:207]\n"
    "s_nop 7\n"
    "buffer_load_dwordx4 v[40:43], v44, s[44:47], s100 offen offset:128\n"
    "s_waitcnt vmcnt(0)\n"
    "s_nop 1\n"
    "v_mfma_f32_16x16x32_bf16 v[200:203], v[40:43], v[128:131], v[200:203]\n"
    "v_mfma_f32_16x16x32_bf16 v[204:207], v[40:43], v[132:135], v[204:207]\n"
    "s_nop 7\n"
    "buffer_load_dwordx4 v[40:43], v44, s[44:47], s100 offen offset:192\n"
    "s_waitcnt vmcnt(0)\n"
    "s_nop 1\n"
    "v_mfma_f32_16x16x32_bf16 v[200:203], v[40:43], v[136:139], v[200:203]\n"
    "v_mfma_f32_16x16x32_bf16 v[204:207], v[40:43], v[140:143], v[204:207]\n"
    "s_nop 7\n"
    "buffer_load_dwordx4 v[40:43], v44, s[44:47], s100 offen offset:256\n"
    "s_waitcnt vmcnt(0)\n"
    "s_nop 1\n"
    "v_mfma_f32_16x16x32_bf16 v[200:203], v[40:43], v[144:147], v[200:203]\n"
    "v_mfma_f32_16x16x32_bf16 v[204:207], v[40:43], v[148:151], v[204:207]\n"
    "s_nop 7\n"
    "buffer_load_dwordx4 v[40:43], v44, s[44:47], s100 offen offset:320\n"
    "s_waitcnt vmcnt(0)\n"
    "s_nop 1\n"
    "v_mfma_f32_16x16x32_bf16 v[200:203], v[40:43], v[152:155], v[200:203]\n"
    "v_mfma_f32_16x16x32_bf16 v[204:207], v[40:43], v[156:159], v[204:207]\n"
    "s_nop 7\n"
    "s_nop 7\n"
    "s_nop 7\n"
    "v_max3_f32 v124, v192, v193, v194\n"
    "v_max3_f32 v124, v124, v195, v200\n"
    "v_max3_f32 v124, v124, v201, v202\n"
    "v_max_f32_e32 v124, v124, v203\n"
    "v_max3_f32 v125, v196, v197, v198\n"
    "v_max3_f32 v125, v125, v199, v204\n"
    "v_max3_f32 v125, v125, v205, v206\n"
    "v_max_f32_e32 v125, v125, v207\n"
    "v_mov_b32_e32 v40, v124\n"
    "s_nop 1\n"
    "v_permlane16_swap_b32_e32 v124, v40\n"
    "v_max_f32_e32 v124, v124, v40\n"
    "v_mov_b32_e32 v40, v124\n"
    "s_nop 1\n"
    "v_permlane32_swap_b32_e32 v124, v40\n"
    "v_max_f32_e32 v124, v124, v40\n"
    "v_mov_b32_e32 v40, v125\n"
    "s_nop 1\n"
    "v_permlane16_swap_b32_e32 v125, v40\n"
    "v_max_f32_e32 v125, v125, v40\n"
    "v_mov_b32_e32 v40, v125\n"
    "s_nop 1\n"
    "v_permlane32_swap_b32_e32 v125, v40\n"
    "v_max_f32_e32 v125, v125, v40\n"
    "v_floor_f32_e64 v124, -v124\n"
    "v_min_f32_e32 v124, v124, v112\n"
    "v_sub_f32_e32 v40, v112, v124\n"
    "v_mov_b32_e32 v112, v124\n"
    "v_sub_f32_e32 v176, v176, v40\n"
    "v_sub_f32_e32 v177, v177, v40\n"
    "v_sub_f32_e32 v178, v178, v40\n"
    "v_sub_f32_e32 v179, v179, v40\n"
    "v_sub_f32_e32 v184, v184, v40\n"
    "v_sub_f32_e32 v185, v185, v40\n"
    "v_sub_f32_e32 v186, v186, v40\n"
    "v_sub_f32_e32 v187, v187, v40\n"
    "v_min_f32_e32 v40, 0x437f0000, v40\n"
    "v_cvt_u32_f32_e32 v40, v40\n"
    "v_sub_u32_e32 v124, 0, v40\n"
    "v_ldexp_f32 v252, v252, v124\n"
    "v_lshlrev_b32_e32 v40, 7, v40\n"
    "v_lshl_or_b32 v40, v40, 16, v40\n"
    "v_pk_sub_u16 v0, v0, v40 clamp\n"
    "v_pk_sub_u16 v1, v1, v40 clamp\n"
    "v_pk_sub_u16 v2, v2, v40 clamp\n"
    "v_pk_sub_u16 v3, v3, v40 clamp\n"
    "v_pk_sub_u16 v8, v8, v40 clamp\n"
    "v_pk_sub_u16 v9, v9, v40 clamp\n"
    "v_pk_sub_u16 v10, v10, v40 clamp\n"
    "v_pk_sub_u16 v11, v11, v40 clamp\n"
    "v_pk_sub_u16 v16, v16, v40 clamp\n"
    "v_pk_sub_u16 v17, v17, v40 clamp\n"
    "v_pk_sub_u16 v18, v18, v40 clamp\n"
    "v_pk_sub_u16 v19, v19, v40 clamp\n"
    "v_pk_sub_u16 v24, v24, v40 clamp\n"
    "v_pk_sub_u16 v25, v25, v40 clamp\n"
    "v_pk_sub_u16 v26, v26, v40 clamp\n"
    "v_pk_sub_u16 v27, v27, v40 clamp\n"
    "v_pk_sub_u16 v32, v32, v40 clamp\n"
    "v_pk_sub_u16 v33, v33, v40 clamp\n"
    "v_pk_sub_u16 v34, v34, v40 clamp\n"
    "v_pk_sub_u16 v35, v35, v40 clamp\n"
    "v_floor_f32_e64 v125, -v125\n"
    "v_min_f32_e32 v125, v125, v116\n"
    "v_sub_f32_e32 v40, v116, v125\n"
    "v_mov_b32_e32 v116, v125\n"
    "v_sub_f32_e32 v180, v180, v40\n"
    "v_sub_f32_e32 v181, v181, v40\n"
    "v_sub_f32_e32 v182, v182, v40\n"
    "v_sub_f32_e32 v183, v183, v40\n"
    "v_sub_f32_e32 v188, v188, v40\n"
    "v_sub_f32_e32 v189, v189, v40\n"
    "v_sub_f32_e32 v190, v190, v40\n"
    "v_sub_f32_e32 v191, v191, v40\n"
    "v_min_f32_e32 v40, 0x437f0000, v40\n"
    "v_cvt_u32_f32_e32 v40, v40\n"
    "v_sub_u32_e32 v125, 0, v40\n"
    "v_ldexp_f32 v253, v253, v125\n"
    "v_lshlrev_b32_e32 v40, 7, v40\n"
    "v_lshl_or_b32 v40, v40, 16, v40\n"
    "v_pk_sub_u16 v4, v4, v40 clamp\n"
    "v_pk_sub_u16 v5, v5, v40 clamp\n"
    "v_pk_sub_u16 v6, v6, v40 clamp\n"
    "v_pk_sub_u16 v7, v7, v40 clamp\n"
    "v_pk_sub_u16 v12, v12, v40 clamp\n"
    "v_pk_sub_u16 v13, v13, v40 clamp\n"
    "v_pk_sub_u16 v14, v14, v40 clamp\n"
    "v_pk_sub_u16 v15, v15, v40 clamp\n"
    "v_pk_sub_u16 v20, v20, v40 clamp\n"
    "v_pk_sub_u16 v21, v21, v40 clamp\n"
    "v_pk_sub_u16 v22, v22, v40 clamp\n"
    "v_pk_sub_u16 v23, v23, v40 clamp\n"
    "v_pk_sub_u16 v28, v28, v40 clamp\n"
    "v_pk_sub_u16 v29, v29, v40 clamp\n"
    "v_pk_sub_u16 v30, v30, v40 clamp\n"
    "v_pk_sub_u16 v31, v31, v40 clamp\n"
    "v_pk_sub_u16 v36, v36, v40 clamp\n"
    "v_pk_sub_u16 v37, v37, v40 clamp\n"
    "v_pk_sub_u16 v38, v38, v40 clamp\n"
    "v_pk_sub_u16 v39, v39, v40 clamp\n"
    "v_mov_b32_e32 v113, v112\n"
    "v_mov_b32_e32 v114, v112\n"
    "v_mov_b32_e32 v115, v112\n"
    "v_mov_b32_e32 v117, v116\n"
    "v_mov_b32_e32 v118, v116\n"
    "v_mov_b32_e32 v119, v116\n"
    "v_add_f32_e32 v192, v112, v192\n"
    "v_add_f32_e32 v193, v112, v193\n"
    "v_add_f32_e32 v194, v112, v194\n"
    "v_exp_f32_e32 v192, v192\n"
    "v_add_f32_e32 v195, v112, v195\n"
    "v_exp_f32_e32 v193, v193\n"
    "v_add_f32_e32 v196, v116, v196\n"
    "v_exp_f32_e32 v194, v194\n"
    "v_add_f32_e32 v197, v116, v197\n"
    "v_exp_f32_e32 v195, v195\n"
    "v_add_f32_e32 v124, v192, v193\n"
    "v_cvt_pk_bf16_f32 v40, v192, v193\n"
    "v_add_f32_e32 v198, v116, v198\n"
    "v_exp_f32_e32 v196, v196\n"
    "v_add_f32_e32 v124, v194, v124\n"
    "v_add_f32_e32 v199, v116, v199\n"
    "v_exp_f32_e32 v197, v197\n"
    "v_add_f32_e32 v124, v195, v124\n"
    "v_cvt_pk_bf16_f32 v41, v194, v195\n"
    "v_add_f32_e32 v200, v112, v200\n"
    "v_exp_f32_e32 v198, v198\n"
    "v_add_f32_e32 v201, v112, v201\n"
    "v_exp_f32_e32 v199, v199\n"
    "v_add_f32_e32 v125, v196, v197\n"
    "v_cvt_pk_bf16_f32 v44, v196, v197\n"
    "v_add_f32_e32 v202, v112, v202\n"
    "v_exp_f32_e32 v200, v200\n"
    "v_add_f32_e32 v125, v198, v125\n"
    "v_add_f32_e32 v203, v112, v203\n"
    "v_exp_f32_e32 v201, v201\n"
    "v_add_f32_e32 v125, v199, v125\n"
    "v_cvt_pk_bf16_f32 v45, v198, v199\n"
    "v_add_f32_e32 v204, v116, v204\n"
    "v_exp_f32_e32 v202, v202\n"
    "v_add_f32_e32 v124, v200, v124\n"
    "v_add_f32_e32 v205, v116, v205\n"
    "v_exp_f32_e32 v203, v203\n"
    "v_add_f32_e32 v124, v201, v124\n"
    "v_cvt_pk_bf16_f32 v42, v200, v201\n"
    "v_add_f32_e32 v206, v116, v206\n"
    "v_exp_f32_e32 v204, v204\n"
    "v_add_f32_e32 v124, v202, v124\n"
    "v_add_f32_e32 v207, v116, v207\n"
    "v_exp_f32_e32 v205, v205\n"
    "v_add_f32_e32 v124, v203, v124\n"
    "v_cvt_pk_bf16_f32 v43, v202, v203\n"
    "v_exp_f32_e32 v206, v206\n"
    "v_add_f32_e32 v125, v204, v125\n"
    "v_exp_f32_e32 v207, v207\n"
    "v_add_f32_e32 v125, v205, v125\n"
    "v_cvt_pk_bf16_f32 v46, v204, v205\n"
    "v_add_f32_e32 v125, v206, v125\n"
    "v_add_f32_e32 v125, v207, v125\n"
    "v_cvt_pk_bf16_f32 v47, v206, v207\n"
    "s_nop 7\n"
    "s_nop 7\n"
    "s_branch .Lback_a5\n"
    ".Lrare_n3:\n"
    "s_nop 7\n"
    "s_nop 7\n"
    "v_mbcnt_lo_u32_b32 v204, -1, 0\n"
    "v_mbcnt_hi_u32_b32 v204, -1, v204\n"
    "v_and_b32_e32 v205, 15, v204\n"
    "v_lshrrev_b32_e32 v204, 4, v204\n"
    "v_lshrrev_b32_e32 v201, 1, v205\n"
    "v_xor_b32_e32 v201, v201, v204\n"
    "v_lshlrev_b32_e32 v201, 4, v201\n"
    "v_lshl_add_u32 v201, v205, 7, v201\n"
    "s_lshl_b32 s100, s17, 3\n"
    "s_add_u32 s100, s100, 0x15000\n"
    "v_add_u32_e32 v201, s100, v201\n"
    "v_xor_b32_e32 v202, 64, v201\n"
    "v_mul_u32_u24_e32 v205, 0x180, v205\n"
    "v_lshl_add_u32 v200, v204, 4, v205\n"
    "s_mul_i32 s100, s16, 1536\n"
    "s_add_u32 s100, s100, 0x12000\n"
    "buffer_load_dwordx4 v[192:195], v200, s[44:47], s100 offen\n"
    "ds_read_b128 v[196:199], v201\n"
    "s_waitcnt vmcnt(0) lgkmcnt(0)\n"
    "v_mfma_f32_16x16x32_bf16 v[240:243], v[192:195], v[196:199], 0\n"
    "s_nop 7\n"
    "buffer_load_dwordx4 v[192:195], v200, s[44:47], s100 offen offset:64\n"
    "ds_read_b128 v[196:199], v202\n"
    "s_waitcnt vmcnt(0) lgkmcnt(0)\n"
    "v_mfma_f32_16x16x32_bf16 v[240:243], v[192:195], v[196:199], v[240:243]\n"
    "s_nop 7\n"
    "buffer_load_dwordx4 v[192:195], v200, s[44:47], s100 offen offset:128\n"
    "ds_read_b128 v[196:199], v201 offset:4096\n"
    "s_waitcnt vmcnt(0) lgkmcnt(0)\n"
    "v_mfma_f32_16x16x32_bf16 v[240:243], v[192:195], v[196:199], v[240:243]\n"
    "s_nop 7\n"
    "buffer_load_dwordx4 v[192:195], v200, s[44:47], s100 offen offset:192\n"
    "ds_read_b128 v[196:199], v202 offset:4096\n"
    "s_waitcnt vmcnt(0) lgkmcnt(0)\n"
    "v_mfma_f32_16x16x32_bf16 v[240:243], v[192:195], v[196:199], v[240:243]\n"
    "s_nop 7\n"
    "buffer_load_dwordx4 v[192:195], v200, s[44:47], s100 offen offset:256\n"
    "ds_read_b128 v[196:199], v201 offset:8192\n"
    "s_waitcnt vmcnt(0) lgkmcnt(0)\n"
    "v_mfma_f32_16x16x32_bf16 v[240:243], v[192:195], v[196:199], v[240:243]\n"
    "s_nop 7\n"
    "buffer_load_dwordx4 v[192:195], v200, s[44:47], s100 offen offset:320\n"
    "ds_read_b128 v[196:199], v202 offset:8192\n"
    "s_waitcnt vmcnt(0) lgkmcnt(0)\n"
    "v_mfma_f32_16x16x32_bf16 v[240:243], v[192:195], v[196:199], v[240:243]\n"
    "s_nop 7\n"
    "s_nop 7\n"
    "v_max3_f32 v204, v240, v241, v242\n"
    "v_max_f32_e32 v204, v204, v243\n"
    "v_mov_b32_e32 v205, v204\n"
    "s_nop 1\n"
    "v_permlane16_swap_b32_e32 v204, v205\n"
    "v_max_f32_e32 v204, v204, v205\n"
    "v_mov_b32_e32 v205, v204\n"
    "s_nop 1\n"
    "v_permlane32_swap_b32_e32 v204, v205\n"
    "v_max_f32_e32 v204, v204, v205\n"
    "v_floor_f32_e64 v204, -v204\n"
    "v_min_f32_e32 v204, v204, v254\n"
    "v_sub_f32_e32 v205, v254, v204\n"
    "v_mov_b32_e32 v254, v204\n"
    "v_min_f32_e32 v205, 0x437f0000, v205\n"
    "v_cvt_u32_f32_e32 v205, v205\n"
    "v_sub_u32_e32 v204, 0, v205\n"
    "v_ldexp_f32 v255, v255, v204\n"
    "v_lshlrev_b32_e32 v205, 7, v205\n"
    "v_lshl_or_b32 v205, v205, 16, v205\n"
    "v_pk_sub_u16 v224, v224, v205 clamp\n"
    "v_pk_sub_u16 v225, v225, v205 clamp\n"
    "v_pk_sub_u16 v226, v226, v205 clamp\n"
    "v_pk_sub_u16 v227, v227, v205 clamp\n"
    "v_pk_sub_u16 v228, v228, v205 clamp\n"
    "v_pk_sub_u16 v229, v229, v205 clamp\n"
    "v_mov_b32_e32 v120, v254\n"
    "v_mov_b32_e32 v121, v254\n"
    "v_mov_b32_e32 v122, v254\n"
    "v_mov_b32_e32 v123, v254\n"
    "v_mov_b32_e32 v192, 0x14400\n"
    "v_mov_b32_e32 v193, 1\n"
    "ds_write_b32 v192, v193\n"
    "s_waitcnt lgkmcnt(0)\n"
    "v_add_f32_e32 v240, v254, v240\n"
    "v_add_f32_e32 v241, v254, v241\n"
    "v_add_f32_e32 v242, v254, v242\n"
    "v_add_f32_e32 v243, v254, v243\n"
    "v_exp_f32_e32 v240, v240\n"
    "v_exp_f32_e32 v241, v241\n"
    "v_exp_f32_e32 v242, v242\n"
    "v_exp_f32_e32 v243, v243\n"
    "v_cvt_pk_bf16_f32 v230, v240, v241\n"
    "v_add_f32_e32 v240, v240, v241\n"
    "v_cvt_pk_bf16_f32 v231, v242, v243\n"
    "v_add_f32_e32 v242, v242, v243\n"
    "v_add_f32_e32 v240, v240, v242\n"
    "s_nop 7\n"
    "s_nop 7\n"
    "s_branch .Lback_n3\n"
    ".Lrare_a6:\n"
    "s_nop 7\n"
    "s_nop 7\n"
    "v_mbcnt_lo_u32_b32 v124, -1, 0\n"
    "v_mbcnt_hi_u32_b32 v124, -1, v124\n"
    "v_and_b32_e32 v125, 15, v124\n"
    "v_mul_u32_u24_e32 v125, 0x180, v125\n"
    "v_lshrrev_b32_e32 v124, 4, v124\n"
    "v_lshl_add_u32 v52, v124, 4, v125\n"
    "s_xor_b32 s100, s18, 0\n"
    "s_mul_i32 s100, s100, 6144\n"
    "s_add_u32 s100, s100, 0x12000\n"
    "buffer_load_dwordx4 v[48:51], v52, s[44:47], s100 offen\n"
    "s_waitcnt vmcnt(0)\n"
    "s_nop 1\n"
    "v_mfma_f32_16x16x32_bf16 v[176:179], v[48:51], v[160:163], 0\n"
    "v_mfma_f32_16x16x32_bf16 v[180:183], v[48:51], v[164:167], 0\n"
    "s_nop 7\n"
    "buffer_load_dwordx4 v[48:51], v52, s[44:47], s100 offen offset:64\n"
    "s_waitcnt vmcnt(0)\n"
    "s_nop 1\n"
    "v_mfma_f32_16x16x32_bf16 v[176:179], v[48:51], v[168:171], v[176:179]\n"
    "v_mfma_f32_16x16x32_bf16 v[180:183], v[48:51], v[172:175], v[180:183]\n"
    "s_nop 7\n"
    "buffer_load_dwordx4 v[48:51], v52, s[44:47], s100 offen offset:128\n"
    "s_waitcnt vmcnt(0)\n"
    "s_nop 1\n"
    "v_mfma_f32_16x16x32_bf16 v[176:179], v[48:51], v[128:131], v[176:179]\n"
    "v_mfma_f32_16x16x32_bf16 v[180:183], v[48:51], v[132:135], v[180:183]\n"
    "s_nop 7\n"
    "buffer_load_dwordx4 v[48:51], v52, s[44:47], s100 offen offset:192\n"
    "s_waitcnt vmcnt(0)\n"
    "s_nop 1\n"
    "v_mfma_f32_16x16x32_bf16 v[176:179], v[48:51], v[136:139], v[176:179]\n"
    "v_mfma_f32_16x16x32_bf16 v[180:183], v[48:51], v[140:143], v[180:183]\n"
    "s_nop 7\n"
    "buffer_load_dwordx4 v[48:51], v52, s[44:47], s100 offen offset:256\n"
    "s_waitcnt vmcnt(0)\n"
    "s_nop 1\n"
    "v_mfma_f32_16x16x32_bf16 v[176:179], v[48:51], v[144:147], v[176:179]\n"
    "v_mfma_f32_16x16x32_bf16 v[180:183], v[48:51], v[148:151], v[180:183]\n"
    "s_nop 7\n"
    "buffer_load_dwordx4 v[48:51], v52, s[44:47], s100 offen offset:320\n"
    "s_waitcnt vmcnt(0)\n"
    "s_nop 1\n"
    "v_mfma_f32_16x16x32_bf16 v[176:179], v[48:51], v[152:155], v[176:179]\n"
    "v_mfma_f32_16x16x32_bf16 v[180:183], v[48:51], v[156:159], v[180:183]\n"
    "s_nop 7\n"
    "s_xor_b32 s100, s18, 1\n"
    "s_mul_i32 s100, s100, 6144\n"
    "s_add_u32 s100, s100, 0x12000\n"
    "buffer_load_dwordx4 v[48:51], v52, s[44:47], s100 offen\n"
    "s_waitcnt vmcnt(0)\n"
    "s_nop 1\n"
    "v_mfma_f32_16x16x32_bf16 v[184:187], v[48:51], v[160:163], 0\n"
    "v_mfma_f32_16x16x32_bf16 v[188:191], v[48:51], v[164:167], 0\n"
    "s_nop 7\n"
    "buffer_load_dwordx4 v[48:51], v52, s[44:47], s100 offen offset:64\n"
    "s_waitcnt vmcnt(0)\n"
    "s_nop 1\n"
    "v_mfma_f32_16x16x32_bf16 v[184:187], v[48:51], v[168:171], v[184:187]\n"
    "v_mfma_f32_16x16x32_bf16 v[188:191], v[48:51], v[172:175], v[188:191]\n"
    "s_nop 7\n"
    "buffer_load_dwordx4 v[48:51], v52, s[44:47], s100 offen offset:128\n"
    "s_waitcnt vmcnt(0)\n"
    "s_nop 1\n"
    "v_mfma_f32_16x16x32_bf16 v[184:187], v[48:51], v[128:131], v[184:187]\n"
    "v_mfma_f32_16x16x32_bf16 v[188:191], v[48:51], v[132:135], v[188:191]\n"
    "s_nop 7\n"
    "buffer_load_dwordx4 v[48:51], v52, s[44:47], s100 offen offset:192\n"
    "s_waitcnt vmcnt(0)\n"
    "s_nop 1\n"
    "v_mfma_f32_16x16x32_bf16 v[184:187], v[48:51], v[136:139], v[184:187]\n"
    "v_mfma_f32_16x16x32_bf16 v[188:191], v[48:51], v[140:143], v[188:191]\n"
    "s_nop 7\n"
    "buffer_load_dwordx4 v[48:51], v52, s[44:47], s100 offen offset:256\n"
    "s_waitcnt vmcnt(0)\n"
    "s_nop 1\n"
    "v_mfma_f32_16x16x32_bf16 v[184:187], v[48:51], v[144:147], v[184:187]\n"
    "v_mfma_f32_16x16x32_bf16 v[188:191], v[48:51], v[148:151], v[188:191]\n"
    "s_nop 7\n"
    "buffer_load_dwordx4 v[48:51], v52, s[44:47], s100 offen offset:320\n"
    "s_waitcnt vmcnt(0)\n"
    "s_nop 1\n"
    "v_mfma_f32_16x16x32_bf16 v[184:187], v[48:51], v[152:155], v[184:187]\n"
    "v_mfma_f32_16x16x32_bf16 v[188:191], v[48:51], v[156:159], v[188:191]\n"
    "s_nop 7\n"
    "s_nop 7\n"
    "s_nop 7\n"
    "v_max3_f32 v124, v176, v177, v178\n"
    "v_max3_f32 v124, v124, v179, v184\n"
    "v_max3_f32 v124, v124, v185, v186\n"
    "v_max_f32_e32 v124, v124, v187\n"
    "v_max3_f32 v125, v180, v181, v182\n"
    "v_max3_f32 v125, v125, v183, v188\n"
    "v_max3_f32 v125, v125, v189, v190\n"
    "v_max_f32_e32 v125, v125, v191\n"
    "v_mov_b32_e32 v48, v124\n"
    "s_nop 1\n"
    "v_permlane16_swap_b32_e32 v124, v48\n"
    "v_max_f32_e32 v124, v124, v48\n"
    "v_mov_b32_e32 v48, v124\n"
    "s_nop 1\n"
    "v_permlane32_swap_b32_e32 v124, v48\n"
    "v_max_f32_e32 v124, v124, v48\n"
    "v_mov_b32_e32 v48, v125\n"
    "s_nop 1\n"
    "v_permlane16_swap_b32_e32 v125, v48\n"
    "v_max_f32_e32 v125, v125, v48\n"
    "v_mov_b32_e32 v48, v125\n"
    "s_nop 1\n"
    "v_permlane32_swap_b32_e32 v125, v48\n"
    "v_max_f32_e32 v125, v125, v48\n"
    "v_floor_f32_e64 v124, -v124\n"
    "v_min_f32_e32 v124, v124, v112\n"
    "v_sub_f32_e32 v48, v112, v124\n"
    "v_mov_b32_e32 v112, v124\n"
    "v_sub_f32_e32 v192, v192, v48\n"
    "v_sub_f32_e32 v193, v193, v48\n"
    "v_sub_f32_e32 v194, v194, v48\n"
    "v_sub_f32_e32 v195, v195, v48\n"
    "v_sub_f32_e32 v200, v200, v48\n"
    "v_sub_f32_e32 v201, v201, v48\n"
    "v_sub_f32_e32 v202, v202, v48\n"
    "v_sub_f32_e32 v203, v203, v48\n"
    "v_min_f32_e32 v48, 0x437f0000, v48\n"
    "v_cvt_u32_f32_e32 v48, v48\n"
    "v_sub_u32_e32 v124, 0, v48\n"
    "v_ldexp_f32 v252, v252, v124\n"
    "v_lshlrev_b32_e32 v48, 7, v48\n"
    "v_lshl_or_b32 v48, v48, 16, v48\n"
    "v_pk_sub_u16 v0, v0, v48 clamp\n"
    "v_pk_sub_u16 v1, v1, v48 clamp\n"
    "v_pk_sub_u16 v2, v2, v48 clamp\n"
    "v_pk_sub_u16 v3, v3, v48 clamp\n"
    "v_pk_sub_u16 v8, v8, v48 clamp\n"
    "v_pk_sub_u16 v9, v9, v48 clamp\n"
    "v_pk_sub_u16 v10, v10, v48 clamp\n"
    "v_pk_sub_u16 v11, v11, v48 clamp\n"
    "v_pk_sub_u16 v16, v16, v48 clamp\n"
    "v_pk_sub_u16 v17, v17, v48 clamp\n"
    "v_pk_sub_u16 v18, v18, v48 clamp\n"
    "v_pk_sub_u16 v19, v19, v48 clamp\n"
    "v_pk_sub_u16 v24, v24, v48 clamp\n"
    "v_pk_sub_u16 v25, v25, v48 clamp\n"
    "v_pk_sub_u16 v26, v26, v48 clamp\n"
    "v_pk_sub_u16 v27, v27, v48 clamp\n"
    "v_pk_sub_u16 v32, v32, v48 clamp\n"
    "v_pk_sub_u16 v33, v33, v48 clamp\n"
    "v_pk_sub_u16 v34, v34, v48 clamp\n"
    "v_pk_sub_u16 v35, v35, v48 clamp\n"
    "v_pk_sub_u16 v40, v40, v48 clamp\n"
    "v_pk_sub_u16 v41, v41, v48 clamp\n"
    "v_pk_sub_u16 v42, v42, v48 clamp\n"
    "v_pk_sub_u16 v43, v43, v48 clamp\n"
    "v_floor_f32_e64 v125, -v125\n"
    "v_min_f32_e32 v125, v125, v116\n"
    "v_sub_f32_e32 v48, v116, v125\n"
    "v_mov_b32_e32 v116, v125\n"
    "v_sub_f32_e32 v196, v196, v48\n"
    "v_sub_f32_e32 v197, v197, v48\n"
    "v_sub_f32_e32 v198, v198, v48\n"
    "v_sub_f32_e32 v199, v199, v48\n"
    "v_sub_f32_e32 v204, v204, v48\n"
    "v_sub_f32_e32 v205, v205, v48\n"
    "v_sub_f32_e32 v206, v206, v48\n"
    "v_sub_f32_e32 v207, v207, v48\n"
    "v_min_f32_e32 v48, 0x437f0000, v48\n"
    "v_cvt_u32_f32_e32 v48, v48\n"
    "v_sub_u32_e32 v125, 0, v48\n"
    "v_ldexp_f32 v253, v253, v125\n"
    "v_lshlrev_b32_e32 v48, 7, v48\n"
    "v_lshl_or_b32 v48, v48, 16, v48\n"
    "v_pk_sub_u16 v4, v4, v48 clamp\n"
    "v_pk_sub_u16 v5, v5, v48 clamp\n"
    "v_pk_sub_u16 v6, v6, v48 clamp\n"
    "v_pk_sub_u16 v7, v7, v48 clamp\n"
    "v_pk_sub_u16 v12, v12, v48 clamp\n"
    "v_pk_sub_u16 v13, v13, v48 clamp\n"
    "v_pk_sub_u16 v14, v14, v48 clamp\n"
    "v_pk_sub_u16 v15, v15, v48 clamp\n"
    "v_pk_sub_u16 v20, v20, v48 clamp\n"
    "v_pk_sub_u16 v21, v21, v48 clamp\n"
    "v_pk_sub_u16 v22, v22, v48 clamp\n"
    "v_pk_sub_u16 v23, v23, v48 clamp\n"
    "v_pk_sub_u16 v28, v28, v48 clamp\n"
    "v_pk_sub_u16 v29, v29, v48 clamp\n"
    "v_pk_sub_u16 v30, v30, v48 clamp\n"
    "v_pk_sub_u16 v31, v31, v48 clamp\n"
    "v_pk_sub_u16 v36, v36, v48 clamp\n"
    "v_pk_sub_u16 v37, v37, v48 clamp\n"
    "v_pk_sub_u16 v38, v38, v48 clamp\n"
    "v_pk_sub_u16 v39, v39, v48 clamp\n"
    "v_pk_sub_u16 v44, v44, v48 clamp\n"
    "v_pk_sub_u16 v45, v45, v48 clamp\n"
    "v_pk_sub_u16 v46, v46, v48 clamp\n"
    "v_pk_sub_u16 v47, v47, v48 clamp\n"
    "v_mov_b32_e32 v113, v112\n"
    "v_mov_b32_e32 v114, v112\n"
    "v_mov_b32_e32 v115, v112\n"
    "v_mov_b32_e32 v117, v116\n"
    "v_mov_b32_e32 v118, v116\n"
    "v_mov_b32_e32 v119, v116\n"
    "v_add_f32_e32 v176, v112, v176\n"
    "v_add_f32_e32 v177, v112, v177\n"
    "v_add_f32_e32 v178, v112, v178\n"
    "v_exp_f32_e32 v176, v176\n"
    "v_add_f32_e32 v179, v112, v179\n"
    "v_exp_f32_e32 v177, v177\n"
    "v_add_f32_e32 v180, v116, v180\n"
    "v_exp_f32_e32 v178, v178\n"
    "v_add_f32_e32 v181, v116, v181\n"
    "v_exp_f32_e32 v179, v179\n"
    "v_add_f32_e32 v124, v176, v177\n"
    "v_cvt_pk_bf16_f32 v48, v176, v177\n"
    "v_add_f32_e32 v182, v116, v182\n"
    "v_exp_f32_e32 v180, v180\n"
    "v_add_f32_e32 v124, v178, v124\n"
    "v_add_f32_e32 v183, v116, v183\n"
    "v_exp_f32_e32 v181, v181\n"
    "v_add_f32_e32 v124, v179, v124\n"
    "v_cvt_pk_bf16_f32 v49, v178, v179\n"
    "v_add_f32_e32 v184, v112, v184\n"
    "v_exp_f32_e32 v182, v182\n"
    "v_add_f32_e32 v185, v112, v185\n"
    "v_exp_f32_e32 v183, v183\n"
    "v_add_f32_e32 v125, v180, v181\n"
    "v_cvt_pk_bf16_f32 v52, v180, v181\n"
    "v_add_f32_e32 v186, v112, v186\n"
    "v_exp_f32_e32 v184, v184\n"
    "v_add_f32_e32 v125, v182, v125\n"
    "v_add_f32_e32 v187, v112, v187\n"
    "v_exp_f32_e32 v185, v185\n"
    "v_add_f32_e32 v125, v183, v125\n"
    "v_cvt_pk_bf16_f32 v53, v182, v183\n"
    "v_add_f32_e32 v188, v116, v188\n"
    "v_exp_f32_e32 v186, v186\n"
    "v_add_f32_e32 v124, v184, v124\n"
    "v_add_f32_e32 v189, v116, v189\n"
    "v_exp_f32_e32 v187, v187\n"
    "v_add_f32_e32 v124, v185, v124\n"
    "v_cvt_pk_bf16_f32 v50, v184, v185\n"
    "v_add_f32_e32 v190, v116, v190\n"
    "v_exp_f32_e32 v188, v188\n"
    "v_add_f32_e32 v124, v186, v124\n"
    "v_add_f32_e32 v191, v116, v191\n"
    "v_exp_f32_e32 v189, v189\n"
    "v_add_f32_e32 v124, v187, v124\n"
    "v_cvt_pk_bf16_f32 v51, v186, v187\n"
    "v_exp_f32_e32 v190, v190\n"
    "v_add_f32_e32 v125, v188, v125\n"
    "v_exp_f32_e32 v191, v191\n"
    "v_add_f32_e32 v125, v189, v125\n"
    "v_cvt_pk_bf16_f32 v54, v188, v189\n"
    "v_add_f32_e32 v125, v190, v125\n"
    "v_add_f32_e32 v125, v191, v125\n"
    "v_cvt_pk_bf16_f32 v55, v190, v191\n"
    "s_nop 7\n"
    "s_nop 7\n"
    "s_branch .Lback_a6\n"
    ".Lrare_a7:\n"
    "s_nop 7\n"
    "s_nop 7\n"
    "v_mbcnt_lo_u32_b32 v124, -1, 0\n"
    "v_mbcnt_hi_u32_b32 v124, -1, v124\n"
    "v_and_b32_e32 v125, 15, v124\n"
    "v_mul_u32_u24_e32 v125, 0x180, v125\n"
    "v_lshrrev_b32_e32 v124, 4, v124\n"
    "v_lshl_add_u32 v60, v124, 4, v125\n"
    "s_xor_b32 s100, s18, 2\n"
    "s_mul_i32 s100, s100, 6144\n"
    "s_add_u32 s100, s100, 0x12000\n"
    "buffer_load_dwordx4 v[56:59], v60, s[44:47], s100 offen\n"
    "s_waitcnt vmcnt(0)\n"
    "s_nop 1\n"
    "v_mfma_f32_16x16x32_bf16 v[192:195], v[56:59], v[160:163], 0\n"
    "v_mfma_f32_16x16x32_bf16 v[196:199], v[56:59], v[164:167], 0\n"
    "s_nop 7\n"
    "buffer_load_dwordx4 v[56:59], v60, s[44:47], s100 offen offset:64\n"
    "s_waitcnt vmcnt(0)\n"
    "s_nop 1\n"
    "v_mfma_f32_16x16x32_bf16 v[192:195], v[56:59], v[168:171], v[192:195]\n"
    "v_mfma_f32_16x16x32_bf16 v[196:199], v[56:59], v[172:175], v[196:199]\n"
    "s_nop 7\n"
    "buffer_load_dwordx4 v[56:59], v60, s[44:47], s100 offen offset:128\n"
    "s_waitcnt vmcnt(0)\n"
    "s_nop 1\n"
    "v_mfma_f32_16x16x32_bf16 v[192:195], v[56:59], v[128:131], v[192:195]\n"
    "v_mfma_f32_16x16x32_bf16 v[196:199], v[56:59], v[132:135], v[196:199]\n"
    "s_nop 7\n"
    "buffer_load_dwordx4 v[56:59], v60, s[44:47], s100 offen offset:192\n"
    "s_waitcnt vmcnt(0)\n"
    "s_nop 1\n"
    "v_mfma_f32_16x16x32_bf16 v[192:195], v[56:59], v[136:139], v[192:195]\n"
    "v_mfma_f32_16x16x32_bf16 v[196:199], v[56:59], v[140:143], v[196:199]\n"
    "s_nop 7\n"
    "buffer_load_dwordx4 v[56:59], v60, s[44:47], s100 offen offset:256\n"
    "s_waitcnt vmcnt(0)\n"
    "s_nop 1\n"
    "v_mfma_f32_16x16x32_bf16 v[192:195], v[56:59], v[144:147], v[192:195]\n"
    "v_mfma_f32_16x16x32_bf16 v[196:199], v[56:59], v[148:151], v[196:199]\n"
    "s_nop 7\n"
    "buffer_load_dwordx4 v[56:59], v60, s[44:47], s100 offen offset:320\n"
    "s_waitcnt vmcnt(0)\n"
    "s_nop 1\n"
    "v_mfma_f32_16x16x32_bf16 v[192:195], v[56:59], v[152:155], v[192:195]\n"
    "v_mfma_f32_16x16x32_bf16 v[196:199], v[56:59], v[156:159], v[196:199]\n"
    "s_nop 7\n"
    "s_xor_b32 s100, s18, 3\n"
    "s_mul_i32 s100, s100, 6144\n"
    "s_add_u32 s100, s100, 0x12000\n"
    "buffer_load_dwordx4 v[56:59], v60, s[44:47], s100 offen\n"
    "s_waitcnt vmcnt(0)\n"
    "s_nop 1\n"
    "v_mfma_f32_16x16x32_bf16 v[200:203], v[56:59], v[160:163], 0\n"
    "v_mfma_f32_16x16x32_bf16 v[204:207], v[56:59], v[164:167], 0\n"
    "s_nop 7\n"
    "buffer_load_dwordx4 v[56:59], v60, s[44:47], s100 offen offset:64\n"
    "s_waitcnt vmcnt(0)\n"
    "s_nop 1\n"
    "v_mfma_f32_16x16x32_bf16 v[200:203], v[56:59], v[168:171], v[200:203]\n"
    "v_mfma_f32_16x16x32_bf16 v[204:207], v[56:59], v[172:175], v[204:207]\n"
    "s_nop 7\n"
    "buffer_load_dwordx4 v[56:59], v60, s[44:47], s100 offen offset:128\n"
    "s_waitcnt vmcnt(0)\n"
    "s_nop 1\n"
    "v_mfma_f32_16x16x32_bf16 v[200:203], v[56:59], v[128:131], v[200:203]\n"
    "v_mfma_f32_16x16x32_bf16 v[204:207], v[56:59], v[132:135], v[204:207]\n"
    "s_nop 7\n"
    "buffer_load_dwordx4 v[56:59], v60, s[44:47], s100 offen offset:192\n"
    "s_waitcnt vmcnt(0)\n"
    "s_nop 1\n"
    "v_mfma_f32_16x16x32_bf16 v[200:203], v[56:59], v[136:139], v[200:203]\n"
    "v_mfma_f32_16x16x32_bf16 v[204:207], v[56:59], v[140:143], v[204:207]\n"
    "s_nop 7\n"
    "buffer_load_dwordx4 v[56:59], v60, s[44:47], s100 offen offset:256\n"
    "s_waitcnt vmcnt(0)\n"
    "s_nop 1\n"
    "v_mfma_f32_16x16x32_bf16 v[200:203], v[56:59], v[144:147], v[200:203]\n"
    "v_mfma_f32_16x16x32_bf16 v[204:207], v[56:59], v[148:151], v[204:207]\n"
    "s_nop 7\n"
    "buffer_load_dwordx4 v[56:59], v60, s[44:47], s100 offen offset:320\n"
    "s_waitcnt vmcnt(0)\n"
    "s_nop 1\n"
    "v_mfma_f32_16x16x32_bf16 v[200:203], v[56:59], v[152:155], v[200:203]\n"
    "v_mfma_f32_16x16x32_bf16 v[204:207], v[56:59], v[156:159], v[204:207]\n"
    "s_nop 7\n"
    "s_nop 7\n"
    "s_nop 7\n"
    "v_max3_f32 v124, v192, v193, v194\n"
    "v_max3_f32 v124, v124, v195, v200\n"
    "v_max3_f32 v124, v124, v201, v202\n"
    "v_max_f32_e32 v124, v124, v203\n"
    "v_max3_f32 v125, v196, v197, v198\n"
    "v_max3_f32 v125, v125, v199, v204\n"
    "v_max3_f32 v125, v125, v205, v206\n"
    "v_max_f32_e32 v125, v125, v207\n"
    "v_mov_b32_e32 v56, v124\n"
    "s_nop 1\n"
    "v_permlane16_swap_b32_e32 v124, v56\n"
    "v_max_f32_e32 v124, v124, v56\n"
    "v_mov_b32_e32 v56, v124\n"
    "s_nop 1\n"
    "v_permlane32_swap_b32_e32 v124, v56\n"
    "v_max_f32_e32 v124, v124, v56\n"
    "v_mov_b32_e32 v56, v125\n"
    "s_nop 1\n"
    "v_permlane16_swap_b32_e32 v125, v56\n"
    "v_max_f32_e32 v125, v125, v56\n"
    "v_mov_b32_e32 v56, v125\n"
    "s_nop 1\n"
    "v_permlane32_swap_b32_e32 v125, v56\n"
    "v_max_f32_e32 v125, v125, v56\n"
    "v_floor_f32_e64 v124, -v124\n"
    "v_min_f32_e32 v124, v124, v112\n"
    "v_sub_f32_e32 v56, v112, v124\n"
    "v_mov_b32_e32 v112, v124\n"
    "v_sub_f32_e32 v176, v176, v56\n"
    "v_sub_f32_e32 v177, v177, v56\n"
    "v_sub_f32_e32 v178, v178, v56\n"
    "v_sub_f32_e32 v179, v179, v56\n"
    "v_sub_f32_e32 v184, v184, v56\n"
    "v_sub_f32_e32 v185, v185, v56\n"
    "v_sub_f32_e32 v186, v186, v56\n"
    "v_sub_f32_e32 v187, v187, v56\n"
    "v_min_f32_e32 v56, 0x437f0000, v56\n"
    "v_cvt_u32_f32_e32 v56, v56\n"
    "v_sub_u32_e32 v124, 0, v56\n"
    "v_ldexp_f32 v252, v252, v124\n"
    "v_lshlrev_b32_e32 v56, 7, v56\n"
    "v_lshl_or_b32 v56, v56, 16, v56\n"
    "v_pk_sub_u16 v0, v0, v56 clamp\n"
    "v_pk_sub_u16 v1, v1, v56 clamp\n"
    "v_pk_sub_u16 v2, v2, v56 clamp\n"
    "v_pk_sub_u16 v3, v3, v56 clamp\n"
    "v_pk_sub_u16 v8, v8, v56 clamp\n"
    "v_pk_sub_u16 v9, v9, v56 clamp\n"
    "v_pk_sub_u16 v10, v10, v56 clamp\n"
    "v_pk_sub_u16 v11, v11, v56 clamp\n"
    "v_pk_sub_u16 v16, v16, v56 clamp\n"
    "v_pk_sub_u16 v17, v17, v56 clamp\n"
    "v_pk_sub_u16 v18, v18, v56 clamp\n"
    "v_pk_sub_u16 v19, v19, v56 clamp\n"
    "v_pk_sub_u16 v24, v24, v56 clamp\n"
    "v_pk_sub_u16 v25, v25, v56 clamp\n"
    "v_pk_sub_u16 v26, v26, v56 clamp\n"
    "v_pk_sub_u16 v27, v27, v56 clamp\n"
    "v_pk_sub_u16 v32, v32, v56 clamp\n"
    "v_pk_sub_u16 v33, v33, v56 clamp\n"
    "v_pk_sub_u16 v34, v34, v56 clamp\n"
    "v_pk_sub_u16 v35, v35, v56 clamp\n"
    "v_pk_sub_u16 v40, v40, v56 clamp\n"
    "v_pk_sub_u16 v41, v41, v56 clamp\n"
    "v_pk_sub_u16 v42, v42, v56 clamp\n"
    "v_pk_sub_u16 v43, v43, v56 clamp\n"
    "v_pk_sub_u16 v48, v48, v56 clamp\n"
    "v_pk_sub_u16 v49, v49, v56 clamp\n"
    "v_pk_sub_u16 v50, v50, v56 clamp\n"
    "v_pk_sub_u16 v51, v51, v56 clamp\n"
    "v_floor_f32_e64 v125, -v125\n"
    "v_min_f32_e32 v125, v125, v116\n"
    "v_sub_f32_e32 v56, v116, v125\n"
    "v_mov_b32_e32 v116, v125\n"
    "v_sub_f32_e32 v180, v180, v56\n"
    "v_sub_f32_e32 v181, v181, v56\n"
    "v_sub_f32_e32 v182, v182, v56\n"
    "v_sub_f32_e32 v183, v183, v56\n"
    "v_sub_f32_e32 v188, v188, v56\n"
    "v_sub_f32_e32 v189, v189, v56\n"
    "v_sub_f32_e32 v190, v190, v56\n"
    "v_sub_f32_e32 v191, v191, v56\n"
    "v_min_f32_e32 v56, 0x437f0000, v56\n"
    "v_cvt_u32_f32_e32 v56, v56\n"
    "v_sub_u32_e32 v125, 0, v56\n"
    "v_ldexp_f32 v253, v253, v125\n"
    "v_lshlrev_b32_e32 v56, 7, v56\n"
    "v_lshl_or_b32 v56, v56, 16, v56\n"
    "v_pk_sub_u16 v4, v4, v56 clamp\n"
    "v_pk_sub_u16 v5, v5, v56 clamp\n"
    "v_pk_sub_u16 v6, v6, v56 clamp\n"
    "v_pk_sub_u16 v7, v7, v56 clamp\n"
    "v_pk_sub_u16 v12, v12, v56 clamp\n"
    "v_pk_sub_u16 v13, v13, v56 clamp\n"
    "v_pk_sub_u16 v14, v14, v56 clamp\n"
    "v_pk_sub_u16 v15, v15, v56 clamp\n"
    "v_pk_sub_u16 v20, v20, v56 clamp\n"
    "v_pk_sub_u16 v21, v21, v56 clamp\n"
    "v_pk_sub_u16 v22, v22, v56 clamp\n"
    "v_pk_sub_u16 v23, v23, v56 clamp\n"
    "v_pk_sub_u16 v28, v28, v56 clamp\n"
    "v_pk_sub_u16 v29, v29, v56 clamp\n"
    "v_pk_sub_u16 v30, v30, v56 clamp\n"
    "v_pk_sub_u16 v31, v31, v56 clamp\n"
    "v_pk_sub_u16 v36, v36, v56 clamp\n"
    "v_pk_sub_u16 v37, v37, v56 clamp\n"
    "v_pk_sub_u16 v38, v38, v56 clamp\n"
    "v_pk_sub_u16 v39, v39, v56 clamp\n"
    "v_pk_sub_u16 v44, v44, v56 clamp\n"
    "v_pk_sub_u16 v45, v45, v56 clamp\n"
    "v_pk_sub_u16 v46, v46, v56 clamp\n"
    "v_pk_sub_u16 v47, v47, v56 clamp\n"
    "v_pk_sub_u16 v52, v52, v56 clamp\n"
    "v_pk_sub_u16 v53, v53, v56 clamp\n"
    "v_pk_sub_u16 v54, v54, v56 clamp\n"
    "v_pk_sub_u16 v55, v55, v56 clamp\n"
    "v_mov_b32_e32 v113, v112\n"
    "v_mov_b32_e32 v114, v112\n"
    "v_mov_b32_e32 v115, v112\n"
    "v_mov_b32_e32 v117, v116\n"
    "v_mov_b32_e32 v118, v116\n"
    "v_mov_b32_e32 v119, v116\n"
    "v_add_f32_e32 v192, v112, v192\n"
    "v_add_f32_e32 v193, v112, v193\n"
    "v_add_f32_e32 v194, v112, v194\n"
    "v_exp_f32_e32 v192, v192\n"
    "v_add_f32_e32 v195, v112, v195\n"
    "v_exp_f32_e32 v193, v193\n"
    "v_add_f32_e32 v196, v116, v196\n"
    "v_exp_f32_e32 v194, v194\n"
    "v_add_f32_e32 v197, v116, v197\n"
    "v_exp_f32_e32 v195, v195\n"
    "v_add_f32_e32 v124, v192, v193\n"
    "v_cvt_pk_bf16_f32 v56, v192, v193\n"
    "v_add_f32_e32 v198, v116, v198\n"
    "v_exp_f32_e32 v196, v196\n"
    "v_add_f32_e32 v124, v194, v124\n"
    "v_add_f32_e32 v199, v116, v199\n"
    "v_exp_f32_e32 v197, v197\n"
    "v_add_f32_e32 v124, v195, v124\n"
    "v_cvt_pk_bf16_f32 v57, v194, v195\n"
    "v_add_f32_e32 v200, v112, v200\n"
    "v_exp_f32_e32 v198, v198\n"
    "v_add_f32_e32 v201, v112, v201\n"
    "v_exp_f32_e32 v199, v199\n"
    "v_add_f32_e32 v125, v196, v197\n"
    "v_cvt_pk_bf16_f32 v60, v196, v197\n"
    "v_add_f32_e32 v202, v112, v202\n"
    "v_exp_f32_e32 v200, v200\n"
    "v_add_f32_e32 v125, v198, v125\n"
    "v_add_f32_e32 v203, v112, v203\n"
    "v_exp_f32_e32 v201, v201\n"
    "v_add_f32_e32 v125, v199, v125\n"
    "v_cvt_pk_bf16_f32 v61, v198, v199\n"
    "v_add_f32_e32 v204, v116, v204\n"
    "v_exp_f32_e32 v202, v202\n"
    "v_add_f32_e32 v124, v200, v124\n"
    "v_add_f32_e32 v205, v116, v205\n"
    "v_exp_f32_e32 v203, v203\n"
    "v_add_f32_e32 v124, v201, v124\n"
    "v_cvt_pk_bf16_f32 v58, v200, v201\n"
    "v_add_f32_e32 v206, v116, v206\n"
    "v_exp_f32_e32 v204, v204\n"
    "v_add_f32_e32 v124, v202, v124\n"
    "v_add_f32_e32 v207, v116, v207\n"
    "v_exp_f32_e32 v205, v205\n"
    "v_add_f32_e32 v124, v203, v124\n"
    "v_cvt_pk_bf16_f32 v59, v202, v203\n"
    "v_exp_f32_e32 v206, v206\n"
    "v_add_f32_e32 v125, v204, v125\n"
    "v_exp_f32_e32 v207, v207\n"
    "v_add_f32_e32 v125, v205, v125\n"
    "v_cvt_pk_bf16_f32 v62, v204, v205\n"
    "v_add_f32_e32 v125, v206, v125\n"
    "v_add_f32_e32 v125, v207, v125\n"
    "v_cvt_pk_bf16_f32 v63, v206, v207\n"
    "s_nop 7\n"
    "s_nop 7\n"
    "s_branch .Lback_a7\n"
    ".Lrare_n4:\n"
    "s_nop 7\n"
    "s_nop 7\n"
    "v_mbcnt_lo_u32_b32 v204, -1, 0\n"
    "v_mbcnt_hi_u32_b32 v204, -1, v204\n"
    "v_and_b32_e32 v205, 15, v204\n"
    "v_lshrrev_b32_e32 v204, 4, v204\n"
    "v_lshrrev_b32_e32 v201, 1, v205\n"
    "v_xor_b32_e32 v201, v201, v204\n"
    "v_lshlrev_b32_e32 v201, 4, v201\n"
    "v_lshl_add_u32 v201, v205, 7, v201\n"
    "s_lshl_b32 s100, s17, 3\n"
    "s_add_u32 s100, s100, 0x15000\n"
    "v_add_u32_e32 v201, s100, v201\n"
    "v_xor_b32_e32 v202, 64, v201\n"
    "v_mul_u32_u24_e32 v205, 0x180, v205\n"
    "v_lshl_add_u32 v200, v204, 4, v205\n"
    "s_mul_i32 s100, s16, 1536\n"
    "s_add_u32 s100, s100, 0x18000\n"
    "buffer_load_dwordx4 v[192:195], v200, s[44:47], s100 offen\n"
    "ds_read_b128 v[196:199], v201\n"
    "s_waitcnt vmcnt(0) lgkmcnt(0)\n"
    "v_mfma_f32_16x16x32_bf16 v[240:243], v[192:195], v[196:199], 0\n"
    "s_nop 7\n"
    "buffer_load_dwordx4 v[192:195], v200, s[44:47], s100 offen offset:64\n"
    "ds_read_b128 v[196:199], v202\n"
    "s_waitcnt vmcnt(0) lgkmcnt(0)\n"
    "v_mfma_f32_16x16x32_bf16 v[240:243], v[192:195], v[196:199], v[240:243]\n"
    "s_nop 7\n"
    "buffer_load_dwordx4 v[192:195], v200, s[44:47], s100 offen offset:128\n"
    "ds_read_b128 v[196:199], v201 offset:4096\n"
    "s_waitcnt vmcnt(0) lgkmcnt(0)\n"
    "v_mfma_f32_16x16x32_bf16 v[240:243], v[192:195], v[196:199], v[240:243]\n"
    "s_nop 7\n"
    "buffer_load_dwordx4 v[192:195], v200, s[44:47], s100 offen offset:192\n"
    "ds_read_b128 v[196:199], v202 offset:4096\n"
    "s_waitcnt vmcnt(0) lgkmcnt(0)\n"
    "v_mfma_f32_16x16x32_bf16 v[240:243], v[192:195], v[196:199], v[240:243]\n"
    "s_nop 7\n"
    "buffer_load_dwordx4 v[192:195], v200, s[44:47], s100 offen offset:256\n"
    "ds_read_b128 v[196:199], v201 offset:8192\n"
    "s_waitcnt vmcnt(0) lgkmcnt(0)\n"
    "v_mfma_f32_16x16x32_bf16 v[240:243], v[192:195], v[196:199], v[240:243]\n"
    "s_nop 7\n"
    "buffer_load_dwordx4 v[192:195], v200, s[44:47], s100 offen offset:320\n"
    "ds_read_b128 v[196:199], v202 offset:8192\n"
    "s_waitcnt vmcnt(0) lgkmcnt(0)\n"
    "v_mfma_f32_16x16x32_bf16 v[240:243], v[192:195], v[196:199], v[240:243]\n"
    "s_nop 7\n"
    "s_nop 7\n"
    "v_max3_f32 v204, v240, v241, v242\n"
    "v_max_f32_e32 v204, v204, v243\n"
    "v_mov_b32_e32 v205, v204\n"
    "s_nop 1\n"
    "v_permlane16_swap_b32_e32 v204, v205\n"
    "v_max_f32_e32 v204, v204, v205\n"
    "v_mov_b32_e32 v205, v204\n"
    "s_nop 1\n"
    "v_permlane32_swap_b32_e32 v204, v205\n"
    "v_max_f32_e32 v204, v204, v205\n"
    "v_floor_f32_e64 v204, -v204\n"
    "v_min_f32_e32 v204, v204, v254\n"
    "v_sub_f32_e32 v205, v254, v204\n"
    "v_mov_b32_e32 v254, v204\n"
    "v_min_f32_e32 v205, 0x437f0000, v205\n"
    "v_cvt_u32_f32_e32 v205, v205\n"
    "v_sub_u32_e32 v204, 0, v205\n"
    "v_ldexp_f32 v255, v255, v204\n"
    "v_lshlrev_b32_e32 v205, 7, v205\n"
    "v_lshl_or_b32 v205, v205, 16, v205\n"
    "v_pk_sub_u16 v224, v224, v205 clamp\n"
    "v_pk_sub_u16 v225, v225, v205 clamp\n"
    "v_pk_sub_u16 v226, v226, v205 clamp\n"
    "v_pk_sub_u16 v227, v227, v205 clamp\n"
    "v_pk_sub_u16 v228, v228, v205 clamp\n"
    "v_pk_sub_u16 v229, v229, v205 clamp\n"
    "v_pk_sub_u16 v230, v230, v205 clamp\n"
    "v_pk_sub_u16 v231, v231, v205 clamp\n"
    "v_mov_b32_e32 v120, v254\n"
    "v_mov_b32_e32 v121, v254\n"
    "v_mov_b32_e32 v122, v254\n"
    "v_mov_b32_e32 v123, v254\n"
    "v_mov_b32_e32 v192, 0x14400\n"
    "v_mov_b32_e32 v193, 1\n"
    "ds_write_b32 v192, v193\n"
    "s_waitcnt lgkmcnt(0)\n"
    "v_add_f32_e32 v240, v254, v240\n"
    "v_add_f32_e32 v241, v254, v241\n"
    "v_add_f32_e32 v242, v254, v242\n"
    "v_add_f32_e32 v243, v254, v243\n"
    "v_exp_f32_e32 v240, v240\n"
    "v_exp_f32_e32 v241, v241\n"
    "v_exp_f32_e32 v242, v242\n"
    "v_exp_f32_e32 v243, v243\n"
    "v_cvt_pk_bf16_f32 v232, v240, v241\n"
    "v_add_f32_e32 v240, v240, v241\n"
    "v_cvt_pk_bf16_f32 v233, v242, v243\n"
    "v_add_f32_e32 v242, v242, v243\n"
    "v_add_f32_e32 v240, v240, v242\n"
    "s_nop 7\n"
    "s_nop 7\n"
    "s_branch .Lback_n4\n"
    ".Lrare_a8:\n"
    "s_nop 7\n"
    "s_nop 7\n"
    "v_mbcnt_lo_u32_b32 v124, -1, 0\n"
    "v_mbcnt_hi_u32_b32 v124, -1, v124\n"
    "v_and_b32_e32 v125, 15, v124\n"
    "v_mul_u32_u24_e32 v125, 0x180, v125\n"
    "v_lshrrev_b32_e32 v124, 4, v124\n"
    "v_lshl_add_u32 v68, v124, 4, v125\n"
    "s_xor_b32 s100, s18, 0\n"
    "s_mul_i32 s100, s100, 6144\n"
    "s_add_u32 s100, s100, 0x18000\n"
    "buffer_load_dwordx4 v[64:67], v68, s[44:47], s100 offen\n"
    "s_waitcnt vmcnt(0)\n"
    "s_nop 1\n"
    "v_mfma_f32_16x16x32_bf16 v[176:179], v[64:67], v[160:163], 0\n"
    "v_mfma_f32_16x16x32_bf16 v[180:183], v[64:67], v[164:167], 0\n"
    "s_nop 7\n"
    "buffer_load_dwordx4 v[64:67], v68, s[44:47], s100 offen offset:64\n"
    "s_waitcnt vmcnt(0)\n"
    "s_nop 1\n"
    "v_mfma_f32_16x16x32_bf16 v[176:179], v[64:67], v[168:171], v[176:179]\n"
    "v_mfma_f32_16x16x32_bf16 v[180:183], v[64:67], v[172:175], v[180:183]\n"
    "s_nop 7\n"
    "buffer_load_dwordx4 v[64:67], v68, s[44:47], s100 offen offset:128\n"
    "s_waitcnt vmcnt(0)\n"
    "s_nop 1\n"
    "v_mfma_f32_16x16x32_bf16 v[176:179], v[64:67], v[128:131], v[176:179]\n"
    "v_mfma_f32_16x16x32_bf16 v[180:183], v[64:67], v[132:135], v[180:183]\n"
    "s_nop 7\n"
    "buffer_load_dwordx4 v[64:67], v68, s[44:47], s100 offen offset:192\n"
    "s_waitcnt vmcnt(0)\n"
    "s_nop 1\n"
    "v_mfma_f32_16x16x32_bf16 v[176:179], v[64:67], v[136:139], v[176:179]\n"
    "v_mfma_f32_16x16x32_bf16 v[180:183], v[64:67], v[140:143], v[180:183]\n"
    "s_nop 7\n"
    "buffer_load_dwordx4 v[64:67], v68, s[44:47], s100 offen offset:256\n"
    "s_waitcnt vmcnt(0)\n"
    "s_nop 1\n"
    "v_mfma_f32_16x16x32_bf16 v[176:179], v[64:67], v[144:147], v[176:179]\n"
    "v_mfma_f32_16x16x32_bf16 v[180:183], v[64:67], v[148:151], v[180:183]\n"
    "s_nop 7\n"
    "buffer_load_dwordx4 v[64:67], v68, s[44:47], s100 offen offset:320\n"
    "s_waitcnt vmcnt(0)\n"
    "s_nop 1\n"
    "v_mfma_f32_16x16x32_bf16 v[176:179], v[64:67], v[152:155], v[176:179]\n"
    "v_mfma_f32_16x16x32_bf16 v[180:183], v[64:67], v[156:159], v[180:183]\n"
    "s_nop 7\n"
    "s_xor_b32 s100, s18, 1\n"
    "s_mul_i32 s100, s100, 6144\n"
    "s_add_u32 s100, s100, 0x18000\n"
    "buffer_load_dwordx4 v[64:67], v68, s[44:47], s100 offen\n"
    "s_waitcnt vmcnt(0)\n"
    "s_nop 1\n"
    "v_mfma_f32_16x16x32_bf16 v[184:187], v[64:67], v[160:163], 0\n"
    "v_mfma_f32_16x16x32_bf16 v[188:191], v[64:67], v[164:167], 0\n"
    "s_nop 7\n"
    "buffer_load_dwordx4 v[64:67], v68, s[44:47], s100 offen offset:64\n"
    "s_waitcnt vmcnt(0)\n"
    "s_nop 1\n"
    "v_mfma_f32_16x16x32_bf16 v[184:187], v[64:67], v[168:171], v[184:187]\n"
    "v_mfma_f32_16x16x32_bf16 v[188:191], v[64:67], v[172:175], v[188:191]\n"
    "s_nop 7\n"
    "buffer_load_dwordx4 v[64:67], v68, s[44:47], s100 offen offset:128\n"
    "s_waitcnt vmcnt(0)\n"
    "s_nop 1\n"
    "v_mfma_f32_16x16x32_bf16 v[184:187], v[64:67], v[128:131], v[184:187]\n"
    "v_mfma_f32_16x16x32_bf16 v[188:191], v[64:67], v[132:135], v[188:191]\n"
    "s_nop 7\n"
    "buffer_load_dwordx4 v[64:67], v68, s[44:47], s100 offen offset:192\n"
    "s_waitcnt vmcnt(0)\n"
    "s_nop 1\n"
    "v_mfma_f32_16x16x32_bf16 v[184:187], v[64:67], v[136:139], v[184:187]\n"
    "v_mfma_f32_16x16x32_bf16 v[188:191], v[64:67], v[140:143], v[188:191]\n"
    "s_nop 7\n"
    "buffer_load_dwordx4 v[64:67], v68, s[44:47], s100 offen offset:256\n"
    "s_waitcnt vmcnt(0)\n"
    "s_nop 1\n"
    "v_mfma_f32_16x16x32_bf16 v[184:187], v[64:67], v[144:147], v[184:187]\n"
    "v_mfma_f32_16x16x32_bf16 v[188:191], v[64:67], v[148:151], v[188:191]\n"
    "s_nop 7\n"
    "buffer_load_dwordx4 v[64:67], v68, s[44:47], s100 offen offset:320\n"
    "s_waitcnt vmcnt(0)\n"
    "s_nop 1\n"
    "v_mfma_f32_16x16x32_bf16 v[184:187], v[64:67], v[152:155], v[184:187]\n"
    "v_mfma_f32_16x16x32_bf16 v[188:191], v[64:67], v[156:159], v[188:191]\n"
    "s_nop 7\n"
    "s_nop 7\n"
    "s_nop 7\n"
    "v_max3_f32 v124, v176, v177, v178\n"
    "v_max3_f32 v124, v124, v179, v184\n"
    "v_max3_f32 v124, v124, v185, v186\n"
    "v_max_f32_e32 v124, v124, v187\n"
    "v_max3_f32 v125, v180, v181, v182\n"
    "v_max3_f32 v125, v125, v183, v188\n"
    "v_max3_f32 v125, v125, v189, v190\n"
    "v_max_f32_e32 v125, v125, v191\n"
    "v_mov_b32_e32 v64, v124\n"
    "s_nop 1\n"
    "v_permlane16_swap_b32_e32 v124, v64\n"
    "v_max_f32_e32 v124, v124, v64\n"
    "v_mov_b32_e32 v64, v124\n"
    "s_nop 1\n"
    "v_permlane32_swap_b32_e32 v124, v64\n"
    "v_max_f32_e32 v124, v124, v64\n"
    "v_mov_b32_e32 v64, v125\n"
    "s_nop 1\n"
    "v_permlane16_swap_b32_e32 v125, v64\n"
    "v_max_f32_e32 v125, v125, v64\n"
    "v_mov_b32_e32 v64, v125\n"
    "s_nop 1\n"
    "v_permlane32_swap_b32_e32 v125, v64\n"
    "v_max_f32_e32 v125, v125, v64\n"
    "v_floor_f32_e64 v124, -v124\n"
    "v_min_f32_e32 v124, v124, v112\n"
    "v_sub_f32_e32 v64, v112, v124\n"
    "v_mov_b32_e32 v112, v124\n"
    "v_sub_f32_e32 v192, v192, v64\n"
    "v_sub_f32_e32 v193, v193, v64\n"
    "v_sub_f32_e32 v194, v194, v64\n"
    "v_sub_f32_e32 v195, v195, v64\n"
    "v_sub_f32_e32 v200, v200, v64\n"
    "v_sub_f32_e32 v201, v201, v64\n"
    "v_sub_f32_e32 v202, v202, v64\n"
    "v_sub_f32_e32 v203, v203, v64\n"
    "v_min_f32_e32 v64, 0x437f0000, v64\n"
    "v_cvt_u32_f32_e32 v64, v64\n"
    "v_sub_u32_e32 v124, 0, v64\n"
    "v_ldexp_f32 v252, v252, v124\n"
    "v_lshlrev_b32_e32 v64, 7, v64\n"
    "v_lshl_or_b32 v64, v64, 16, v64\n"
    "v_pk_sub_u16 v0, v0, v64 clamp\n"
    "v_pk_sub_u16 v1, v1, v64 clamp\n"
    "v_pk_sub_u16 v2, v2, v64 clamp\n"
    "v_pk_sub_u16 v3, v3, v64 clamp\n"
    "v_pk_sub_u16 v8, v8, v64 clamp\n"
    "v_pk_sub_u16 v9, v9, v64 clamp\n"
    "v_pk_sub_u16 v10, v10, v64 clamp\n"
    "v_pk_sub_u16 v11, v11, v64 clamp\n"
    "v_pk_sub_u16 v16, v16, v64 clamp\n"
    "v_pk_sub_u16 v17, v17, v64 clamp\n"
    "v_pk_sub_u16 v18, v18, v64 clamp\n"
    "v_pk_sub_u16 v19, v19, v64 clamp\n"
    "v_pk_sub_u16 v24, v24, v64 clamp\n"
    "v_pk_sub_u16 v25, v25, v64 clamp\n"
    "v_pk_sub_u16 v26, v26, v64 clamp\n"
    "v_pk_sub_u16 v27, v27, v64 clamp\n"
    "v_pk_sub_u16 v32, v32, v64 clamp\n"
    "v_pk_sub_u16 v33, v33, v64 clamp\n"
    "v_pk_sub_u16 v34, v34, v64 clamp\n"
    "v_pk_sub_u16 v35, v35, v64 clamp\n"
    "v_pk_sub_u16 v40, v40, v64 clamp\n"
    "v_pk_sub_u16 v41, v41, v64 clamp\n"
    "v_pk_sub_u16 v42, v42, v64 clamp\n"
    "v_pk_sub_u16 v43, v43, v64 clamp\n"
    "v_pk_sub_u16 v48, v48, v64 clamp\n"
    "v_pk_sub_u16 v49, v49, v64 clamp\n"
    "v_pk_sub_u16 v50, v50, v64 clamp\n"
    "v_pk_sub_u16 v51, v51, v64 clamp\n"
    "v_pk_sub_u16 v56, v56, v64 clamp\n"
    "v_pk_sub_u16 v57, v57, v64 clamp\n"
    "v_pk_sub_u16 v58, v58, v64 clamp\n"
    "v_pk_sub_u16 v59, v59, v64 clamp\n"
    "v_floor_f32_e64 v125, -v125\n"
    "v_min_f32_e32 v125, v125, v116\n"
    "v_sub_f32_e32 v64, v116, v125\n"
    "v_mov_b32_e32 v116, v125\n"
    "v_sub_f32_e32 v196, v196, v64\n"
    "v_sub_f32_e32 v197, v197, v64\n"
    "v_sub_f32_e32 v198, v198, v64\n"
    "v_sub_f32_e32 v199, v199, v64\n"
    "v_sub_f32_e32 v204, v204, v64\n"
    "v_sub_f32_e32 v205, v205, v64\n"
    "v_sub_f32_e32 v206, v206, v64\n"
    "v_sub_f32_e32 v207, v207, v64\n"
    "v_min_f32_e32 v64, 0x437f0000, v64\n"
    "v_cvt_u32_f32_e32 v64, v64\n"
    "v_sub_u32_e32 v125, 0, v64\n"
    "v_ldexp_f32 v253, v253, v125\n"
    "v_lshlrev_b32_e32 v64, 7, v64\n"
    "v_lshl_or_b32 v64, v64, 16, v64\n"
    "v_pk_sub_u16 v4, v4, v64 clamp\n"
    "v_pk_sub_u16 v5, v5, v64 clamp\n"
    "v_pk_sub_u16 v6, v6, v64 clamp\n"
    "v_pk_sub_u16 v7, v7, v64 clamp\n"
    "v_pk_sub_u16 v12, v12, v64 clamp\n"
    "v_pk_sub_u16 v13, v13, v64 clamp\n"
    "v_pk_sub_u16 v14, v14, v64 clamp\n"
    "v_pk_sub_u16 v15, v15, v64 clamp\n"
    "v_pk_sub_u16 v20, v20, v64 clamp\n"
    "v_pk_sub_u16 v21, v21, v64 clamp\n"
    "v_pk_sub_u16 v22, v22, v64 clamp\n"
    "v_pk_sub_u16 v23, v23, v64 clamp\n"
    "v_pk_sub_u16 v28, v28, v64 clamp\n"
    "v_pk_sub_u16 v29, v29, v64 clamp\n"
    "v_pk_sub_u16 v30, v30, v64 clamp\n"
    "v_pk_sub_u16 v31, v31, v64 clamp\n"
    "v_pk_sub_u16 v36, v36, v64 clamp\n"
    "v_pk_sub_u16 v37, v37, v64 clamp\n"
    "v_pk_sub_u16 v38, v38, v64 clamp\n"
    "v_pk_sub_u16 v39, v39, v64 clamp\n"
    "v_pk_sub_u16 v44, v44, v64 clamp\n"
    "v_pk_sub_u16 v45, v45, v64 clamp\n"
    "v_pk_sub_u16 v46, v46, v64 clamp\n"
    "v_pk_sub_u16 v47, v47, v64 clamp\n"
    "v_pk_sub_u16 v52, v52, v64 clamp\n"
    "v_pk_sub_u16 v53, v53, v64 clamp\n"
    "v_pk_sub_u16 v54, v54, v64 clamp\n"
    "v_pk_sub_u16 v55, v55, v64 clamp\n"
    "v_pk_sub_u16 v60, v60, v64 clamp\n"
    "v_pk_sub_u16 v61, v61, v64 clamp\n"
    "v_pk_sub_u16 v62, v62, v64 clamp\n"
    "v_pk_sub_u16 v63, v63, v64 clamp\n"
    "v_mov_b32_e32 v113, v112\n"
    "v_mov_b32_e32 v114, v112\n"
    "v_mov_b32_e32 v115, v112\n"
    "v_mov_b32_e32 v117, v116\n"
    "v_mov_b32_e32 v118, v116\n"
    "v_mov_b32_e32 v119, v116\n"
    "v_add_f32_e32 v176, v112, v176\n"
    "v_add_f32_e32 v177, v112, v177\n"
    "v_add_f32_e32 v178, v112, v178\n"
    "v_exp_f32_e32 v176, v176\n"
    "v_add_f32_e32 v179, v112, v179\n"
    "v_exp_f32_e32 v177, v177\n"
    "v_add_f32_e32 v180, v116, v180\n"
    "v_exp_f32_e32 v178, v178\n"
    "v_add_f32_e32 v181, v116, v181\n"
    "v_exp_f32_e32 v179, v179\n"
    "v_add_f32_e32 v124, v176, v177\n"
    "v_cvt_pk_bf16_f32 v64, v176, v177\n"
    "v_add_f32_e32 v182, v116, v182\n"
    "v_exp_f32_e32 v180, v180\n"
    "v_add_f32_e32 v124, v178, v124\n"
    "v_add_f32_e32 v183, v116, v183\n"
    "v_exp_f32_e32 v181, v181\n"
    "v_add_f32_e32 v124, v179, v124\n"
    "v_cvt_pk_bf16_f32 v65, v178, v179\n"
    "v_add_f32_e32 v184, v112, v184\n"
    "v_exp_f32_e32 v182, v182\n"
    "v_add_f32_e32 v185, v112, v185\n"
    "v_exp_f32_e32 v183, v183\n"
    "v_add_f32_e32 v125, v180, v181\n"
    "v_cvt_pk_bf16_f32 v68, v180, v181\n"
    "v_add_f32_e32 v186, v112, v186\n"
    "v_exp_f32_e32 v184, v184\n"
    "v_add_f32_e32 v125, v182, v125\n"
    "v_add_f32_e32 v187, v112, v187\n"
    "v_exp_f32_e32 v185, v185\n"
    "v_add_f32_e32 v125, v183, v125\n"
    "v_cvt_pk_bf16_f32 v69, v182, v183\n"
    "v_add_f32_e32 v188, v116, v188\n"
    "v_exp_f32_e32 v186, v186\n"
    "v_add_f32_e32 v124, v184, v124\n"
    "v_add_f32_e32 v189, v116, v189\n"
    "v_exp_f32_e32 v187, v187\n"
    "v_add_f32_e32 v124, v185, v124\n"
    "v_cvt_pk_bf16_f32 v66, v184, v185\n"
    "v_add_f32_e32 v190, v116, v190\n"
    "v_exp_f32_e32 v188, v188\n"
    "v_add_f32_e32 v124, v186, v124\n"
    "v_add_f32_e32 v191, v116, v191\n"
    "v_exp_f32_e32 v189, v189\n"
    "v_add_f32_e32 v124, v187, v124\n"
    "v_cvt_pk_bf16_f32 v67, v186, v187\n"
    "v_exp_f32_e32 v190, v190\n"
    "v_add_f32_e32 v125, v188, v125\n"
    "v_exp_f32_e32 v191, v191\n"
    "v_add_f32_e32 v125, v189, v125\n"
    "v_cvt_pk_bf16_f32 v70, v188, v189\n"
    "v_add_f32_e32 v125, v190, v125\n"
    "v_add_f32_e32 v125, v191, v125\n"
    "v_cvt_pk_bf16_f32 v71, v190, v191\n"
    "s_nop 7\n"
    "s_nop 7\n"
    "s_branch .Lback_a8\n"
    ".Lrare_a9:\n"
    "s_nop 7\n"
    "s_nop 7\n"
    "v_mbcnt_lo_u32_b32 v124, -1, 0\n"
    "v_mbcnt_hi_u32_b32 v124, -1, v124\n"
    "v_and_b32_e32 v125, 15, v124\n"
    "v_mul_u32_u24_e32 v125, 0x180, v125\n"
    "v_lshrrev_b32_e32 v124, 4, v124\n"
    "v_lshl_add_u32 v76, v124, 4, v125\n"
    "s_xor_b32 s100, s18, 2\n"
    "s_mul_i32 s100, s100, 6144\n"
    "s_add_u32 s100, s100, 0x18000\n"
    "buffer_load_dwordx4 v[72:75], v76, s[44:47], s100 offen\n"
    "s_waitcnt vmcnt(0)\n"
    "s_nop 1\n"
    "v_mfma_f32_16x16x32_bf16 v[192:195], v[72:75], v[160:163], 0\n"
    "v_mfma_f32_16x16x32_bf16 v[196:199], v[72:75], v[164:167], 0\n"
    "s_nop 7\n"
    "buffer_load_dwordx4 v[72:75], v76, s[44:47], s100 offen offset:64\n"
    "s_waitcnt vmcnt(0)\n"
    "s_nop 1\n"
    "v_mfma_f32_16x16x32_bf16 v[192:195], v[72:75], v[168:171], v[192:195]\n"
    "v_mfma_f32_16x16x32_bf16 v[196:199], v[72:75], v[172:175], v[196:199]\n"
    "s_nop 7\n"
    "buffer_load_dwordx4 v[72:75], v76, s[44:47], s100 offen offset:128\n"
    "s_waitcnt vmcnt(0)\n"
    "s_nop 1\n"
    "v_mfma_f32_16x16x32_bf16 v[192:195], v[72:75], v[128:131], v[192:195]\n"
    "v_mfma_f32_16x16x32_bf16 v[196:199], v[72:75], v[132:135], v[196:199]\n"
    "s_nop 7\n"
    "buffer_load_dwordx4 v[72:75], v76, s[44:47], s100 offen offset:192\n"
    "s_waitcnt vmcnt(0)\n"
    "s_nop 1\n"
    "v_mfma_f32_16x16x32_bf16 v[192:195], v[72:75], v[136:139], v[192:195]\n"
    "v_mfma_f32_16x16x32_bf16 v[196:199], v[72:75], v[140:143], v[196:199]\n"
    "s_nop 7\n"
    "buffer_load_dwordx4 v[72:75], v76, s[44:47], s100 offen offset:256\n"
    "s_waitcnt vmcnt(0)\n"
    "s_nop 1\n"
    "v_mfma_f32_16x16x32_bf16 v[192:195], v[72:75], v[144:147], v[192:195]\n"
    "v_mfma_f32_16x16x32_bf16 v[196:199], v[72:75], v[148:151], v[196:199]\n"
    "s_nop 7\n"
    "buffer_load_dwordx4 v[72:75], v76, s[44:47], s100 offen offset:320\n"
    "s_waitcnt vmcnt(0)\n"
    "s_nop 1\n"
    "v_mfma_f32_16x16x32_bf16 v[192:195], v[72:75], v[152:155], v[192:195]\n"
    "v_mfma_f32_16x16x32_bf16 v[196:199], v[72:75], v[156:159], v[196:199]\n"
    "s_nop 7\n"
    "s_xor_b32 s100, s18, 3\n"
    "s_mul_i32 s100, s100, 6144\n"
    "s_add_u32 s100, s100, 0x18000\n"
    "buffer_load_dwordx4 v[72:75], v76, s[44:47], s100 offen\n"
    "s_waitcnt vmcnt(0)\n"
    "s_nop 1\n"
    "v_mfma_f32_16x16x32_bf16 v[200:203], v[72:75], v[160:163], 0\n"
    "v_mfma_f32_16x16x32_bf16 v[204:207], v[72:75], v[164:167], 0\n"
    "s_nop 7\n"
    "buffer_load_dwordx4 v[72:75], v76, s[44:47], s100 offen offset:64\n"
    "s_waitcnt vmcnt(0)\n"
    "s_nop 1\n"
    "v_mfma_f32_16x16x32_bf16 v[200:203], v[72:75], v[168:171], v[200:203]\n"
    "v_mfma_f32_16x16x32_bf16 v[204:207], v[72:75], v[172:175], v[204:207]\n"
    "s_nop 7\n"
    "buffer_load_dwordx4 v[72:75], v76, s[44:47], s100 offen offset:128\n"
    "s_waitcnt vmcnt(0)\n"
    "s_nop 1\n"
    "v_mfma_f32_16x16x32_bf16 v[200:203], v[72:75], v[128:131], v[200:203]\n"
    "v_mfma_f32_16x16x32_bf16 v[204:207], v[72:75], v[132:135], v[204:207]\n"
    "s_nop 7\n"
    "buffer_load_dwordx4 v[72:75], v76, s[44:47], s100 offen offset:192\n"
    "s_waitcnt vmcnt(0)\n"
    "s_nop 1\n"
    "v_mfma_f32_16x16x32_bf16 v[200:203], v[72:75], v[136:139], v[200:203]\n"
    "v_mfma_f32_16x16x32_bf16 v[204:207], v[72:75], v[140:143], v[204:207]\n"
    "s_nop 7\n"
    "buffer_load_dwordx4 v[72:75], v76, s[44:47], s100 offen offset:256\n"
    "s_waitcnt vmcnt(0)\n"
    "s_nop 1\n"
    "v_mfma_f32_16x16x32_bf16 v[200:203], v[72:75], v[144:147], v[200:203]\n"
    "v_mfma_f32_16x16x32_bf16 v[204:207], v[72:75], v[148:151], v[204:207]\n"
    "s_nop 7\n"
    "buffer_load_dwordx4 v[72:75], v76, s[44:47], s100 offen offset:320\n"
    "s_waitcnt vmcnt(0)\n"
    "s_nop 1\n"
    "v_mfma_f32_16x16x32_bf16 v[200:203], v[72:75], v[152:155], v[200:203]\n"
    "v_mfma_f32_16x16x32_bf16 v[204:207], v[72:75], v[156:159], v[204:207]\n"
    "s_nop 7\n"
    "s_nop 7\n"
    "s_nop 7\n"
    "v_max3_f32 v124, v192, v193, v194\n"
    "v_max3_f32 v124, v124, v195, v200\n"
    "v_max3_f32 v124, v124, v201, v202\n"
    "v_max_f32_e32 v124, v124, v203\n"
    "v_max3_f32 v125, v196, v197, v198\n"
    "v_max3_f32 v125, v125, v199, v204\n"
    "v_max3_f32 v125, v125, v205, v206\n"
    "v_max_f32_e32 v125, v125, v207\n"
    "v_mov_b32_e32 v72, v124\n"
    "s_nop 1\n"
    "v_permlane16_swap_b32_e32 v124, v72\n"
    "v_max_f32_e32 v124, v124, v72\n"
    "v_mov_b32_e32 v72, v124\n"
    "s_nop 1\n"
    "v_permlane32_swap_b32_e32 v124, v72\n"
    "v_max_f32_e32 v124, v124, v72\n"
    "v_mov_b32_e32 v72, v125\n"
    "s_nop 1\n"
    "v_permlane16_swap_b32_e32 v125, v72\n"
    "v_max_f32_e32 v125, v125, v72\n"
    "v_mov_b32_e32 v72, v125\n"
    "s_nop 1\n"
    "v_permlane32_swap_b32_e32 v125, v72\n"
    "v_max_f32_e32 v125, v125, v72\n"
    "v_floor_f32_e64 v124, -v124\n"
    "v_min_f32_e32 v124, v124, v112\n"
    "v_sub_f32_e32 v72, v112, v124\n"
    "v_mov_b32_e32 v112, v124\n"
    "v_sub_f32_e32 v176, v176, v72\n"
    "v_sub_f32_e32 v177, v177, v72\n"
    "v_sub_f32_e32 v178, v178, v72\n"
    "v_sub_f32_e32 v179, v179, v72\n"
    "v_sub_f32_e32 v184, v184, v72\n"
    "v_sub_f32_e32 v185, v185, v72\n"
    "v_sub_f32_e32 v186, v186, v72\n"
    "v_sub_f32_e32 v187, v187, v72\n"
    "v_min_f32_e32 v72, 0x437f0000, v72\n"
    "v_cvt_u32_f32_e32 v72, v72\n"
    "v_sub_u32_e32 v124, 0, v72\n"
    "v_ldexp_f32 v252, v252, v124\n"
    "v_lshlrev_b32_e32 v72, 7, v72\n"
    "v_lshl_or_b32 v72, v72, 16, v72\n"
    "v_pk_sub_u16 v0, v0, v72 clamp\n"
    "v_pk_sub_u16 v1, v1, v72 clamp\n"
    "v_pk_sub_u16 v2, v2, v72 clamp\n"
    "v_pk_sub_u16 v3, v3, v72 clamp\n"
    "v_pk_sub_u16 v8, v8, v72 clamp\n"
    "v_pk_sub_u16 v9, v9, v72 clamp\n"
    "v_pk_sub_u16 v10, v10, v72 clamp\n"
    "v_pk_sub_u16 v11, v11, v72 clamp\n"
    "v_pk_sub_u16 v16, v16, v72 clamp\n"
    "v_pk_sub_u16 v17, v17, v72 clamp\n"
    "v_pk_sub_u16 v18, v18, v72 clamp\n"
    "v_pk_sub_u16 v19, v19, v72 clamp\n"
    "v_pk_sub_u16 v24, v24, v72 clamp\n"
    "v_pk_sub_u16 v25, v25, v72 clamp\n"
    "v_pk_sub_u16 v26, v26, v72 clamp\n"
    "v_pk_sub_u16 v27, v27, v72 clamp\n"
    "v_pk_sub_u16 v32, v32, v72 clamp\n"
    "v_pk_sub_u16 v33, v33, v72 clamp\n"
    "v_pk_sub_u16 v34, v34, v72 clamp\n"
    "v_pk_sub_u16 v35, v35, v72 clamp\n"
    "v_pk_sub_u16 v40, v40, v72 clamp\n"
    "v_pk_sub_u16 v41, v41, v72 clamp\n"
    "v_pk_sub_u16 v42, v42, v72 clamp\n"
    "v_pk_sub_u16 v43, v43, v72 clamp\n"
    "v_pk_sub_u16 v48, v48, v72 clamp\n"
    "v_pk_sub_u16 v49, v49, v72 clamp\n"
    "v_pk_sub_u16 v50, v50, v72 clamp\n"
    "v_pk_sub_u16 v51, v51, v72 clamp\n"
    "v_pk_sub_u16 v56, v56, v72 clamp\n"
    "v_pk_sub_u16 v57, v57, v72 clamp\n"
    "v_pk_sub_u16 v58, v58, v72 clamp\n"
    "v_pk_sub_u16 v59, v59, v72 clamp\n"
    "v_pk_sub_u16 v64, v64, v72 clamp\n"
    "v_pk_sub_u16 v65, v65, v72 clamp\n"
    "v_pk_sub_u16 v66, v66, v72 clamp\n"
    "v_pk_sub_u16 v67, v67, v72 clamp\n"
    "v_floor_f32_e64 v125, -v125\n"
    "v_min_f32_e32 v125, v125, v116\n"
    "v_sub_f32_e32 v72, v116, v125\n"
    "v_mov_b32_e32 v116, v125\n"
    "v_sub_f32_e32 v180, v180, v72\n"
    "v_sub_f32_e32 v181, v181, v72\n"
    "v_sub_f32_e32 v182, v182, v72\n"
    "v_sub_f32_e32 v183, v183, v72\n"
    "v_sub_f32_e32 v188, v188, v72\n"
    "v_sub_f32_e32 v189, v189, v72\n"
    "v_sub_f32_e32 v190, v190, v72\n"
    "v_sub_f32_e32 v191, v191, v72\n"
    "v_min_f32_e32 v72, 0x437f0000, v72\n"
    "v_cvt_u32_f32_e32 v72, v72\n"
    "v_sub_u32_e32 v125, 0, v72\n"
    "v_ldexp_f32 v253, v253, v125\n"
    "v_lshlrev_b32_e32 v72, 7, v72\n"
    "v_lshl_or_b32 v72, v72, 16, v72\n"
    "v_pk_sub_u16 v4, v4, v72 clamp\n"
    "v_pk_sub_u16 v5, v5, v72 clamp\n"
    "v_pk_sub_u16 v6, v6, v72 clamp\n"
    "v_pk_sub_u16 v7, v7, v72 clamp\n"
    "v_pk_sub_u16 v12, v12, v72 clamp\n"
    "v_pk_sub_u16 v13, v13, v72 clamp\n"
    "v_pk_sub_u16 v14, v14, v72 clamp\n"
    "v_pk_sub_u16 v15, v15, v72 clamp\n"
    "v_pk_sub_u16 v20, v20, v72 clamp\n"
    "v_pk_sub_u16 v21, v21, v72 clamp\n"
    "v_pk_sub_u16 v22, v22, v72 clamp\n"
    "v_pk_sub_u16 v23, v23, v72 clamp\n"
    "v_pk_sub_u16 v28, v28, v72 clamp\n"
    "v_pk_sub_u16 v29, v29, v72 clamp\n"
    "v_pk_sub_u16 v30, v30, v72 clamp\n"
    "v_pk_sub_u16 v31, v31, v72 clamp\n"
    "v_pk_sub_u16 v36, v36, v72 clamp\n"
    "v_pk_sub_u16 v37, v37, v72 clamp\n"
    "v_pk_sub_u16 v38, v38, v72 clamp\n"
    "v_pk_sub_u16 v39, v39, v72 clamp\n"
    "v_pk_sub_u16 v44, v44, v72 clamp\n"
    "v_pk_sub_u16 v45, v45, v72 clamp\n"
    "v_pk_sub_u16 v46, v46, v72 clamp\n"
    "v_pk_sub_u16 v47, v47, v72 clamp\n"
    "v_pk_sub_u16 v52, v52, v72 clamp\n"
    "v_pk_sub_u16 v53, v53, v72 clamp\n"
    "v_pk_sub_u16 v54, v54, v72 clamp\n"
    "v_pk_sub_u16 v55, v55, v72 clamp\n"
    "v_pk_sub_u16 v60, v60, v72 clamp\n"
    "v_pk_sub_u16 v61, v61, v72 clamp\n"
    "v_pk_sub_u16 v62, v62, v72 clamp\n"
    "v_pk_sub_u16 v63, v63, v72 clamp\n"
    "v_pk_sub_u16 v68, v68, v72 clamp\n"
    "v_pk_sub_u16 v69, v69, v72 clamp\n"
    "v_pk_sub_u16 v70, v70, v72 clamp\n"
    "v_pk_sub_u16 v71, v71, v72 clamp\n"
    "v_mov_b32_e32 v113, v112\n"
    "v_mov_b32_e32 v114, v112\n"
    "v_mov_b32_e32 v115, v112\n"
    "v_mov_b32_e32 v117, v116\n"
    "v_mov_b32_e32 v118, v116\n"
    "v_mov_b32_e32 v119, v116\n"
    "v_add_f32_e32 v192, v112, v192\n"
    "v_add_f32_e32 v193, v112, v193\n"
    "v_add_f32_e32 v194, v112, v194\n"
    "v_exp_f32_e32 v192, v192\n"
    "v_add_f32_e32 v195, v112, v195\n"
    "v_exp_f32_e32 v193, v193\n"
    "v_add_f32_e32 v196, v116, v196\n"
    "v_exp_f32_e32 v194, v194\n"
    "v_add_f32_e32 v197, v116, v197\n"
    "v_exp_f32_e32 v195, v195\n"
    "v_add_f32_e32 v124, v192, v193\n"
    "v_cvt_pk_bf16_f32 v72, v192, v193\n"
    "v_add_f32_e32 v198, v116, v198\n"
    "v_exp_f32_e32 v196, v196\n"
    "v_add_f32_e32 v124, v194, v124\n"
    "v_add_f32_e32 v199, v116, v199\n"
    "v_exp_f32_e32 v197, v197\n"
    "v_add_f32_e32 v124, v195, v124\n"
    "v_cvt_pk_bf16_f32 v73, v194, v195\n"
    "v_add_f32_e32 v200, v112, v200\n"
    "v_exp_f32_e32 v198, v198\n"
    "v_add_f32_e32 v201, v112, v201\n"
    "v_exp_f32_e32 v199, v199\n"
    "v_add_f32_e32 v125, v196, v197\n"
    "v_cvt_pk_bf16_f32 v76, v196, v197\n"
    "v_add_f32_e32 v202, v112, v202\n"
    "v_exp_f32_e32 v200, v200\n"
    "v_add_f32_e32 v125, v198, v125\n"
    "v_add_f32_e32 v203, v112, v203\n"
    "v_exp_f32_e32 v201, v201\n"
    "v_add_f32_e32 v125, v199, v125\n"
    "v_cvt_pk_bf16_f32 v77, v198, v199\n"
    "v_add_f32_e32 v204, v116, v204\n"
    "v_exp_f32_e32 v202, v202\n"
    "v_add_f32_e32 v124, v200, v124\n"
    "v_add_f32_e32 v205, v116, v205\n"
    "v_exp_f32_e32 v203, v203\n"
    "v_add_f32_e32 v124, v201, v124\n"
    "v_cvt_pk_bf16_f32 v74, v200, v201\n"
    "v_add_f32_e32 v206, v116, v206\n"
    "v_exp_f32_e32 v204, v204\n"
    "v_add_f32_e32 v124, v202, v124\n"
    "v_add_f32_e32 v207, v116, v207\n"
    "v_exp_f32_e32 v205, v205\n"
    "v_add_f32_e32 v124, v203, v124\n"
    "v_cvt_pk_bf16_f32 v75, v202, v203\n"
    "v_exp_f32_e32 v206, v206\n"
    "v_add_f32_e32 v125, v204, v125\n"
    "v_exp_f32_e32 v207, v207\n"
    "v_add_f32_e32 v125, v205, v125\n"
    "v_cvt_pk_bf16_f32 v78, v204, v205\n"
    "v_add_f32_e32 v125, v206, v125\n"
    "v_add_f32_e32 v125, v207, v125\n"
    "v_cvt_pk_bf16_f32 v79, v206, v207\n"
    "s_nop 7\n"
    "s_nop 7\n"
    "s_branch .Lback_a9\n"
    ".Lrare_n5:\n"
    "s_nop 7\n"
    "s_nop 7\n"
    "v_mbcnt_lo_u32_b32 v204, -1, 0\n"
    "v_mbcnt_hi_u32_b32 v204, -1, v204\n"
    "v_and_b32_e32 v205, 15, v204\n"
    "v_lshrrev_b32_e32 v204, 4, v204\n"
    "v_lshrrev_b32_e32 v201, 1, v205\n"
    "v_xor_b32_e32 v201, v201, v204\n"
    "v_lshlrev_b32_e32 v201, 4, v201\n"
    "v_lshl_add_u32 v201, v205, 7, v201\n"
    "s_lshl_b32 s100, s17, 3\n"
    "s_add_u32 s100, s100, 0x15000\n"
    "v_add_u32_e32 v201, s100, v201\n"
    "v_xor_b32_e32 v202, 64, v201\n"
    "v_mul_u32_u24_e32 v205, 0x180, v205\n"
    "v_lshl_add_u32 v200, v204, 4, v205\n"
    "s_mul_i32 s100, s16, 1536\n"
    "s_add_u32 s100, s100, 0x1e000\n"
    "buffer_load_dwordx4 v[192:195], v200, s[44:47], s100 offen\n"
    "ds_read_b128 v[196:199], v201\n"
    "s_waitcnt vmcnt(0) lgkmcnt(0)\n"
    "v_mfma_f32_16x16x32_bf16 v[240:243], v[192:195], v[196:199], 0\n"
    "s_nop 7\n"
    "buffer_load_dwordx4 v[192:195], v200, s[44:47], s100 offen offset:64\n"
    "ds_read_b128 v[196:199], v202\n"
    "s_waitcnt vmcnt(0) lgkmcnt(0)\n"
    "v_mfma_f32_16x16x32_bf16 v[240:243], v[192:195], v[196:199], v[240:243]\n"
    "s_nop 7\n"
    "buffer_load_dwordx4 v[192:195], v200, s[44:47], s100 offen offset:128\n"
    "ds_read_b128 v[196:199], v201 offset:4096\n"
    "s_waitcnt vmcnt(0) lgkmcnt(0)\n"
    "v_mfma_f32_16x16x32_bf16 v[240:243], v[192:195], v[196:199], v[240:243]\n"
    "s_nop 7\n"
    "buffer_load_dwordx4 v[192:195], v200, s[44:47], s100 offen offset:192\n"
    "ds_read_b128 v[196:199], v202 offset:4096\n"
    "s_waitcnt vmcnt(0) lgkmcnt(0)\n"
    "v_mfma_f32_16x16x32_bf16 v[240:243], v[192:195], v[196:199], v[240:243]\n"
    "s_nop 7\n"
    "buffer_load_dwordx4 v[192:195], v200, s[44:47], s100 offen offset:256\n"
    "ds_read_b128 v[196:199], v201 offset:8192\n"
    "s_waitcnt vmcnt(0) lgkmcnt(0)\n"
    "v_mfma_f32_16x16x32_bf16 v[240:243], v[192:195], v[196:199], v[240:243]\n"
    "s_nop 7\n"
    "buffer_load_dwordx4 v[192:195], v200, s[44:47], s100 offen offset:320\n"
    "ds_read_b128 v[196:199], v202 offset:8192\n"
    "s_waitcnt vmcnt(0) lgkmcnt(0)\n"
    "v_mfma_f32_16x16x32_bf16 v[240:243], v[192:195], v[196:199], v[240:243]\n"
    "s_nop 7\n"
    "s_nop 7\n"
    "v_max3_f32 v204, v240, v241, v242\n"
    "v_max_f32_e32 v204, v204, v243\n"
    "v_mov_b32_e32 v205, v204\n"
    "s_nop 1\n"
    "v_permlane16_swap_b32_e32 v204, v205\n"
    "v_max_f32_e32 v204, v204, v205\n"
    "v_mov_b32_e32 v205, v204\n"
    "s_nop 1\n"
    "v_permlane32_swap_b32_e32 v204, v205\n"
    "v_max_f32_e32 v204, v204, v205\n"
    "v_floor_f32_e64 v204, -v204\n"
    "v_min_f32_e32 v204, v204, v254\n"
    "v_sub_f32_e32 v205, v254, v204\n"
    "v_mov_b32_e32 v254, v204\n"
    "v_min_f32_e32 v205, 0x437f0000, v205\n"
    "v_cvt_u32_f32_e32 v205, v205\n"
    "v_sub_u32_e32 v204, 0, v205\n"
    "v_ldexp_f32 v255, v255, v204\n"
    "v_lshlrev_b32_e32 v205, 7, v205\n"
    "v_lshl_or_b32 v205, v205, 16, v205\n"
    "v_pk_sub_u16 v224, v224, v205 clamp\n"
    "v_pk_sub_u16 v225, v225, v205 clamp\n"
    "v_pk_sub_u16 v226, v226, v205 clamp\n"
    "v_pk_sub_u16 v227, v227, v205 clamp\n"
    "v_pk_sub_u16 v228, v228, v205 clamp\n"
    "v_pk_sub_u16 v229, v229, v205 clamp\n"
    "v_pk_sub_u16 v230, v230, v205 clamp\n"
    "v_pk_sub_u16 v231, v231, v205 clamp\n"
    "v_pk_sub_u16 v232, v232, v205 clamp\n"
    "v_pk_sub_u16 v233, v233, v205 clamp\n"
    "v_mov_b32_e32 v120, v254\n"
    "v_mov_b32_e32 v121, v254\n"
    "v_mov_b32_e32 v122, v254\n"
    "v_mov_b32_e32 v123, v254\n"
    "v_mov_b32_e32 v192, 0x14400\n"
    "v_mov_b32_e32 v193, 1\n"
    "ds_write_b32 v192, v193\n"
    "s_waitcnt lgkmcnt(0)\n"
    "v_add_f32_e32 v240, v254, v240\n"
    "v_add_f32_e32 v241, v254, v241\n"
    "v_add_f32_e32 v242, v254, v242\n"
    "v_add_f32_e32 v243, v254, v243\n"
    "v_exp_f32_e32 v240, v240\n"
    "v_exp_f32_e32 v241, v241\n"
    "v_exp_f32_e32 v242, v242\n"
    "v_exp_f32_e32 v243, v243\n"
    "v_cvt_pk_bf16_f32 v234, v240, v241\n"
    "v_add_f32_e32 v240, v240, v241\n"
    "v_cvt_pk_bf16_f32 v235, v242, v243\n"
    "v_add_f32_e32 v242, v242, v243\n"
    "v_add_f32_e32 v240, v240, v242\n"
    "s_nop 7\n"
    "s_nop 7\n"
    "s_branch .Lback_n5\n"
    ".Lrare_a10:\n"
    "s_nop 7\n"
    "s_nop 7\n"
    "v_mbcnt_lo_u32_b32 v124, -1, 0\n"
    "v_mbcnt_hi_u32_b32 v124, -1, v124\n"
    "v_and_b32_e32 v125, 15, v124\n"
    "v_mul_u32_u24_e32 v125, 0x180, v125\n"
    "v_lshrrev_b32_e32 v124, 4, v124\n"
    "v_lshl_add_u32 v84, v124, 4, v125\n"
    "s_xor_b32 s100, s18, 0\n"
    "s_mul_i32 s100, s100, 6144\n"
    "s_add_u32 s100, s100, 0x1e000\n"
    "buffer_load_dwordx4 v[80:83], v84, s[44:47], s100 offen\n"
    "s_waitcnt vmcnt(0)\n"
    "s_nop 1\n"
    "v_mfma_f32_16x16x32_bf16 v[176:179], v[80:83], v[160:163], 0\n"
    "v_mfma_f32_16x16x32_bf16 v[180:183], v[80:83], v[164:167], 0\n"
    "s_nop 7\n"
    "buffer_load_dwordx4 v[80:83], v84, s[44:47], s100 offen offset:64\n"
    "s_waitcnt vmcnt(0)\n"
    "s_nop 1\n"
    "v_mfma_f32_16x16x32_bf16 v[176:179], v[80:83], v[168:171], v[176:179]\n"
    "v_mfma_f32_16x16x32_bf16 v[180:183], v[80:83], v[172:175], v[180:183]\n"
    "s_nop 7\n"
    "buffer_load_dwordx4 v[80:83], v84, s[44:47], s100 offen offset:128\n"
    "s_waitcnt vmcnt(0)\n"
    "s_nop 1\n"
    "v_mfma_f32_16x16x32_bf16 v[176:179], v[80:83], v[128:131], v[176:179]\n"
    "v_mfma_f32_16x16x32_bf16 v[180:183], v[80:83], v[132:135], v[180:183]\n"
    "s_nop 7\n"
    "buffer_load_dwordx4 v[80:83], v84, s[44:47], s100 offen offset:192\n"
    "s_waitcnt vmcnt(0)\n"
    "s_nop 1\n"
    "v_mfma_f32_16x16x32_bf16 v[176:179], v[80:83], v[136:139], v[176:179]\n"
    "v_mfma_f32_16x16x32_bf16 v[180:183], v[80:83], v[140:143], v[180:183]\n"
    "s_nop 7\n"
    "buffer_load_dwordx4 v[80:83], v84, s[44:47], s100 offen offset:256\n"
    "s_waitcnt vmcnt(0)\n"
    "s_nop 1\n"
    "v_mfma_f32_16x16x32_bf16 v[176:179], v[80:83], v[144:147], v[176:179]\n"
    "v_mfma_f32_16x16x32_bf16 v[180:183], v[80:83], v[148:151], v[180:183]\n"
    "s_nop 7\n"
    "buffer_load_dwordx4 v[80:83], v84, s[44:47], s100 offen offset:320\n"
    "s_waitcnt vmcnt(0)\n"
    "s_nop 1\n"
    "v_mfma_f32_16x16x32_bf16 v[176:179], v[80:83], v[152:155], v[176:179]\n"
    "v_mfma_f32_16x16x32_bf16 v[180:183], v[80:83], v[156:159], v[180:183]\n"
    "s_nop 7\n"
    "s_xor_b32 s100, s18, 1\n"
    "s_mul_i32 s100, s100, 6144\n"
    "s_add_u32 s100, s100, 0x1e000\n"
    "buffer_load_dwordx4 v[80:83], v84, s[44:47], s100 offen\n"
    "s_waitcnt vmcnt(0)\n"
    "s_nop 1\n"
    "v_mfma_f32_16x16x32_bf16 v[184:187], v[80:83], v[160:163], 0\n"
    "v_mfma_f32_16x16x32_bf16 v[188:191], v[80:83], v[164:167], 0\n"
    "s_nop 7\n"
    "buffer_load_dwordx4 v[80:83], v84, s[44:47], s100 offen offset:64\n"
    "s_waitcnt vmcnt(0)\n"
    "s_nop 1\n"
    "v_mfma_f32_16x16x32_bf16 v[184:187], v[80:83], v[168:171], v[184:187]\n"
    "v_mfma_f32_16x16x32_bf16 v[188:191], v[80:83], v[172:175], v[188:191]\n"
    "s_nop 7\n"
    "buffer_load_dwordx4 v[80:83], v84, s[44:47], s100 offen offset:128\n"
    "s_waitcnt vmcnt(0)\n"
    "s_nop 1\n"
    "v_mfma_f32_16x16x32_bf16 v[184:187], v[80:83], v[128:131], v[184:187]\n"
    "v_mfma_f32_16x16x32_bf16 v[188:191], v[80:83], v[132:135], v[188:191]\n"
    "s_nop 7\n"
    "buffer_load_dwordx4 v[80:83], v84, s[44:47], s100 offen offset:192\n"
    "s_waitcnt vmcnt(0)\n"
    "s_nop 1\n"
    "v_mfma_f32_16x16x32_bf16 v[184:187], v[80:83], v[136:139], v[184:187]\n"
    "v_mfma_f32_16x16x32_bf16 v[188:191], v[80:83], v[140:143], v[188:191]\n"
    "s_nop 7\n"
    "buffer_load_dwordx4 v[80:83], v84, s[44:47], s100 offen offset:256\n"
    "s_waitcnt vmcnt(0)\n"
    "s_nop 1\n"
    "v_mfma_f32_16x16x32_bf16 v[184:187], v[80:83], v[144:147], v[184:187]\n"
    "v_mfma_f32_16x16x32_bf16 v[188:191], v[80:83], v[148:151], v[188:191]\n"
    "s_nop 7\n"
    "buffer_load_dwordx4 v[80:83], v84, s[44:47], s100 offen offset:320\n"
    "s_waitcnt vmcnt(0)\n"
    "s_nop 1\n"
    "v_mfma_f32_16x16x32_bf16 v[184:187], v[80:83], v[152:155], v[184:187]\n"
    "v_mfma_f32_16x16x32_bf16 v[188:191], v[80:83], v[156:159], v[188:191]\n"
    "s_nop 7\n"
    "s_nop 7\n"
    "s_nop 7\n"
    "v_max3_f32 v124, v176, v177, v178\n"
    "v_max3_f32 v124, v124, v179, v184\n"
    "v_max3_f32 v124, v124, v185, v186\n"
    "v_max_f32_e32 v124, v124, v187\n"
    "v_max3_f32 v125, v180, v181, v182\n"
    "v_max3_f32 v125, v125, v183, v188\n"
    "v_max3_f32 v125, v125, v189, v190\n"
    "v_max_f32_e32 v125, v125, v191\n"
    "v_mov_b32_e32 v80, v124\n"
    "s_nop 1\n"
    "v_permlane16_swap_b32_e32 v124, v80\n"
    "v_max_f32_e32 v124, v124, v80\n"
    "v_mov_b32_e32 v80, v124\n"
    "s_nop 1\n"
    "v_permlane32_swap_b32_e32 v124, v80\n"
    "v_max_f32_e32 v124, v124, v80\n"
    "v_mov_b32_e32 v80, v125\n"
    "s_nop 1\n"
    "v_permlane16_swap_b32_e32 v125, v80\n"
    "v_max_f32_e32 v125, v125, v80\n"
    "v_mov_b32_e32 v80, v125\n"
    "s_nop 1\n"
    "v_permlane32_swap_b32_e32 v125, v80\n"
    "v_max_f32_e32 v125, v125, v80\n"
    "v_floor_f32_e64 v124, -v124\n"
    "v_min_f32_e32 v124, v124, v112\n"
    "v_sub_f32_e32 v80, v112, v124\n"
    "v_mov_b32_e32 v112, v124\n"
    "v_sub_f32_e32 v192, v192, v80\n"
    "v_sub_f32_e32 v193, v193, v80\n"
    "v_sub_f32_e32 v194, v194, v80\n"
    "v_sub_f32_e32 v195, v195, v80\n"
    "v_sub_f32_e32 v200, v200, v80\n"
    "v_sub_f32_e32 v201, v201, v80\n"
    "v_sub_f32_e32 v202, v202, v80\n"
    "v_sub_f32_e32 v203, v203, v80\n"
    "v_min_f32_e32 v80, 0x437f0000, v80\n"
    "v_cvt_u32_f32_e32 v80, v80\n"
    "v_sub_u32_e32 v124, 0, v80\n"
    "v_ldexp_f32 v252, v252, v124\n"
    "v_lshlrev_b32_e32 v80, 7, v80\n"
    "v_lshl_or_b32 v80, v80, 16, v80\n"
    "v_pk_sub_u16 v0, v0, v80 clamp\n"
    "v_pk_sub_u16 v1, v1, v80 clamp\n"
    "v_pk_sub_u16 v2, v2, v80 clamp\n"
    "v_pk_sub_u16 v3, v3, v80 clamp\n"
    "v_pk_sub_u16 v8, v8, v80 clamp\n"
    "v_pk_sub_u16 v9, v9, v80 clamp\n"
    "v_pk_sub_u16 v10, v10, v80 clamp\n"
    "v_pk_sub_u16 v11, v11, v80 clamp\n"
    "v_pk_sub_u16 v16, v16, v80 clamp\n"
    "v_pk_sub_u16 v17, v17, v80 clamp\n"
    "v_pk_sub_u16 v18, v18, v80 clamp\n"
    "v_pk_sub_u16 v19, v19, v80 clamp\n"
    "v_pk_sub_u16 v24, v24, v80 clamp\n"
    "v_pk_sub_u16 v25, v25, v80 clamp\n"
    "v_pk_sub_u16 v26, v26, v80 clamp\n"
    "v_pk_sub_u16 v27, v27, v80 clamp\n"
    "v_pk_sub_u16 v32, v32, v80 clamp\n"
    "v_pk_sub_u16 v33, v33, v80 clamp\n"
    "v_pk_sub_u16 v34, v34, v80 clamp\n"
    "v_pk_sub_u16 v35, v35, v80 clamp\n"
    "v_pk_sub_u16 v40, v40, v80 clamp\n"
    "v_pk_sub_u16 v41, v41, v80 clamp\n"
    "v_pk_sub_u16 v42, v42, v80 clamp\n"
    "v_pk_sub_u16 v43, v43, v80 clamp\n"
    "v_pk_sub_u16 v48, v48, v80 clamp\n"
    "v_pk_sub_u16 v49, v49, v80 clamp\n"
    "v_pk_sub_u16 v50, v50, v80 clamp\n"
    "v_pk_sub_u16 v51, v51, v80 clamp\n"
    "v_pk_sub_u16 v56, v56, v80 clamp\n"
    "v_pk_sub_u16 v57, v57, v80 clamp\n"
    "v_pk_sub_u16 v58, v58, v80 clamp\n"
    "v_pk_sub_u16 v59, v59, v80 clamp\n"
    "v_pk_sub_u16 v64, v64, v80 clamp\n"
    "v_pk_sub_u16 v65, v65, v80 clamp\n"
    "v_pk_sub_u16 v66, v66, v80 clamp\n"
    "v_pk_sub_u16 v67, v67, v80 clamp\n"
    "v_pk_sub_u16 v72, v72, v80 clamp\n"
    "v_pk_sub_u16 v73, v73, v80 clamp\n"
    "v_pk_sub_u16 v74, v74, v80 clamp\n"
    "v_pk_sub_u16 v75, v75, v80 clamp\n"
    "v_floor_f32_e64 v125, -v125\n"
    "v_min_f32_e32 v125, v125, v116\n"
    "v_sub_f32_e32 v80, v116, v125\n"
    "v_mov_b32_e32 v116, v125\n"
    "v_sub_f32_e32 v196, v196, v80\n"
    "v_sub_f32_e32 v197, v197, v80\n"
    "v_sub_f32_e32 v198, v198, v80\n"
    "v_sub_f32_e32 v199, v199, v80\n"
    "v_sub_f32_e32 v204, v204, v80\n"
    "v_sub_f32_e32 v205, v205, v80\n"
    "v_sub_f32_e32 v206, v206, v80\n"
    "v_sub_f32_e32 v207, v207, v80\n"
    "v_min_f32_e32 v80, 0x437f0000, v80\n"
    "v_cvt_u32_f32_e32 v80, v80\n"
    "v_sub_u32_e32 v125, 0, v80\n"
    "v_ldexp_f32 v253, v253, v125\n"
    "v_lshlrev_b32_e32 v80, 7, v80\n"
    "v_lshl_or_b32 v80, v80, 16, v80\n"
    "v_pk_sub_u16 v4, v4, v80 clamp\n"
    "v_pk_sub_u16 v5, v5, v80 clamp\n"
    "v_pk_sub_u16 v6, v6, v80 clamp\n"
    "v_pk_sub_u16 v7, v7, v80 clamp\n"
    "v_pk_sub_u16 v12, v12, v80 clamp\n"
    "v_pk_sub_u16 v13, v13, v80 clamp\n"
    "v_pk_sub_u16 v14, v14, v80 clamp\n"
    "v_pk_sub_u16 v15, v15, v80 clamp\n"
    "v_pk_sub_u16 v20, v20, v80 clamp\n"
    "v_pk_sub_u16 v21, v21, v80 clamp\n"
    "v_pk_sub_u16 v22, v22, v80 clamp\n"
    "v_pk_sub_u16 v23, v23, v80 clamp\n"
    "v_pk_sub_u16 v28, v28, v80 clamp\n"
    "v_pk_sub_u16 v29, v29, v80 clamp\n"
    "v_pk_sub_u16 v30, v30, v80 clamp\n"
    "v_pk_sub_u16 v31, v31, v80 clamp\n"
    "v_pk_sub_u16 v36, v36, v80 clamp\n"
    "v_pk_sub_u16 v37, v37, v80 clamp\n"
    "v_pk_sub_u16 v38, v38, v80 clamp\n"
    "v_pk_sub_u16 v39, v39, v80 clamp\n"
    "v_pk_sub_u16 v44, v44, v80 clamp\n"
    "v_pk_sub_u16 v45, v45, v80 clamp\n"
    "v_pk_sub_u16 v46, v46, v80 clamp\n"
    "v_pk_sub_u16 v47, v47, v80 clamp\n"
    "v_pk_sub_u16 v52, v52, v80 clamp\n"
    "v_pk_sub_u16 v53, v53, v80 clamp\n"
    "v_pk_sub_u16 v54, v54, v80 clamp\n"
    "v_pk_sub_u16 v55, v55, v80 clamp\n"
    "v_pk_sub_u16 v60, v60, v80 clamp\n"
    "v_pk_sub_u16 v61, v61, v80 clamp\n"
    "v_pk_sub_u16 v62, v62, v80 clamp\n"
    "v_pk_sub_u16 v63, v63, v80 clamp\n"
    "v_pk_sub_u16 v68, v68, v80 clamp\n"
    "v_pk_sub_u16 v69, v69, v80 clamp\n"
    "v_pk_sub_u16 v70, v70, v80 clamp\n"
    "v_pk_sub_u16 v71, v71, v80 clamp\n"
    "v_pk_sub_u16 v76, v76, v80 clamp\n"
    "v_pk_sub_u16 v77, v77, v80 clamp\n"
    "v_pk_sub_u16 v78, v78, v80 clamp\n"
    "v_pk_sub_u16 v79, v79, v80 clamp\n"
    "v_mov_b32_e32 v113, v112\n"
    "v_mov_b32_e32 v114, v112\n"
    "v_mov_b32_e32 v115, v112\n"
    "v_mov_b32_e32 v117, v116\n"
    "v_mov_b32_e32 v118, v116\n"
    "v_mov_b32_e32 v119, v116\n"
    "v_add_f32_e32 v176, v112, v176\n"
    "v_add_f32_e32 v177, v112, v177\n"
    "v_add_f32_e32 v178, v112, v178\n"
    "v_exp_f32_e32 v176, v176\n"
    "v_add_f32_e32 v179, v112, v179\n"
    "v_exp_f32_e32 v177, v177\n"
    "v_add_f32_e32 v180, v116, v180\n"
    "v_exp_f32_e32 v178, v178\n"
    "v_add_f32_e32 v181, v116, v181\n"
    "v_exp_f32_e32 v179, v179\n"
    "v_add_f32_e32 v124, v176, v177\n"
    "v_cvt_pk_bf16_f32 v80, v176, v177\n"
    "v_add_f32_e32 v182, v116, v182\n"
    "v_exp_f32_e32 v180, v180\n"
    "v_add_f32_e32 v124, v178, v124\n"
    "v_add_f32_e32 v183, v116, v183\n"
    "v_exp_f32_e32 v181, v181\n"
    "v_add_f32_e32 v124, v179, v124\n"
    "v_cvt_pk_bf16_f32 v81, v178, v179\n"
    "v_add_f32_e32 v184, v112, v184\n"
    "v_exp_f32_e32 v182, v182\n"
    "v_add_f32_e32 v185, v112, v185\n"
    "v_exp_f32_e32 v183, v183\n"
    "v_add_f32_e32 v125, v180, v181\n"
    "v_cvt_pk_bf16_f32 v84, v180, v181\n"
    "v_add_f32_e32 v186, v112, v186\n"
    "v_exp_f32_e32 v184, v184\n"
    "v_add_f32_e32 v125, v182, v125\n"
    "v_add_f32_e32 v187, v112, v187\n"
    "v_exp_f32_e32 v185, v185\n"
    "v_add_f32_e32 v125, v183, v125\n"
    "v_cvt_pk_bf16_f32 v85, v182, v183\n"
    "v_add_f32_e32 v188, v116, v188\n"
    "v_exp_f32_e32 v186, v186\n"
    "v_add_f32_e32 v124, v184, v124\n"
    "v_add_f32_e32 v189, v116, v189\n"
    "v_exp_f32_e32 v187, v187\n"
    "v_add_f32_e32 v124, v185, v124\n"
    "v_cvt_pk_bf16_f32 v82, v184, v185\n"
    "v_add_f32_e32 v190, v116, v190\n"
    "v_exp_f32_e32 v188, v188\n"
    "v_add_f32_e32 v124, v186, v124\n"
    "v_add_f32_e32 v191, v116, v191\n"
    "v_exp_f32_e32 v189, v189\n"
    "v_add_f32_e32 v124, v187, v124\n"
    "v_cvt_pk_bf16_f32 v83, v186, v187\n"
    "v_exp_f32_e32 v190, v190\n"
    "v_add_f32_e32 v125, v188, v125\n"
    "v_exp_f32_e32 v191, v191\n"
    "v_add_f32_e32 v125, v189, v125\n"
    "v_cvt_pk_bf16_f32 v86, v188, v189\n"
    "v_add_f32_e32 v125, v190, v125\n"
    "v_add_f32_e32 v125, v191, v125\n"
    "v_cvt_pk_bf16_f32 v87, v190, v191\n"
    "s_nop 7\n"
    "s_nop 7\n"
    "s_branch .Lback_a10\n"
    ".Lrare_a11:\n"
    "s_nop 7\n"
    "s_nop 7\n"
    "v_mbcnt_lo_u32_b32 v124, -1, 0\n"
    "v_mbcnt_hi_u32_b32 v124, -1, v124\n"
    "v_and_b32_e32 v125, 15, v124\n"
    "v_mul_u32_u24_e32 v125, 0x180, v125\n"
    "v_lshrrev_b32_e32 v124, 4, v124\n"
    "v_lshl_add_u32 v92, v124, 4, v125\n"
    "s_xor_b32 s100, s18, 2\n"
    "s_mul_i32 s100, s100, 6144\n"
    "s_add_u32 s100, s100, 0x1e000\n"
    "buffer_load_dwordx4 v[88:91], v92, s[44:47], s100 offen\n"
    "s_waitcnt vmcnt(0)\n"
    "s_nop 1\n"
    "v_mfma_f32_16x16x32_bf16 v[192:195], v[88:91], v[160:163], 0\n"
    "v_mfma_f32_16x16x32_bf16 v[196:199], v[88:91], v[164:167], 0\n"
    "s_nop 7\n"
    "buffer_load_dwordx4 v[88:91], v92, s[44:47], s100 offen offset:64\n"
    "s_waitcnt vmcnt(0)\n"
    "s_nop 1\n"
    "v_mfma_f32_16x16x32_bf16 v[192:195], v[88:91], v[168:171], v[192:195]\n"
    "v_mfma_f32_16x16x32_bf16 v[196:199], v[88:91], v[172:175], v[196:199]\n"
    "s_nop 7\n"
    "buffer_load_dwordx4 v[88:91], v92, s[44:47], s100 offen offset:128\n"
    "s_waitcnt vmcnt(0)\n"
    "s_nop 1\n"
    "v_mfma_f32_16x16x32_bf16 v[192:195], v[88:91], v[128:131], v[192:195]\n"
    "v_mfma_f32_16x16x32_bf16 v[196:199], v[88:91], v[132:135], v[196:199]\n"
    "s_nop 7\n"
    "buffer_load_dwordx4 v[88:91], v92, s[44:47], s100 offen offset:192\n"
    "s_waitcnt vmcnt(0)\n"
    "s_nop 1\n"
    "v_mfma_f32_16x16x32_bf16 v[192:195], v[88:91], v[136:139], v[192:195]\n"
    "v_mfma_f32_16x16x32_bf16 v[196:199], v[88:91], v[140:143], v[196:199]\n"
    "s_nop 7\n"
    "buffer_load_dwordx4 v[88:91], v92, s[44:47], s100 offen offset:256\n"
    "s_waitcnt vmcnt(0)\n"
    "s_nop 1\n"
    "v_mfma_f32_16x16x32_bf16 v[192:195], v[88:91], v[144:147], v[192:195]\n"
    "v_mfma_f32_16x16x32_bf16 v[196:199], v[88:91], v[148:151], v[196:199]\n"
    "s_nop 7\n"
    "buffer_load_dwordx4 v[88:91], v92, s[44:47], s100 offen offset:320\n"
    "s_waitcnt vmcnt(0)\n"
    "s_nop 1\n"
    "v_mfma_f32_16x16x32_bf16 v[192:195], v[88:91], v[152:155], v[192:195]\n"
    "v_mfma_f32_16x16x32_bf16 v[196:199], v[88:91], v[156:159], v[196:199]\n"
    "s_nop 7\n"
    "s_xor_b32 s100, s18, 3\n"
    "s_mul_i32 s100, s100, 6144\n"
    "s_add_u32 s100, s100, 0x1e000\n"
    "buffer_load_dwordx4 v[88:91], v92, s[44:47], s100 offen\n"
    "s_waitcnt vmcnt(0)\n"
    "s_nop 1\n"
    "v_mfma_f32_16x16x32_bf16 v[200:203], v[88:91], v[160:163], 0\n"
    "v_mfma_f32_16x16x32_bf16 v[204:207], v[88:91], v[164:167], 0\n"
    "s_nop 7\n"
    "buffer_load_dwordx4 v[88:91], v92, s[44:47], s100 offen offset:64\n"
    "s_waitcnt vmcnt(0)\n"
    "s_nop 1\n"
    "v_mfma_f32_16x16x32_bf16 v[200:203], v[88:91], v[168:171], v[200:203]\n"
    "v_mfma_f32_16x16x32_bf16 v[204:207], v[88:91], v[172:175], v[204:207]\n"
    "s_nop 7\n"
    "buffer_load_dwordx4 v[88:91], v92, s[44:47], s100 offen offset:128\n"
    "s_waitcnt vmcnt(0)\n"
    "s_nop 1\n"
    "v_mfma_f32_16x16x32_bf16 v[200:203], v[88:91], v[128:131], v[200:203]\n"
    "v_mfma_f32_16x16x32_bf16 v[204:207], v[88:91], v[132:135], v[204:207]\n"
    "s_nop 7\n"
    "buffer_load_dwordx4 v[88:91], v92, s[44:47], s100 offen offset:192\n"
    "s_waitcnt vmcnt(0)\n"
    "s_nop 1\n"
    "v_mfma_f32_16x16x32_bf16 v[200:203], v[88:91], v[136:139], v[200:203]\n"
    "v_mfma_f32_16x16x32_bf16 v[204:207], v[88:91], v[140:143], v[204:207]\n"
    "s_nop 7\n"
    "buffer_load_dwordx4 v[88:91], v92, s[44:47], s100 offen offset:256\n"
    "s_waitcnt vmcnt(0)\n"
    "s_nop 1\n"
    "v_mfma_f32_16x16x32_bf16 v[200:203], v[88:91], v[144:147], v[200:203]\n"
    "v_mfma_f32_16x16x32_bf16 v[204:207], v[88:91], v[148:151], v[204:207]\n"
    "s_nop 7\n"
    "buffer_load_dwordx4 v[88:91], v92, s[44:47], s100 offen offset:320\n"
    "s_waitcnt vmcnt(0)\n"
    "s_nop 1\n"
    "v_mfma_f32_16x16x32_bf16 v[200:203], v[88:91], v[152:155], v[200:203]\n"
    "v_mfma_f32_16x16x32_bf16 v[204:207], v[88:91], v[156:159], v[204:207]\n"
    "s_nop 7\n"
    "s_nop 7\n"
    "s_nop 7\n"
    "v_max3_f32 v124, v192, v193, v194\n"
    "v_max3_f32 v124, v124, v195, v200\n"
    "v_max3_f32 v124, v124, v201, v202\n"
    "v_max_f32_e32 v124, v124, v203\n"
    "v_max3_f32 v125, v196, v197, v198\n"
    "v_max3_f32 v125, v125, v199, v204\n"
    "v_max3_f32 v125, v125, v205, v206\n"
    "v_max_f32_e32 v125, v125, v207\n"
    "v_mov_b32_e32 v88, v124\n"
    "s_nop 1\n"
    "v_permlane16_swap_b32_e32 v124, v88\n"
    "v_max_f32_e32 v124, v124, v88\n"
    "v_mov_b32_e32 v88, v124\n"
    "s_nop 1\n"
    "v_permlane32_swap_b32_e32 v124, v88\n"
    "v_max_f32_e32 v124, v124, v88\n"
    "v_mov_b32_e32 v88, v125\n"
    "s_nop 1\n"
    "v_permlane16_swap_b32_e32 v125, v88\n"
    "v_max_f32_e32 v125, v125, v88\n"
    "v_mov_b32_e32 v88, v125\n"
    "s_nop 1\n"
    "v_permlane32_swap_b32_e32 v125, v88\n"
    "v_max_f32_e32 v125, v125, v88\n"
    "v_floor_f32_e64 v124, -v124\n"
    "v_min_f32_e32 v124, v124, v112\n"
    "v_sub_f32_e32 v88, v112, v124\n"
    "v_mov_b32_e32 v112, v124\n"
    "v_sub_f32_e32 v176, v176, v88\n"
    "v_sub_f32_e32 v177, v177, v88\n"
    "v_sub_f32_e32 v178, v178, v88\n"
    "v_sub_f32_e32 v179, v179, v88\n"
    "v_sub_f32_e32 v184, v184, v88\n"
    "v_sub_f32_e32 v185, v185, v88\n"
    "v_sub_f32_e32 v186, v186, v88\n"
    "v_sub_f32_e32 v187, v187, v88\n"
    "v_min_f32_e32 v88, 0x437f0000, v88\n"
    "v_cvt_u32_f32_e32 v88, v88\n"
    "v_sub_u32_e32 v124, 0, v88\n"
    "v_ldexp_f32 v252, v252, v124\n"
    "v_lshlrev_b32_e32 v88, 7, v88\n"
    "v_lshl_or_b32 v88, v88, 16, v88\n"
    "v_pk_sub_u16 v0, v0, v88 clamp\n"
    "v_pk_sub_u16 v1, v1, v88 clamp\n"
    "v_pk_sub_u16 v2, v2, v88 clamp\n"
    "v_pk_sub_u16 v3, v3, v88 clamp\n"
    "v_pk_sub_u16 v8, v8, v88 clamp\n"
    "v_pk_sub_u16 v9, v9, v88 clamp\n"
    "v_pk_sub_u16 v10, v10, v88 clamp\n"
    "v_pk_sub_u16 v11, v11, v88 clamp\n"
    "v_pk_sub_u16 v16, v16, v88 clamp\n"
    "v_pk_sub_u16 v17, v17, v88 clamp\n"
    "v_pk_sub_u16 v18, v18, v88 clamp\n"
    "v_pk_sub_u16 v19, v19, v88 clamp\n"
    "v_pk_sub_u16 v24, v24, v88 clamp\n"
    "v_pk_sub_u16 v25, v25, v88 clamp\n"
    "v_pk_sub_u16 v26, v26, v88 clamp\n"
    "v_pk_sub_u16 v27, v27, v88 clamp\n"
    "v_pk_sub_u16 v32, v32, v88 clamp\n"
    "v_pk_sub_u16 v33, v33, v88 clamp\n"
    "v_pk_sub_u16 v34, v34, v88 clamp\n"
    "v_pk_sub_u16 v35, v35, v88 clamp\n"
    "v_pk_sub_u16 v40, v40, v88 clamp\n"
    "v_pk_sub_u16 v41, v41, v88 clamp\n"
    "v_pk_sub_u16 v42, v42, v88 clamp\n"
    "v_pk_sub_u16 v43, v43, v88 clamp\n"
    "v_pk_sub_u16 v48, v48, v88 clamp\n"
    "v_pk_sub_u16 v49, v49, v88 clamp\n"
    "v_pk_sub_u16 v50, v50, v88 clamp\n"
    "v_pk_sub_u16 v51, v51, v88 clamp\n"
    "v_pk_sub_u16 v56, v56, v88 clamp\n"
    "v_pk_sub_u16 v57, v57, v88 clamp\n"
    "v_pk_sub_u16 v58, v58, v88 clamp\n"
    "v_pk_sub_u16 v59, v59, v88 clamp\n"
    "v_pk_sub_u16 v64, v64, v88 clamp\n"
    "v_pk_sub_u16 v65, v65, v88 clamp\n"
    "v_pk_sub_u16 v66, v66, v88 clamp\n"
    "v_pk_sub_u16 v67, v67, v88 clamp\n"
    "v_pk_sub_u16 v72, v72, v88 clamp\n"
    "v_pk_sub_u16 v73, v73, v88 clamp\n"
    "v_pk_sub_u16 v74, v74, v88 clamp\n"
    "v_pk_sub_u16 v75, v75, v88 clamp\n"
    "v_pk_sub_u16 v80, v80, v88 clamp\n"
    "v_pk_sub_u16 v81, v81, v88 clamp\n"
    "v_pk_sub_u16 v82, v82, v88 clamp\n"
    "v_pk_sub_u16 v83, v83, v88 clamp\n"
    "v_floor_f32_e64 v125, -v125\n"
    "v_min_f32_e32 v125, v125, v116\n"
    "v_sub_f32_e32 v88, v116, v125\n"
    "v_mov_b32_e32 v116, v125\n"
    "v_sub_f32_e32 v180, v180, v88\n"
    "v_sub_f32_e32 v181, v181, v88\n"
    "v_sub_f32_e32 v182, v182, v88\n"
    "v_sub_f32_e32 v183, v183, v88\n"
    "v_sub_f32_e32 v188, v188, v88\n"
    "v_sub_f32_e32 v189, v189, v88\n"
    "v_sub_f32_e32 v190, v190, v88\n"
    "v_sub_f32_e32 v191, v191, v88\n"
    "v_min_f32_e32 v88, 0x437f0000, v88\n"
    "v_cvt_u32_f32_e32 v88, v88\n"
    "v_sub_u32_e32 v125, 0, v88\n"
    "v_ldexp_f32 v253, v253, v125\n"
    "v_lshlrev_b32_e32 v88, 7, v88\n"
    "v_lshl_or_b32 v88, v88, 16, v88\n"
    "v_pk_sub_u16 v4, v4, v88 clamp\n"
    "v_pk_sub_u16 v5, v5, v88 clamp\n"
    "v_pk_sub_u16 v6, v6, v88 clamp\n"
    "v_pk_sub_u16 v7, v7, v88 clamp\n"
    "v_pk_sub_u16 v12, v12, v88 clamp\n"
    "v_pk_sub_u16 v13, v13, v88 clamp\n"
    "v_pk_sub_u16 v14, v14, v88 clamp\n"
    "v_pk_sub_u16 v15, v15, v88 clamp\n"
    "v_pk_sub_u16 v20, v20, v88 clamp\n"
    "v_pk_sub_u16 v21, v21, v88 clamp\n"
    "v_pk_sub_u16 v22, v22, v88 clamp\n"
    "v_pk_sub_u16 v23, v23, v88 clamp\n"
    "v_pk_sub_u16 v28, v28, v88 clamp\n"
    "v_pk_sub_u16 v29, v29, v88 clamp\n"
    "v_pk_sub_u16 v30, v30, v88 clamp\n"
    "v_pk_sub_u16 v31, v31, v88 clamp\n"
    "v_pk_sub_u16 v36, v36, v88 clamp\n"
    "v_pk_sub_u16 v37, v37, v88 clamp\n"
    "v_pk_sub_u16 v38, v38, v88 clamp\n"
    "v_pk_sub_u16 v39, v39, v88 clamp\n"
    "v_pk_sub_u16 v44, v44, v88 clamp\n"
    "v_pk_sub_u16 v45, v45, v88 clamp\n"
    "v_pk_sub_u16 v46, v46, v88 clamp\n"
    "v_pk_sub_u16 v47, v47, v88 clamp\n"
    "v_pk_sub_u16 v52, v52, v88 clamp\n"
    "v_pk_sub_u16 v53, v53, v88 clamp\n"
    "v_pk_sub_u16 v54, v54, v88 clamp\n"
    "v_pk_sub_u16 v55, v55, v88 clamp\n"
    "v_pk_sub_u16 v60, v60, v88 clamp\n"
    "v_pk_sub_u16 v61, v61, v88 clamp\n"
    "v_pk_sub_u16 v62, v62, v88 clamp\n"
    "v_pk_sub_u16 v63, v63, v88 clamp\n"
    "v_pk_sub_u16 v68, v68, v88 clamp\n"
    "v_pk_sub_u16 v69, v69, v88 clamp\n"
    "v_pk_sub_u16 v70, v70, v88 clamp\n"
    "v_pk_sub_u16 v71, v71, v88 clamp\n"
    "v_pk_sub_u16 v76, v76, v88 clamp\n"
    "v_pk_sub_u16 v77, v77, v88 clamp\n"
    "v_pk_sub_u16 v78, v78, v88 clamp\n"
    "v_pk_sub_u16 v79, v79, v88 clamp\n"
    "v_pk_sub_u16 v84, v84, v88 clamp\n"
    "v_pk_sub_u16 v85, v85, v88 clamp\n"
    "v_pk_sub_u16 v86, v86, v88 clamp\n"
    "v_pk_sub_u16 v87, v87, v88 clamp\n"
    "v_mov_b32_e32 v113, v112\n"
    "v_mov_b32_e32 v114, v112\n"
    "v_mov_b32_e32 v115, v112\n"
    "v_mov_b32_e32 v117, v116\n"
    "v_mov_b32_e32 v118, v116\n"
    "v_mov_b32_e32 v119, v116\n"
    "v_add_f32_e32 v192, v112, v192\n"
    "v_add_f32_e32 v193, v112, v193\n"
    "v_add_f32_e32 v194, v112, v194\n"
    "v_exp_f32_e32 v192, v192\n"
    "v_add_f32_e32 v195, v112, v195\n"
    "v_exp_f32_e32 v193, v193\n"
    "v_add_f32_e32 v196, v116, v196\n"
    "v_exp_f32_e32 v194, v194\n"
    "v_add_f32_e32 v197, v116, v197\n"
    "v_exp_f32_e32 v195, v195\n"
    "v_add_f32_e32 v124, v192, v193\n"
    "v_cvt_pk_bf16_f32 v88, v192, v193\n"
    "v_add_f32_e32 v198, v116, v198\n"
    "v_exp_f32_e32 v196, v196\n"
    "v_add_f32_e32 v124, v194, v124\n"
    "v_add_f32_e32 v199, v116, v199\n"
    "v_exp_f32_e32 v197, v197\n"
    "v_add_f32_e32 v124, v195, v124\n"
    "v_cvt_pk_bf16_f32 v89, v194, v195\n"
    "v_add_f32_e32 v200, v112, v200\n"
    "v_exp_f32_e32 v198, v198\n"
    "v_add_f32_e32 v201, v112, v201\n"
    "v_exp_f32_e32 v199, v199\n"
    "v_add_f32_e32 v125, v196, v197\n"
    "v_cvt_pk_bf16_f32 v92, v196, v197\n"
    "v_add_f32_e32 v202, v112, v202\n"
    "v_exp_f32_e32 v200, v200\n"
    "v_add_f32_e32 v125, v198, v125\n"
    "v_add_f32_e32 v203, v112, v203\n"
    "v_exp_f32_e32 v201, v201\n"
    "v_add_f32_e32 v125, v199, v125\n"
    "v_cvt_pk_bf16_f32 v93, v198, v199\n"
    "v_add_f32_e32 v204, v116, v204\n"
    "v_exp_f32_e32 v202, v202\n"
    "v_add_f32_e32 v124, v200, v124\n"
    "v_add_f32_e32 v205, v116, v205\n"
    "v_exp_f32_e32 v203, v203\n"
    "v_add_f32_e32 v124, v201, v124\n"
    "v_cvt_pk_bf16_f32 v90, v200, v201\n"
    "v_add_f32_e32 v206, v116, v206\n"
    "v_exp_f32_e32 v204, v204\n"
    "v_add_f32_e32 v124, v202, v124\n"
    "v_add_f32_e32 v207, v116, v207\n"
    "v_exp_f32_e32 v205, v205\n"
    "v_add_f32_e32 v124, v203, v124\n"
    "v_cvt_pk_bf16_f32 v91, v202, v203\n"
    "v_exp_f32_e32 v206, v206\n"
    "v_add_f32_e32 v125, v204, v125\n"
    "v_exp_f32_e32 v207, v207\n"
    "v_add_f32_e32 v125, v205, v125\n"
    "v_cvt_pk_bf16_f32 v94, v204, v205\n"
    "v_add_f32_e32 v125, v206, v125\n"
    "v_add_f32_e32 v125, v207, v125\n"
    "v_cvt_pk_bf16_f32 v95, v206, v207\n"
    "s_nop 7\n"
    "s_nop 7\n"
    "s_branch .Lback_a11\n"
    ".Lrare_n6:\n"
    "s_nop 7\n"
    "s_nop 7\n"
    "v_mbcnt_lo_u32_b32 v204, -1, 0\n"
    "v_mbcnt_hi_u32_b32 v204, -1, v204\n"
    "v_and_b32_e32 v205, 15, v204\n"
    "v_lshrrev_b32_e32 v204, 4, v204\n"
    "v_lshrrev_b32_e32 v201, 1, v205\n"
    "v_xor_b32_e32 v201, v201, v204\n"
    "v_lshlrev_b32_e32 v201, 4, v201\n"
    "v_lshl_add_u32 v201, v205, 7, v201\n"
    "s_lshl_b32 s100, s17, 3\n"
    "s_add_u32 s100, s100, 0x15000\n"
    "v_add_u32_e32 v201, s100, v201\n"
    "v_xor_b32_e32 v202, 64, v201\n"
    "v_mul_u32_u24_e32 v205, 0x180, v205\n"
    "v_lshl_add_u32 v200, v204, 4, v205\n"
    "s_mul_i32 s100, s16, 1536\n"
    "s_add_u32 s100, s100, 0x24000\n"
    "buffer_load_dwordx4 v[192:195], v200, s[44:47], s100 offen\n"
    "ds_read_b128 v[196:199], v201\n"
    "s_waitcnt vmcnt(0) lgkmcnt(0)\n"
    "v_mfma_f32_16x16x32_bf16 v[240:243], v[192:195], v[196:199], 0\n"
    "s_nop 7\n"
    "buffer_load_dwordx4 v[192:195], v200, s[44:47], s100 offen offset:64\n"
    "ds_read_b128 v[196:199], v202\n"
    "s_waitcnt vmcnt(0) lgkmcnt(0)\n"
    "v_mfma_f32_16x16x32_bf16 v[240:243], v[192:195], v[196:199], v[240:243]\n"
    "s_nop 7\n"
    "buffer_load_dwordx4 v[192:195], v200, s[44:47], s100 offen offset:128\n"
    "ds_read_b128 v[196:199], v201 offset:4096\n"
    "s_waitcnt vmcnt(0) lgkmcnt(0)\n"
    "v_mfma_f32_16x16x32_bf16 v[240:243], v[192:195], v[196:199], v[240:243]\n"
    "s_nop 7\n"
    "buffer_load_dwordx4 v[192:195], v200, s[44:47], s100 offen offset:192\n"
    "ds_read_b128 v[196:199], v202 offset:4096\n"
    "s_waitcnt vmcnt(0) lgkmcnt(0)\n"
    "v_mfma_f32_16x16x32_bf16 v[240:243], v[192:195], v[196:199], v[240:243]\n"
    "s_nop 7\n"
    "buffer_load_dwordx4 v[192:195], v200, s[44:47], s100 offen offset:256\n"
    "ds_read_b128 v[196:199], v201 offset:8192\n"
    "s_waitcnt vmcnt(0) lgkmcnt(0)\n"
    "v_mfma_f32_16x16x32_bf16 v[240:243], v[192:195], v[196:199], v[240:243]\n"
    "s_nop 7\n"
    "buffer_load_dwordx4 v[192:195], v200, s[44:47], s100 offen offset:320\n"
    "ds_read_b128 v[196:199], v202 offset:8192\n"
    "s_waitcnt vmcnt(0) lgkmcnt(0)\n"
    "v_mfma_f32_16x16x32_bf16 v[240:243], v[192:195], v[196:199], v[240:243]\n"
    "s_nop 7\n"
    "s_nop 7\n"
    "v_max3_f32 v204, v240, v241, v242\n"
    "v_max_f32_e32 v204, v204, v243\n"
    "v_mov_b32_e32 v205, v204\n"
    "s_nop 1\n"
    "v_permlane16_swap_b32_e32 v204, v205\n"
    "v_max_f32_e32 v204, v204, v205\n"
    "v_mov_b32_e32 v205, v204\n"
    "s_nop 1\n"
    "v_permlane32_swap_b32_e32 v204, v205\n"
    "v_max_f32_e32 v204, v204, v205\n"
    "v_floor_f32_e64 v204, -v204\n"
    "v_min_f32_e32 v204, v204, v254\n"
    "v_sub_f32_e32 v205, v254, v204\n"
    "v_mov_b32_e32 v254, v204\n"
    "v_min_f32_e32 v205, 0x437f0000, v205\n"
    "v_cvt_u32_f32_e32 v205, v205\n"
    "v_sub_u32_e32 v204, 0, v205\n"
    "v_ldexp_f32 v255, v255, v204\n"
    "v_lshlrev_b32_e32 v205, 7, v205\n"
    "v_lshl_or_b32 v205, v205, 16, v205\n"
    "v_pk_sub_u16 v224, v224, v205 clamp\n"
    "v_pk_sub_u16 v225, v225, v205 clamp\n"
    "v_pk_sub_u16 v226, v226, v205 clamp\n"
    "v_pk_sub_u16 v227, v227, v205 clamp\n"
    "v_pk_sub_u16 v228, v228, v205 clamp\n"
    "v_pk_sub_u16 v229, v229, v205 clamp\n"
    "v_pk_sub_u16 v230, v230, v205 clamp\n"
    "v_pk_sub_u16 v231, v231, v205 clamp\n"
    "v_pk_sub_u16 v232, v232, v205 clamp\n"
    "v_pk_sub_u16 v233, v233, v205 clamp\n"
    "v_pk_sub_u16 v234, v234, v205 clamp\n"
    "v_pk_sub_u16 v235, v235, v205 clamp\n"
    "v_mov_b32_e32 v120, v254\n"
    "v_mov_b32_e32 v121, v254\n"
    "v_mov_b32_e32 v122, v254\n"
    "v_mov_b32_e32 v123, v254\n"
    "v_mov_b32_e32 v192, 0x14400\n"
    "v_mov_b32_e32 v193, 1\n"
    "ds_write_b32 v192, v193\n"
    "s_waitcnt lgkmcnt(0)\n"
    "v_add_f32_e32 v240, v254, v240\n"
    "v_add_f32_e32 v241, v254, v241\n"
    "v_add_f32_e32 v242, v254, v242\n"
    "v_add_f32_e32 v243, v254, v243\n"
    "v_exp_f32_e32 v240, v240\n"
    "v_exp_f32_e32 v241, v241\n"
    "v_exp_f32_e32 v242, v242\n"
    "v_exp_f32_e32 v243, v243\n"
    "v_cvt_pk_bf16_f32 v236, v240, v241\n"
    "v_add_f32_e32 v240, v240, v241\n"
    "v_cvt_pk_bf16_f32 v237, v242, v243\n"
    "v_add_f32_e32 v242, v242, v243\n"
    "v_add_f32_e32 v240, v240, v242\n"
    "s_nop 7\n"
    "s_nop 7\n"
    "s_branch .Lback_n6\n"
    ".Lrare_a12:\n"
    "s_nop 7\n"
    "s_nop 7\n"
    "v_mbcnt_lo_u32_b32 v124, -1, 0\n"
    "v_mbcnt_hi_u32_b32 v124, -1, v124\n"
    "v_and_b32_e32 v125, 15, v124\n"
    "v_mul_u32_u24_e32 v125, 0x180, v125\n"
    "v_lshrrev_b32_e32 v124, 4, v124\n"
    "v_lshl_add_u32 v100, v124, 4, v125\n"
    "s_xor_b32 s100, s18, 0\n"
    "s_mul_i32 s100, s100, 6144\n"
    "s_add_u32 s100, s100, 0x24000\n"
    "buffer_load_dwordx4 v[96:99], v100, s[44:47], s100 offen\n"
    "s_waitcnt vmcnt(0)\n"
    "s_nop 1\n"
    "v_mfma_f32_16x16x32_bf16 v[176:179], v[96:99], v[160:163], 0\n"
    "v_mfma_f32_16x16x32_bf16 v[180:183], v[96:99], v[164:167], 0\n"
    "s_nop 7\n"
    "buffer_load_dwordx4 v[96:99], v100, s[44:47], s100 offen offset:64\n"
    "s_waitcnt vmcnt(0)\n"
    "s_nop 1\n"
    "v_mfma_f32_16x16x32_bf16 v[176:179], v[96:99], v[168:171], v[176:179]\n"
    "v_mfma_f32_16x16x32_bf16 v[180:183], v[96:99], v[172:175], v[180:183]\n"
    "s_nop 7\n"
    "buffer_load_dwordx4 v[96:99], v100, s[44:47], s100 offen offset:128\n"
    "s_waitcnt vmcnt(0)\n"
    "s_nop 1\n"
    "v_mfma_f32_16x16x32_bf16 v[176:179], v[96:99], v[128:131], v[176:179]\n"
    "v_mfma_f32_16x16x32_bf16 v[180:183], v[96:99], v[132:135], v[180:183]\n"
    "s_nop 7\n"
    "buffer_load_dwordx4 v[96:99], v100, s[44:47], s100 offen offset:192\n"
    "s_waitcnt vmcnt(0)\n"
    "s_nop 1\n"
    "v_mfma_f32_16x16x32_bf16 v[176:179], v[96:99], v[136:139], v[176:179]\n"
    "v_mfma_f32_16x16x32_bf16 v[180:183], v[96:99], v[140:143], v[180:183]\n"
    "s_nop 7\n"
    "buffer_load_dwordx4 v[96:99], v100, s[44:47], s100 offen offset:256\n"
    "s_waitcnt vmcnt(0)\n"
    "s_nop 1\n"
    "v_mfma_f32_16x16x32_bf16 v[176:179], v[96:99], v[144:147], v[176:179]\n"
    "v_mfma_f32_16x16x32_bf16 v[180:183], v[96:99], v[148:151], v[180:183]\n"
    "s_nop 7\n"
    "buffer_load_dwordx4 v[96:99], v100, s[44:47], s100 offen offset:320\n"
    "s_waitcnt vmcnt(0)\n"
    "s_nop 1\n"
    "v_mfma_f32_16x16x32_bf16 v[176:179], v[96:99], v[152:155], v[176:179]\n"
    "v_mfma_f32_16x16x32_bf16 v[180:183], v[96:99], v[156:159], v[180:183]\n"
    "s_nop 7\n"
    "s_xor_b32 s100, s18, 1\n"
    "s_mul_i32 s100, s100, 6144\n"
    "s_add_u32 s100, s100, 0x24000\n"
    "buffer_load_dwordx4 v[96:99], v100, s[44:47], s100 offen\n"
    "s_waitcnt vmcnt(0)\n"
    "s_nop 1\n"
    "v_mfma_f32_16x16x32_bf16 v[184:187], v[96:99], v[160:163], 0\n"
    "v_mfma_f32_16x16x32_bf16 v[188:191], v[96:99], v[164:167], 0\n"
    "s_nop 7\n"
    "buffer_load_dwordx4 v[96:99], v100, s[44:47], s100 offen offset:64\n"
    "s_waitcnt vmcnt(0)\n"
    "s_nop 1\n"
    "v_mfma_f32_16x16x32_bf16 v[184:187], v[96:99], v[168:171], v[184:187]\n"
    "v_mfma_f32_16x16x32_bf16 v[188:191], v[96:99], v[172:175], v[188:191]\n"
    "s_nop 7\n"
    "buffer_load_dwordx4 v[96:99], v100, s[44:47], s100 offen offset:128\n"
    "s_waitcnt vmcnt(0)\n"
    "s_nop 1\n"
    "v_mfma_f32_16x16x32_bf16 v[184:187], v[96:99], v[128:131], v[184:187]\n"
    "v_mfma_f32_16x16x32_bf16 v[188:191], v[96:99], v[132:135], v[188:191]\n"
    "s_nop 7\n"
    "buffer_load_dwordx4 v[96:99], v100, s[44:47], s100 offen offset:192\n"
    "s_waitcnt vmcnt(0)\n"
    "s_nop 1\n"
    "v_mfma_f32_16x16x32_bf16 v[184:187], v[96:99], v[136:139], v[184:187]\n"
    "v_mfma_f32_16x16x32_bf16 v[188:191], v[96:99], v[140:143], v[188:191]\n"
    "s_nop 7\n"
    "buffer_load_dwordx4 v[96:99], v100, s[44:47], s100 offen offset:256\n"
    "s_waitcnt vmcnt(0)\n"
    "s_nop 1\n"
    "v_mfma_f32_16x16x32_bf16 v[184:187], v[96:99], v[144:147], v[184:187]\n"
    "v_mfma_f32_16x16x32_bf16 v[188:191], v[96:99], v[148:151], v[188:191]\n"
    "s_nop 7\n"
    "buffer_load_dwordx4 v[96:99], v100, s[44:47], s100 offen offset:320\n"
    "s_waitcnt vmcnt(0)\n"
    "s_nop 1\n"
    "v_mfma_f32_16x16x32_bf16 v[184:187], v[96:99], v[152:155], v[184:187]\n"
    "v_mfma_f32_16x16x32_bf16 v[188:191], v[96:99], v[156:159], v[188:191]\n"
    "s_nop 7\n"
    "s_nop 7\n"
    "s_nop 7\n"
    "v_max3_f32 v124, v176, v177, v178\n"
    "v_max3_f32 v124, v124, v179, v184\n"
    "v_max3_f32 v124, v124, v185, v186\n"
    "v_max_f32_e32 v124, v124, v187\n"
    "v_max3_f32 v125, v180, v181, v182\n"
    "v_max3_f32 v125, v125, v183, v188\n"
    "v_max3_f32 v125, v125, v189, v190\n"
    "v_max_f32_e32 v125, v125, v191\n"
    "v_mov_b32_e32 v96, v124\n"
    "s_nop 1\n"
    "v_permlane16_swap_b32_e32 v124, v96\n"
    "v_max_f32_e32 v124, v124, v96\n"
    "v_mov_b32_e32 v96, v124\n"
    "s_nop 1\n"
    "v_permlane32_swap_b32_e32 v124, v96\n"
    "v_max_f32_e32 v124, v124, v96\n"
    "v_mov_b32_e32 v96, v125\n"
    "s_nop 1\n"
    "v_permlane16_swap_b32_e32 v125, v96\n"
    "v_max_f32_e32 v125, v125, v96\n"
    "v_mov_b32_e32 v96, v125\n"
    "s_nop 1\n"
    "v_permlane32_swap_b32_e32 v125, v96\n"
    "v_max_f32_e32 v125, v125, v96\n"
    "v_floor_f32_e64 v124, -v124\n"
    "v_min_f32_e32 v124, v124, v112\n"
    "v_sub_f32_e32 v96, v112, v124\n"
    "v_mov_b32_e32 v112, v124\n"
    "v_sub_f32_e32 v192, v192, v96\n"
    "v_sub_f32_e32 v193, v193, v96\n"
    "v_sub_f32_e32 v194, v194, v96\n"
    "v_sub_f32_e32 v195, v195, v96\n"
    "v_sub_f32_e32 v200, v200, v96\n"
    "v_sub_f32_e32 v201, v201, v96\n"
    "v_sub_f32_e32 v202, v202, v96\n"
    "v_sub_f32_e32 v203, v203, v96\n"
    "v_min_f32_e32 v96, 0x437f0000, v96\n"
    "v_cvt_u32_f32_e32 v96, v96\n"
    "v_sub_u32_e32 v124, 0, v96\n"
    "v_ldexp_f32 v252, v252, v124\n"
    "v_lshlrev_b32_e32 v96, 7, v96\n"
    "v_lshl_or_b32 v96, v96, 16, v96\n"
    "v_pk_sub_u16 v0, v0, v96 clamp\n"
    "v_pk_sub_u16 v1, v1, v96 clamp\n"
    "v_pk_sub_u16 v2, v2, v96 clamp\n"
    "v_pk_sub_u16 v3, v3, v96 clamp\n"
    "v_pk_sub_u16 v8, v8, v96 clamp\n"
    "v_pk_sub_u16 v9, v9, v96 clamp\n"
    "v_pk_sub_u16 v10, v10, v96 clamp\n"
    "v_pk_sub_u16 v11, v11, v96 clamp\n"
    "v_pk_sub_u16 v16, v16, v96 clamp\n"
    "v_pk_sub_u16 v17, v17, v96 clamp\n"
    "v_pk_sub_u16 v18, v18, v96 clamp\n"
    "v_pk_sub_u16 v19, v19, v96 clamp\n"
    "v_pk_sub_u16 v24, v24, v96 clamp\n"
    "v_pk_sub_u16 v25, v25, v96 clamp\n"
    "v_pk_sub_u16 v26, v26, v96 clamp\n"
    "v_pk_sub_u16 v27, v27, v96 clamp\n"
    "v_pk_sub_u16 v32, v32, v96 clamp\n"
    "v_pk_sub_u16 v33, v33, v96 clamp\n"
    "v_pk_sub_u16 v34, v34, v96 clamp\n"
    "v_pk_sub_u16 v35, v35, v96 clamp\n"
    "v_pk_sub_u16 v40, v40, v96 clamp\n"
    "v_pk_sub_u16 v41, v41, v96 clamp\n"
    "v_pk_sub_u16 v42, v42, v96 clamp\n"
    "v_pk_sub_u16 v43, v43, v96 clamp\n"
    "v_pk_sub_u16 v48, v48, v96 clamp\n"
    "v_pk_sub_u16 v49, v49, v96 clamp\n"
    "v_pk_sub_u16 v50, v50, v96 clamp\n"
    "v_pk_sub_u16 v51, v51, v96 clamp\n"
    "v_pk_sub_u16 v56, v56, v96 clamp\n"
    "v_pk_sub_u16 v57, v57, v96 clamp\n"
    "v_pk_sub_u16 v58, v58, v96 clamp\n"
    "v_pk_sub_u16 v59, v59, v96 clamp\n"
    "v_pk_sub_u16 v64, v64, v96 clamp\n"
    "v_pk_sub_u16 v65, v65, v96 clamp\n"
    "v_pk_sub_u16 v66, v66, v96 clamp\n"
    "v_pk_sub_u16 v67, v67, v96 clamp\n"
    "v_pk_sub_u16 v72, v72, v96 clamp\n"
    "v_pk_sub_u16 v73, v73, v96 clamp\n"
    "v_pk_sub_u16 v74, v74, v96 clamp\n"
    "v_pk_sub_u16 v75, v75, v96 clamp\n"
    "v_pk_sub_u16 v80, v80, v96 clamp\n"
    "v_pk_sub_u16 v81, v81, v96 clamp\n"
    "v_pk_sub_u16 v82, v82, v96 clamp\n"
    "v_pk_sub_u16 v83, v83, v96 clamp\n"
    "v_pk_sub_u16 v88, v88, v96 clamp\n"
    "v_pk_sub_u16 v89, v89, v96 clamp\n"
    "v_pk_sub_u16 v90, v90, v96 clamp\n"
    "v_pk_sub_u16 v91, v91, v96 clamp\n"
    "v_floor_f32_e64 v125, -v125\n"
    "v_min_f32_e32 v125, v125, v116\n"
    "v_sub_f32_e32 v96, v116, v125\n"
    "v_mov_b32_e32 v116, v125\n"
    "v_sub_f32_e32 v196, v196, v96\n"
    "v_sub_f32_e32 v197, v197, v96\n"
    "v_sub_f32_e32 v198, v198, v96\n"
    "v_sub_f32_e32 v199, v199, v96\n"
    "v_sub_f32_e32 v204, v204, v96\n"
    "v_sub_f32_e32 v205, v205, v96\n"
    "v_sub_f32_e32 v206, v206, v96\n"
    "v_sub_f32_e32 v207, v207, v96\n"
    "v_min_f32_e32 v96, 0x437f0000, v96\n"
    "v_cvt_u32_f32_e32 v96, v96\n"
    "v_sub_u32_e32 v125, 0, v96\n"
    "v_ldexp_f32 v253, v253, v125\n"
    "v_lshlrev_b32_e32 v96, 7, v96\n"
    "v_lshl_or_b32 v96, v96, 16, v96\n"
    "v_pk_sub_u16 v4, v4, v96 clamp\n"
    "v_pk_sub_u16 v5, v5, v96 clamp\n"
    "v_pk_sub_u16 v6, v6, v96 clamp\n"
    "v_pk_sub_u16 v7, v7, v96 clamp\n"
    "v_pk_sub_u16 v12, v12, v96 clamp\n"
    "v_pk_sub_u16 v13, v13, v96 clamp\n"
    "v_pk_sub_u16 v14, v14, v96 clamp\n"
    "v_pk_sub_u16 v15, v15, v96 clamp\n"
    "v_pk_sub_u16 v20, v20, v96 clamp\n"
    "v_pk_sub_u16 v21, v21, v96 clamp\n"
    "v_pk_sub_u16 v22, v22, v96 clamp\n"
    "v_pk_sub_u16 v23, v23, v96 clamp\n"
    "v_pk_sub_u16 v28, v28, v96 clamp\n"
    "v_pk_sub_u16 v29, v29, v96 clamp\n"
    "v_pk_sub_u16 v30, v30, v96 clamp\n"
    "v_pk_sub_u16 v31, v31, v96 clamp\n"
    "v_pk_sub_u16 v36, v36, v96 clamp\n"
    "v_pk_sub_u16 v37, v37, v96 clamp\n"
    "v_pk_sub_u16 v38, v38, v96 clamp\n"
    "v_pk_sub_u16 v39, v39, v96 clamp\n"
    "v_pk_sub_u16 v44, v44, v96 clamp\n"
    "v_pk_sub_u16 v45, v45, v96 clamp\n"
    "v_pk_sub_u16 v46, v46, v96 clamp\n"
    "v_pk_sub_u16 v47, v47, v96 clamp\n"
    "v_pk_sub_u16 v52, v52, v96 clamp\n"
    "v_pk_sub_u16 v53, v53, v96 clamp\n"
    "v_pk_sub_u16 v54, v54, v96 clamp\n"
    "v_pk_sub_u16 v55, v55, v96 clamp\n"
    "v_pk_sub_u16 v60, v60, v96 clamp\n"
    "v_pk_sub_u16 v61, v61, v96 clamp\n"
    "v_pk_sub_u16 v62, v62, v96 clamp\n"
    "v_pk_sub_u16 v63, v63, v96 clamp\n"
    "v_pk_sub_u16 v68, v68, v96 clamp\n"
    "v_pk_sub_u16 v69, v69, v96 clamp\n"
    "v_pk_sub_u16 v70, v70, v96 clamp\n"
    "v_pk_sub_u16 v71, v71, v96 clamp\n"
    "v_pk_sub_u16 v76, v76, v96 clamp\n"
    "v_pk_sub_u16 v77, v77, v96 clamp\n"
    "v_pk_sub_u16 v78, v78, v96 clamp\n"
    "v_pk_sub_u16 v79, v79, v96 clamp\n"
    "v_pk_sub_u16 v84, v84, v96 clamp\n"
    "v_pk_sub_u16 v85, v85, v96 clamp\n"
    "v_pk_sub_u16 v86, v86, v96 clamp\n"
    "v_pk_sub_u16 v87, v87, v96 clamp\n"
    "v_pk_sub_u16 v92, v92, v96 clamp\n"
    "v_pk_sub_u16 v93, v93, v96 clamp\n"
    "v_pk_sub_u16 v94, v94, v96 clamp\n"
    "v_pk_sub_u16 v95, v95, v96 clamp\n"
    "v_mov_b32_e32 v113, v112\n"
    "v_mov_b32_e32 v114, v112\n"
    "v_mov_b32_e32 v115, v112\n"
    "v_mov_b32_e32 v117, v116\n"
    "v_mov_b32_e32 v118, v116\n"
    "v_mov_b32_e32 v119, v116\n"
    "v_add_f32_e32 v176, v112, v176\n"
    "v_add_f32_e32 v177, v112, v177\n"
    "v_add_f32_e32 v178, v112, v178\n"
    "v_exp_f32_e32 v176, v176\n"
    "v_add_f32_e32 v179, v112, v179\n"
    "v_exp_f32_e32 v177, v177\n"
    "v_add_f32_e32 v180, v116, v180\n"
    "v_exp_f32_e32 v178, v178\n"
    "v_add_f32_e32 v181, v116, v181\n"
    "v_exp_f32_e32 v179, v179\n"
    "v_add_f32_e32 v124, v176, v177\n"
    "v_cvt_pk_bf16_f32 v96, v176, v177\n"
    "v_add_f32_e32 v182, v116, v182\n"
    "v_exp_f32_e32 v180, v180\n"
    "v_add_f32_e32 v124, v178, v124\n"
    "v_add_f32_e32 v183, v116, v183\n"
    "v_exp_f32_e32 v181, v181\n"
    "v_add_f32_e32 v124, v179, v124\n"
    "v_cvt_pk_bf16_f32 v97, v178, v179\n"
    "v_add_f32_e32 v184, v112, v184\n"
    "v_exp_f32_e32 v182, v182\n"
    "v_add_f32_e32 v185, v112, v185\n"
    "v_exp_f32_e32 v183, v183\n"
    "v_add_f32_e32 v125, v180, v181\n"
    "v_cvt_pk_bf16_f32 v100, v180, v181\n"
    "v_add_f32_e32 v186, v112, v186\n"
    "v_exp_f32_e32 v184, v184\n"
    "v_add_f32_e32 v125, v182, v125\n"
    "v_add_f32_e32 v187, v112, v187\n"
    "v_exp_f32_e32 v185, v185\n"
    "v_add_f32_e32 v125, v183, v125\n"
    "v_cvt_pk_bf16_f32 v101, v182, v183\n"
    "v_add_f32_e32 v188, v116, v188\n"
    "v_exp_f32_e32 v186, v186\n"
    "v_add_f32_e32 v124, v184, v124\n"
    "v_add_f32_e32 v189, v116, v189\n"
    "v_exp_f32_e32 v187, v187\n"
    "v_add_f32_e32 v124, v185, v124\n"
    "v_cvt_pk_bf16_f32 v98, v184, v185\n"
    "v_add_f32_e32 v190, v116, v190\n"
    "v_exp_f32_e32 v188, v188\n"
    "v_add_f32_e32 v124, v186, v124\n"
    "v_add_f32_e32 v191, v116, v191\n"
    "v_exp_f32_e32 v189, v189\n"
    "v_add_f32_e32 v124, v187, v124\n"
    "v_cvt_pk_bf16_f32 v99, v186, v187\n"
    "v_exp_f32_e32 v190, v190\n"
    "v_add_f32_e32 v125, v188, v125\n"
    "v_exp_f32_e32 v191, v191\n"
    "v_add_f32_e32 v125, v189, v125\n"
    "v_cvt_pk_bf16_f32 v102, v188, v189\n"
    "v_add_f32_e32 v125, v190, v125\n"
    "v_add_f32_e32 v125, v191, v125\n"
    "v_cvt_pk_bf16_f32 v103, v190, v191\n"
    "s_nop 7\n"
    "s_nop 7\n"
    "s_branch .Lback_a12\n"
    ".Lrare_a13:\n"
    "s_nop 7\n"
    "s_nop 7\n"
    "v_mbcnt_lo_u32_b32 v124, -1, 0\n"
    "v_mbcnt_hi_u32_b32 v124, -1, v124\n"
    "v_and_b32_e32 v125, 15, v124\n"
    "v_mul_u32_u24_e32 v125, 0x180, v125\n"
    "v_lshrrev_b32_e32 v124, 4, v124\n"
    "v_lshl_add_u32 v108, v124, 4, v125\n"
    "s_xor_b32 s100, s18, 2\n"
    "s_mul_i32 s100, s100, 6144\n"
    "s_add_u32 s100, s100, 0x24000\n"
    "buffer_load_dwordx4 v[104:107], v108, s[44:47], s100 offen\n"
    "s_waitcnt vmcnt(0)\n"
    "s_nop 1\n"
    "v_mfma_f32_16x16x32_bf16 v[192:195], v[104:107], v[160:163], 0\n"
    "v_mfma_f32_16x16x32_bf16 v[196:199], v[104:107], v[164:167], 0\n"
    "s_nop 7\n"
    "buffer_load_dwordx4 v[104:107], v108, s[44:47], s100 offen offset:64\n"
    "s_waitcnt vmcnt(0)\n"
    "s_nop 1\n"
    "v_mfma_f32_16x16x32_bf16 v[192:195], v[104:107], v[168:171], v[192:195]\n"
    "v_mfma_f32_16x16x32_bf16 v[196:199], v[104:107], v[172:175], v[196:199]\n"
    "s_nop 7\n"
    "buffer_load_dwordx4 v[104:107], v108, s[44:47], s100 offen offset:128\n"
    "s_waitcnt vmcnt(0)\n"
    "s_nop 1\n"
    "v_mfma_f32_16x16x32_bf16 v[192:195], v[104:107], v[128:131], v[192:195]\n"
    "v_mfma_f32_16x16x32_bf16 v[196:199], v[104:107], v[132:135], v[196:199]\n"
    "s_nop 7\n"
    "buffer_load_dwordx4 v[104:107], v108, s[44:47], s100 offen offset:192\n"
    "s_waitcnt vmcnt(0)\n"
    "s_nop 1\n"
    "v_mfma_f32_16x16x32_bf16 v[192:195], v[104:107], v[136:139], v[192:195]\n"
    "v_mfma_f32_16x16x32_bf16 v[196:199], v[104:107], v[140:143], v[196:199]\n"
    "s_nop 7\n"
    "buffer_load_dwordx4 v[104:107], v108, s[44:47], s100 offen offset:256\n"
    "s_waitcnt vmcnt(0)\n"
    "s_nop 1\n"
    "v_mfma_f32_16x16x32_bf16 v[192:195], v[104:107], v[144:147], v[192:195]\n"
    "v_mfma_f32_16x16x32_bf16 v[196:199], v[104:107], v[148:151], v[196:199]\n"
    "s_nop 7\n"
    "buffer_load_dwordx4 v[104:107], v108, s[44:47], s100 offen offset:320\n"
    "s_waitcnt vmcnt(0)\n"
    "s_nop 1\n"
    "v_mfma_f32_16x16x32_bf16 v[192:195], v[104:107], v[152:155], v[192:195]\n"
    "v_mfma_f32_16x16x32_bf16 v[196:199], v[104:107], v[156:159], v[196:199]\n"
    "s_nop 7\n"
    "s_xor_b32 s100, s18, 3\n"
    "s_mul_i32 s100, s100, 6144\n"
    "s_add_u32 s100, s100, 0x24000\n"
    "buffer_load_dwordx4 v[104:107], v108, s[44:47], s100 offen\n"
    "s_waitcnt vmcnt(0)\n"
    "s_nop 1\n"
    "v_mfma_f32_16x16x32_bf16 v[200:203], v[104:107], v[160:163], 0\n"
    "v_mfma_f32_16x16x32_bf16 v[204:207], v[104:107], v[164:167], 0\n"
    "s_nop 7\n"
    "buffer_load_dwordx4 v[104:107], v108, s[44:47], s100 offen offset:64\n"
    "s_waitcnt vmcnt(0)\n"
    "s_nop 1\n"
    "v_mfma_f32_16x16x32_bf16 v[200:203], v[104:107], v[168:171], v[200:203]\n"
    "v_mfma_f32_16x16x32_bf16 v[204:207], v[104:107], v[172:175], v[204:207]\n"
    "s_nop 7\n"
    "buffer_load_dwordx4 v[104:107], v108, s[44:47], s100 offen offset:128\n"
    "s_waitcnt vmcnt(0)\n"
    "s_nop 1\n"
    "v_mfma_f32_16x16x32_bf16 v[200:203], v[104:107], v[128:131], v[200:203]\n"
    "v_mfma_f32_16x16x32_bf16 v[204:207], v[104:107], v[132:135], v[204:207]\n"
    "s_nop 7\n"
    "buffer_load_dwordx4 v[104:107], v108, s[44:47], s100 offen offset:192\n"
    "s_waitcnt vmcnt(0)\n"
    "s_nop 1\n"
    "v_mfma_f32_16x16x32_bf16 v[200:203], v[104:107], v[136:139], v[200:203]\n"
    "v_mfma_f32_16x16x32_bf16 v[204:207], v[104:107], v[140:143], v[204:207]\n"
    "s_nop 7\n"
    "buffer_load_dwordx4 v[104:107], v108, s[44:47], s100 offen offset:256\n"
    "s_waitcnt vmcnt(0)\n"
    "s_nop 1\n"
    "v_mfma_f32_16x16x32_bf16 v[200:203], v[104:107], v[144:147], v[200:203]\n"
    "v_mfma_f32_16x16x32_bf16 v[204:207], v[104:107], v[148:151], v[204:207]\n"
    "s_nop 7\n"
    "buffer_load_dwordx4 v[104:107], v108, s[44:47], s100 offen offset:320\n"
    "s_waitcnt vmcnt(0)\n"
    "s_nop 1\n"
    "v_mfma_f32_16x16x32_bf16 v[200:203], v[104:107], v[152:155], v[200:203]\n"
    "v_mfma_f32_16x16x32_bf16 v[204:207], v[104:107], v[156:159], v[204:207]\n"
    "s_nop 7\n"
    "s_nop 7\n"
    "s_nop 7\n"
    "v_max3_f32 v124, v192, v193, v194\n"
    "v_max3_f32 v124, v124, v195, v200\n"
    "v_max3_f32 v124, v124, v201, v202\n"
    "v_max_f32_e32 v124, v124, v203\n"
    "v_max3_f32 v125, v196, v197, v198\n"
    "v_max3_f32 v125, v125, v199, v204\n"
    "v_max3_f32 v125, v125, v205, v206\n"
    "v_max_f32_e32 v125, v125, v207\n"
    "v_mov_b32_e32 v104, v124\n"
    "s_nop 1\n"
    "v_permlane16_swap_b32_e32 v124, v104\n"
    "v_max_f32_e32 v124, v124, v104\n"
    "v_mov_b32_e32 v104, v124\n"
    "s_nop 1\n"
    "v_permlane32_swap_b32_e32 v124, v104\n"
    "v_max_f32_e32 v124, v124, v104\n"
    "v_mov_b32_e32 v104, v125\n"
    "s_nop 1\n"
    "v_permlane16_swap_b32_e32 v125, v104\n"
    "v_max_f32_e32 v125, v125, v104\n"
    "v_mov_b32_e32 v104, v125\n"
    "s_nop 1\n"
    "v_permlane32_swap_b32_e32 v125, v104\n"
    "v_max_f32_e32 v125, v125, v104\n"
    "v_floor_f32_e64 v124, -v124\n"
    "v_min_f32_e32 v124, v124, v112\n"
    "v_sub_f32_e32 v104, v112, v124\n"
    "v_mov_b32_e32 v112, v124\n"
    "v_sub_f32_e32 v176, v176, v104\n"
    "v_sub_f32_e32 v177, v177, v104\n"
    "v_sub_f32_e32 v178, v178, v104\n"
    "v_sub_f32_e32 v179, v179, v104\n"
    "v_sub_f32_e32 v184, v184, v104\n"
    "v_sub_f32_e32 v185, v185, v104\n"
    "v_sub_f32_e32 v186, v186, v104\n"
    "v_sub_f32_e32 v187, v187, v104\n"
    "v_min_f32_e32 v104, 0x437f0000, v104\n"
    "v_cvt_u32_f32_e32 v104, v104\n"
    "v_sub_u32_e32 v124, 0, v104\n"
    "v_ldexp_f32 v252, v252, v124\n"
    "v_lshlrev_b32_e32 v104, 7, v104\n"
    "v_lshl_or_b32 v104, v104, 16, v104\n"
    "v_pk_sub_u16 v0, v0, v104 clamp\n"
    "v_pk_sub_u16 v1, v1, v104 clamp\n"
    "v_pk_sub_u16 v2, v2, v104 clamp\n"
    "v_pk_sub_u16 v3, v3, v104 clamp\n"
    "v_pk_sub_u16 v8, v8, v104 clamp\n"
    "v_pk_sub_u16 v9, v9, v104 clamp\n"
    "v_pk_sub_u16 v10, v10, v104 clamp\n"
    "v_pk_sub_u16 v11, v11, v104 clamp\n"
    "v_pk_sub_u16 v16, v16, v104 clamp\n"
    "v_pk_sub_u16 v17, v17, v104 clamp\n"
    "v_pk_sub_u16 v18, v18, v104 clamp\n"
    "v_pk_sub_u16 v19, v19, v104 clamp\n"
    "v_pk_sub_u16 v24, v24, v104 clamp\n"
    "v_pk_sub_u16 v25, v25, v104 clamp\n"
    "v_pk_sub_u16 v26, v26, v104 clamp\n"
    "v_pk_sub_u16 v27, v27, v104 clamp\n"
    "v_pk_sub_u16 v32, v32, v104 clamp\n"
    "v_pk_sub_u16 v33, v33, v104 clamp\n"
    "v_pk_sub_u16 v34, v34, v104 clamp\n"
    "v_pk_sub_u16 v35, v35, v104 clamp\n"
    "v_pk_sub_u16 v40, v40, v104 clamp\n"
    "v_pk_sub_u16 v41, v41, v104 clamp\n"
    "v_pk_sub_u16 v42, v42, v104 clamp\n"
    "v_pk_sub_u16 v43, v43, v104 clamp\n"
    "v_pk_sub_u16 v48, v48, v104 clamp\n"
    "v_pk_sub_u16 v49, v49, v104 clamp\n"
    "v_pk_sub_u16 v50, v50, v104 clamp\n"
    "v_pk_sub_u16 v51, v51, v104 clamp\n"
    "v_pk_sub_u16 v56, v56, v104 clamp\n"
    "v_pk_sub_u16 v57, v57, v104 clamp\n"
    "v_pk_sub_u16 v58, v58, v104 clamp\n"
    "v_pk_sub_u16 v59, v59, v104 clamp\n"
    "v_pk_sub_u16 v64, v64, v104 clamp\n"
    "v_pk_sub_u16 v65, v65, v104 clamp\n"
    "v_pk_sub_u16 v66, v66, v104 clamp\n"
    "v_pk_sub_u16 v67, v67, v104 clamp\n"
    "v_pk_sub_u16 v72, v72, v104 clamp\n"
    "v_pk_sub_u16 v73, v73, v104 clamp\n"
    "v_pk_sub_u16 v74, v74, v104 clamp\n"
    "v_pk_sub_u16 v75, v75, v104 clamp\n"
    "v_pk_sub_u16 v80, v80, v104 clamp\n"
    "v_pk_sub_u16 v81, v81, v104 clamp\n"
    "v_pk_sub_u16 v82, v82, v104 clamp\n"
    "v_pk_sub_u16 v83, v83, v104 clamp\n"
    "v_pk_sub_u16 v88, v88, v104 clamp\n"
    "v_pk_sub_u16 v89, v89, v104 clamp\n"
    "v_pk_sub_u16 v90, v90, v104 clamp\n"
    "v_pk_sub_u16 v91, v91, v104 clamp\n"
    "v_pk_sub_u16 v96, v96, v104 clamp\n"
    "v_pk_sub_u16 v97, v97, v104 clamp\n"
    "v_pk_sub_u16 v98, v98, v104 clamp\n"
    "v_pk_sub_u16 v99, v99, v104 clamp\n"
    "v_floor_f32_e64 v125, -v125\n"
    "v_min_f32_e32 v125, v125, v116\n"
    "v_sub_f32_e32 v104, v116, v125\n"
    "v_mov_b32_e32 v116, v125\n"
    "v_sub_f32_e32 v180, v180, v104\n"
    "v_sub_f32_e32 v181, v181, v104\n"
    "v_sub_f32_e32 v182, v182, v104\n"
    "v_sub_f32_e32 v183, v183, v104\n"
    "v_sub_f32_e32 v188, v188, v104\n"
    "v_sub_f32_e32 v189, v189, v104\n"
    "v_sub_f32_e32 v190, v190, v104\n"
    "v_sub_f32_e32 v191, v191, v104\n"
    "v_min_f32_e32 v104, 0x437f0000, v104\n"
    "v_cvt_u32_f32_e32 v104, v104\n"
    "v_sub_u32_e32 v125, 0, v104\n"
    "v_ldexp_f32 v253, v253, v125\n"
    "v_lshlrev_b32_e32 v104, 7, v104\n"
    "v_lshl_or_b32 v104, v104, 16, v104\n"
    "v_pk_sub_u16 v4, v4, v104 clamp\n"
    "v_pk_sub_u16 v5, v5, v104 clamp\n"
    "v_pk_sub_u16 v6, v6, v104 clamp\n"
    "v_pk_sub_u16 v7, v7, v104 clamp\n"
    "v_pk_sub_u16 v12, v12, v104 clamp\n"
    "v_pk_sub_u16 v13, v13, v104 clamp\n"
    "v_pk_sub_u16 v14, v14, v104 clamp\n"
    "v_pk_sub_u16 v15, v15, v104 clamp\n"
    "v_pk_sub_u16 v20, v20, v104 clamp\n"
    "v_pk_sub_u16 v21, v21, v104 clamp\n"
    "v_pk_sub_u16 v22, v22, v104 clamp\n"
    "v_pk_sub_u16 v23, v23, v104 clamp\n"
    "v_pk_sub_u16 v28, v28, v104 clamp\n"
    "v_pk_sub_u16 v29, v29, v104 clamp\n"
    "v_pk_sub_u16 v30, v30, v104 clamp\n"
    "v_pk_sub_u16 v31, v31, v104 clamp\n"
    "v_pk_sub_u16 v36, v36, v104 clamp\n"
    "v_pk_sub_u16 v37, v37, v104 clamp\n"
    "v_pk_sub_u16 v38, v38, v104 clamp\n"
    "v_pk_sub_u16 v39, v39, v104 clamp\n"
    "v_pk_sub_u16 v44, v44, v104 clamp\n"
    "v_pk_sub_u16 v45, v45, v104 clamp\n"
    "v_pk_sub_u16 v46, v46, v104 clamp\n"
    "v_pk_sub_u16 v47, v47, v104 clamp\n"
    "v_pk_sub_u16 v52, v52, v104 clamp\n"
    "v_pk_sub_u16 v53, v53, v104 clamp\n"
    "v_pk_sub_u16 v54, v54, v104 clamp\n"
    "v_pk_sub_u16 v55, v55, v104 clamp\n"
    "v_pk_sub_u16 v60, v60, v104 clamp\n"
    "v_pk_sub_u16 v61, v61, v104 clamp\n"
    "v_pk_sub_u16 v62, v62, v104 clamp\n"
    "v_pk_sub_u16 v63, v63, v104 clamp\n"
    "v_pk_sub_u16 v68, v68, v104 clamp\n"
    "v_pk_sub_u16 v69, v69, v104 clamp\n"
    "v_pk_sub_u16 v70, v70, v104 clamp\n"
    "v_pk_sub_u16 v71, v71, v104 clamp\n"
    "v_pk_sub_u16 v76, v76, v104 clamp\n"
    "v_pk_sub_u16 v77, v77, v104 clamp\n"
    "v_pk_sub_u16 v78, v78, v104 clamp\n"
    "v_pk_sub_u16 v79, v79, v104 clamp\n"
    "v_pk_sub_u16 v84, v84, v104 clamp\n"
    "v_pk_sub_u16 v85, v85, v104 clamp\n"
    "v_pk_sub_u16 v86, v86, v104 clamp\n"
    "v_pk_sub_u16 v87, v87, v104 clamp\n"
    "v_pk_sub_u16 v92, v92, v104 clamp\n"
    "v_pk_sub_u16 v93, v93, v104 clamp\n"
    "v_pk_sub_u16 v94, v94, v104 clamp\n"
    "v_pk_sub_u16 v95, v95, v104 clamp\n"
    "v_pk_sub_u16 v100, v100, v104 clamp\n"
    "v_pk_sub_u16 v101, v101, v104 clamp\n"
    "v_pk_sub_u16 v102, v102, v104 clamp\n"
    "v_pk_sub_u16 v103, v103, v104 clamp\n"
    "v_mov_b32_e32 v113, v112\n"
    "v_mov_b32_e32 v114, v112\n"
    "v_mov_b32_e32 v115, v112\n"
    "v_mov_b32_e32 v117, v116\n"
    "v_mov_b32_e32 v118, v116\n"
    "v_mov_b32_e32 v119, v116\n"
    "v_add_f32_e32 v192, v112, v192\n"
    "v_add_f32_e32 v193, v112, v193\n"
    "v_add_f32_e32 v194, v112, v194\n"
    "v_exp_f32_e32 v192, v192\n"
    "v_add_f32_e32 v195, v112, v195\n"
    "v_exp_f32_e32 v193, v193\n"
    "v_add_f32_e32 v196, v116, v196\n"
    "v_exp_f32_e32 v194, v194\n"
    "v_add_f32_e32 v197, v116, v197\n"
    "v_exp_f32_e32 v195, v195\n"
    "v_add_f32_e32 v124, v192, v193\n"
    "v_cvt_pk_bf16_f32 v104, v192, v193\n"
    "v_add_f32_e32 v198, v116, v198\n"
    "v_exp_f32_e32 v196, v196\n"
    "v_add_f32_e32 v124, v194, v124\n"
    "v_add_f32_e32 v199, v116, v199\n"
    "v_exp_f32_e32 v197, v197\n"
    "v_add_f32_e32 v124, v195, v124\n"
    "v_cvt_pk_bf16_f32 v105, v194, v195\n"
    "v_add_f32_e32 v200, v112, v200\n"
    "v_exp_f32_e32 v198, v198\n"
    "v_add_f32_e32 v201, v112, v201\n"
    "v_exp_f32_e32 v199, v199\n"
    "v_add_f32_e32 v125, v196, v197\n"
    "v_cvt_pk_bf16_f32 v108, v196, v197\n"
    "v_add_f32_e32 v202, v112, v202\n"
    "v_exp_f32_e32 v200, v200\n"
    "v_add_f32_e32 v125, v198, v125\n"
    "v_add_f32_e32 v203, v112, v203\n"
    "v_exp_f32_e32 v201, v201\n"
    "v_add_f32_e32 v125, v199, v125\n"
    "v_cvt_pk_bf16_f32 v109, v198, v199\n"
    "v_add_f32_e32 v204, v116, v204\n"
    "v_exp_f32_e32 v202, v202\n"
    "v_add_f32_e32 v124, v200, v124\n"
    "v_add_f32_e32 v205, v116, v205\n"
    "v_exp_f32_e32 v203, v203\n"
    "v_add_f32_e32 v124, v201, v124\n"
    "v_cvt_pk_bf16_f32 v106, v200, v201\n"
    "v_add_f32_e32 v206, v116, v206\n"
    "v_exp_f32_e32 v204, v204\n"
    "v_add_f32_e32 v124, v202, v124\n"
    "v_add_f32_e32 v207, v116, v207\n"
    "v_exp_f32_e32 v205, v205\n"
    "v_add_f32_e32 v124, v203, v124\n"
    "v_cvt_pk_bf16_f32 v107, v202, v203\n"
    "v_exp_f32_e32 v206, v206\n"
    "v_add_f32_e32 v125, v204, v125\n"
    "v_exp_f32_e32 v207, v207\n"
    "v_add_f32_e32 v125, v205, v125\n"
    "v_cvt_pk_bf16_f32 v110, v204, v205\n"
    "v_add_f32_e32 v125, v206, v125\n"
    "v_add_f32_e32 v125, v207, v125\n"
    "v_cvt_pk_bf16_f32 v111, v206, v207\n"
    "s_nop 7\n"
    "s_nop 7\n"
    "s_branch .Lback_a13\n"
    ".Lrare_n7:\n"
    "s_nop 7\n"
    "s_nop 7\n"
    "v_mbcnt_lo_u32_b32 v168, -1, 0\n"
    "v_mbcnt_hi_u32_b32 v168, -1, v168\n"
    "v_and_b32_e32 v169, 15, v168\n"
    "v_lshrrev_b32_e32 v168, 4, v168\n"
    "v_lshrrev_b32_e32 v174, 1, v169\n"
    "v_xor_b32_e32 v174, v174, v168\n"
    "v_lshlrev_b32_e32 v174, 4, v174\n"
    "v_lshl_add_u32 v174, v169, 7, v174\n"
    "s_lshl_b32 s100, s17, 3\n"
    "s_add_u32 s100, s100, 0x15000\n"
    "v_add_u32_e32 v174, s100, v174\n"
    "v_xor_b32_e32 v175, 64, v174\n"
    "v_mul_u32_u24_e32 v169, 0x180, v169\n"
    "v_lshl_add_u32 v173, v168, 4, v169\n"
    "s_mul_i32 s100, s16, 1536\n"
    "s_add_u32 s100, s100, 0x2a000\n"
    "buffer_load_dwordx4 v[112:115], v173, s[44:47], s100 offen\n"
    "ds_read_b128 v[116:119], v174\n"
    "s_waitcnt vmcnt(0) lgkmcnt(0)\n"
    "v_mfma_f32_16x16x32_bf16 v[240:243], v[112:115], v[116:119], 0\n"
    "s_nop 7\n"
    "buffer_load_dwordx4 v[112:115], v173, s[44:47], s100 offen offset:64\n"
    "ds_read_b128 v[116:119], v175\n"
    "s_waitcnt vmcnt(0) lgkmcnt(0)\n"
    "v_mfma_f32_16x16x32_bf16 v[240:243], v[112:115], v[116:119], v[240:243]\n"
    "s_nop 7\n"
    "buffer_load_dwordx4 v[112:115], v173, s[44:47], s100 offen offset:128\n"
    "ds_read_b128 v[116:119], v174 offset:4096\n"
    "s_waitcnt vmcnt(0) lgkmcnt(0)\n"
    "v_mfma_f32_16x16x32_bf16 v[240:243], v[112:115], v[116:119], v[240:243]\n"
    "s_nop 7\n"
    "buffer_load_dwordx4 v[112:115], v173, s[44:47], s100 offen offset:192\n"
    "ds_read_b128 v[116:119], v175 offset:4096\n"
    "s_waitcnt vmcnt(0) lgkmcnt(0)\n"
    "v_mfma_f32_16x16x32_bf16 v[240:243], v[112:115], v[116:119], v[240:243]\n"
    "s_nop 7\n"
    "buffer_load_dwordx4 v[112:115], v173, s[44:47], s100 offen offset:256\n"
    "ds_read_b128 v[116:119], v174 offset:8192\n"
    "s_waitcnt vmcnt(0) lgkmcnt(0)\n"
    "v_mfma_f32_16x16x32_bf16 v[240:243], v[112:115], v[116:119], v[240:243]\n"
    "s_nop 7\n"
    "buffer_load_dwordx4 v[112:115], v173, s[44:47], s100 offen offset:320\n"
    "ds_read_b128 v[116:119], v175 offset:8192\n"
    "s_waitcnt vmcnt(0) lgkmcnt(0)\n"
    "v_mfma_f32_16x16x32_bf16 v[240:243], v[112:115], v[116:119], v[240:243]\n"
    "s_nop 7\n"
    "s_nop 7\n"
    "v_max3_f32 v168, v240, v241, v242\n"
    "v_max_f32_e32 v168, v168, v243\n"
    "v_mov_b32_e32 v169, v168\n"
    "s_nop 1\n"
    "v_permlane16_swap_b32_e32 v168, v169\n"
    "v_max_f32_e32 v168, v168, v169\n"
    "v_mov_b32_e32 v169, v168\n"
    "s_nop 1\n"
    "v_permlane32_swap_b32_e32 v168, v169\n"
    "v_max_f32_e32 v168, v168, v169\n"
    "v_floor_f32_e64 v168, -v168\n"
    "v_min_f32_e32 v168, v168, v254\n"
    "v_sub_f32_e32 v169, v254, v168\n"
    "v_mov_b32_e32 v254, v168\n"
    "v_min_f32_e32 v169, 0x437f0000, v169\n"
    "v_cvt_u32_f32_e32 v169, v169\n"
    "v_sub_u32_e32 v168, 0, v169\n"
    "v_ldexp_f32 v255, v255, v168\n"
    "v_lshlrev_b32_e32 v169, 7, v169\n"
    "v_lshl_or_b32 v169, v169, 16, v169\n"
    "v_pk_sub_u16 v224, v224, v169 clamp\n"
    "v_pk_sub_u16 v225, v225, v169 clamp\n"
    "v_pk_sub_u16 v226, v226, v169 clamp\n"
    "v_pk_sub_u16 v227, v227, v169 clamp\n"
    "v_pk_sub_u16 v228, v228, v169 clamp\n"
    "v_pk_sub_u16 v229, v229, v169 clamp\n"
    "v_pk_sub_u16 v230, v230, v169 clamp\n"
    "v_pk_sub_u16 v231, v231, v169 clamp\n"
    "v_pk_sub_u16 v232, v232, v169 clamp\n"
    "v_pk_sub_u16 v233, v233, v169 clamp\n"
    "v_pk_sub_u16 v234, v234, v169 clamp\n"
    "v_pk_sub_u16 v235, v235, v169 clamp\n"
    "v_pk_sub_u16 v236, v236, v169 clamp\n"
    "v_pk_sub_u16 v237, v237, v169 clamp\n"
    "v_mov_b32_e32 v112, 0x14400\n"
    "v_mov_b32_e32 v113, 1\n"
    "ds_write_b32 v112, v113\n"
    "s_waitcnt lgkmcnt(0)\n"
    "v_add_f32_e32 v240, v254, v240\n"
    "v_add_f32_e32 v241, v254, v241\n"
    "v_add_f32_e32 v242, v254, v242\n"
    "v_add_f32_e32 v243, v254, v243\n"
    "v_exp_f32_e32 v240, v240\n"
    "v_exp_f32_e32 v241, v241\n"
    "v_exp_f32_e32 v242, v242\n"
    "v_exp_f32_e32 v243, v243\n"
    "v_cvt_pk_bf16_f32 v238, v240, v241\n"
    "v_add_f32_e32 v240, v240, v241\n"
    "v_cvt_pk_bf16_f32 v239, v242, v243\n"
    "v_add_f32_e32 v242, v242, v243\n"
    "v_add_f32_e32 v240, v240, v242\n"
    "s_nop 7\n"
    "s_nop 7\n"
    "s_branch .Lback_n7\n"
    ".Lslow:\n"
    "s_waitcnt lgkmcnt(0)\n"
    "ds_read_b128 v[128:131], v121\n"
    "v_add_u32_e32 v133, s16, v121\n"
    "ds_read_b32 v132, v133 offset:512\n"
    "s_waitcnt lgkmcnt(0)\n"
    "v_min_f32_e32 v128, v128, v129\n"
    "v_min_f32_e32 v130, v130, v131\n"
    "v_min_f32_e32 v128, v128, v130\n"
    "v_sub_f32_e32 v129, v254, v128\n"
    "v_min_f32_e32 v129, 0x437f0000, v129\n"
    "v_cvt_u32_f32_e32 v129, v129\n"
    "v_sub_u32_e32 v130, 0, v129\n"
    "v_ldexp_f32 v132, v132, v130\n"
    "ds_write_b32 v133, v132 offset:512\n"
    "v_lshlrev_b32_e32 v129, 7, v129\n"
    "v_lshl_or_b32 v129, v129, 16, v129\n"
    "v_pk_sub_u16 v224, v224, v129 clamp\n"
    "v_pk_sub_u16 v225, v225, v129 clamp\n"
    "ds_write_b64 v126, v[224:225]\n"
    "v_pk_sub_u16 v226, v226, v129 clamp\n"
    "v_pk_sub_u16 v227, v227, v129 clamp\n"
    "ds_write_b64 v126, v[226:227] offset:128\n"
    "v_pk_sub_u16 v228, v228, v129 clamp\n"
    "v_pk_sub_u16 v229, v229, v129 clamp\n"
    "ds_write_b64 v126, v[228:229] offset:256\n"
    "v_pk_sub_u16 v230, v230, v129 clamp\n"
    "v_pk_sub_u16 v231, v231, v129 clamp\n"
    "ds_write_b64 v126, v[230:231] offset:384\n"
    "v_pk_sub_u16 v232, v232, v129 clamp\n"
    "v_pk_sub_u16 v233, v233, v129 clamp\n"
    "ds_write_b64 v126, v[232:233] offset:512\n"
    "v_pk_sub_u16 v234, v234, v129 clamp\n"
    "v_pk_sub_u16 v235, v235, v129 clamp\n"
    "ds_write_b64 v126, v[234:235] offset:640\n"
    "v_pk_sub_u16 v236, v236, v129 clamp\n"
    "v_pk_sub_u16 v237, v237, v129 clamp\n"
    "ds_write_b64 v126, v[236:237] offset:768\n"
    "v_pk_sub_u16 v238, v238, v129 clamp\n"
    "v_pk_sub_u16 v239, v239, v129 clamp\n"
    "ds_write_b64 v126, v[238:239] offset:896\n"
    "s_waitcnt lgkmcnt(0)\n"
    "s_barrier\n"
    "s_nop 7\n"
    "s_branch .Lslowback\n"
    ".Lrare_a14:\n"
    "s_nop 7\n"
    "s_nop 7\n"
    "v_mbcnt_lo_u32_b32 v124, -1, 0\n"
    "v_mbcnt_hi_u32_b32 v124, -1, v124\n"
    "v_and_b32_e32 v125, 15, v124\n"
    "v_mul_u32_u24_e32 v125, 0x180, v125\n"
    "v_lshrrev_b32_e32 v124, 4, v124\n"
    "v_lshl_add_u32 v170, v124, 4, v125\n"
    "s_add_u32 s19, s63, 0\n"
    "buffer_load_dwordx4 v[116:119], v170, s[40:43], s19 offen\n"
    "s_waitcnt vmcnt(0)\n"
    "v_lshlrev_b32_e32 v236, 16, v116\n"
    "v_and_b32_e32 v237, 0xffff0000, v116\n"
    "v_lshlrev_b32_e32 v238, 16, v117\n"
    "v_and_b32_e32 v239, 0xffff0000, v117\n"
    "v_mul_f32_e32 v236, s36, v236\n"
    "v_mul_f32_e32 v237, s36, v237\n"
    "v_mul_f32_e32 v238, s36, v238\n"
    "v_mul_f32_e32 v239, s36, v239\n"
    "v_cvt_pk_bf16_f32 v116, v236, v237\n"
    "v_cvt_pk_bf16_f32 v117, v238, v239\n"
    "v_lshlrev_b32_e32 v236, 16, v118\n"
    "v_and_b32_e32 v237, 0xffff0000, v118\n"
    "v_lshlrev_b32_e32 v238, 16, v119\n"
    "v_and_b32_e32 v239, 0xffff0000, v119\n"
    "v_mul_f32_e32 v236, s36, v236\n"
    "v_mul_f32_e32 v237, s36, v237\n"
    "v_mul_f32_e32 v238, s36, v238\n"
    "v_mul_f32_e32 v239, s36, v239\n"
    "v_cvt_pk_bf16_f32 v118, v236, v237\n"
    "v_cvt_pk_bf16_f32 v119, v238, v239\n"
    "s_xor_b32 s100, s18, 0\n"
    "s_mul_i32 s100, s100, 6144\n"
    "s_add_u32 s100, s100, 0x2a000\n"
    "buffer_load_dwordx4 v[112:115], v170, s[44:47], s100 offen\n"
    "s_waitcnt vmcnt(0)\n"
    "s_nop 1\n"
    "v_mfma_f32_16x16x32_bf16 v[176:179], v[112:115], v[116:119], 0\n"
    "s_nop 7\n"
    "s_xor_b32 s100, s18, 1\n"
    "s_mul_i32 s100, s100, 6144\n"
    "s_add_u32 s100, s100, 0x2a000\n"
    "buffer_load_dwordx4 v[112:115], v170, s[44:47], s100 offen\n"
    "s_waitcnt vmcnt(0)\n"
    "s_nop 1\n"
    "v_mfma_f32_16x16x32_bf16 v[184:187], v[112:115], v[116:119], 0\n"
    "s_nop 7\n"
    "buffer_load_dwordx4 v[116:119], v170, s[40:43], s19 offen offset:64\n"
    "s_waitcnt vmcnt(0)\n"
    "v_lshlrev_b32_e32 v236, 16, v116\n"
    "v_and_b32_e32 v237, 0xffff0000, v116\n"
    "v_lshlrev_b32_e32 v238, 16, v117\n"
    "v_and_b32_e32 v239, 0xffff0000, v117\n"
    "v_mul_f32_e32 v236, s36, v236\n"
    "v_mul_f32_e32 v237, s36, v237\n"
    "v_mul_f32_e32 v238, s36, v238\n"
    "v_mul_f32_e32 v239, s36, v239\n"
    "v_cvt_pk_bf16_f32 v116, v236, v237\n"
    "v_cvt_pk_bf16_f32 v117, v238, v239\n"
    "v_lshlrev_b32_e32 v236, 16, v118\n"
    "v_and_b32_e32 v237, 0xffff0000, v118\n"
    "v_lshlrev_b32_e32 v238, 16, v119\n"
    "v_and_b32_e32 v239, 0xffff0000, v119\n"
    "v_mul_f32_e32 v236, s36, v236\n"
    "v_mul_f32_e32 v237, s36, v237\n"
    "v_mul_f32_e32 v238, s36, v238\n"
    "v_mul_f32_e32 v239, s36, v239\n"
    "v_cvt_pk_bf16_f32 v118, v236, v237\n"
    "v_cvt_pk_bf16_f32 v119, v238, v239\n"
    "s_xor_b32 s100, s18, 0\n"
    "s_mul_i32 s100, s100, 6144\n"
    "s_add_u32 s100, s100, 0x2a000\n"
    "buffer_load_dwordx4 v[112:115], v170, s[44:47], s100 offen offset:64\n"
    "s_waitcnt vmcnt(0)\n"
    "s_nop 1\n"
    "v_mfma_f32_16x16x32_bf16 v[176:179], v[112:115], v[116:119], v[176:179]\n"
    "s_nop 7\n"
    "s_xor_b32 s100, s18, 1\n"
    "s_mul_i32 s100, s100, 6144\n"
    "s_add_u32 s100, s100, 0x2a000\n"
    "buffer_load_dwordx4 v[112:115], v170, s[44:47], s100 offen offset:64\n"
    "s_waitcnt vmcnt(0)\n"
    "s_nop 1\n"
    "v_mfma_f32_16x16x32_bf16 v[184:187], v[112:115], v[116:119], v[184:187]\n"
    "s_nop 7\n"
    "buffer_load_dwordx4 v[116:119], v170, s[40:43], s19 offen offset:128\n"
    "s_waitcnt vmcnt(0)\n"
    "v_lshlrev_b32_e32 v236, 16, v116\n"
    "v_and_b32_e32 v237, 0xffff0000, v116\n"
    "v_lshlrev_b32_e32 v238, 16, v117\n"
    "v_and_b32_e32 v239, 0xffff0000, v117\n"
    "v_mul_f32_e32 v236, s36, v236\n"
    "v_mul_f32_e32 v237, s36, v237\n"
    "v_mul_f32_e32 v238, s36, v238\n"
    "v_mul_f32_e32 v239, s36, v239\n"
    "v_cvt_pk_bf16_f32 v116, v236, v237\n"
    "v_cvt_pk_bf16_f32 v117, v238, v239\n"
    "v_lshlrev_b32_e32 v236, 16, v118\n"
    "v_and_b32_e32 v237, 0xffff0000, v118\n"
    "v_lshlrev_b32_e32 v238, 16, v119\n"
    "v_and_b32_e32 v239, 0xffff0000, v119\n"
    "v_mul_f32_e32 v236, s36, v236\n"
    "v_mul_f32_e32 v237, s36, v237\n"
    "v_mul_f32_e32 v238, s36, v238\n"
    "v_mul_f32_e32 v239, s36, v239\n"
    "v_cvt_pk_bf16_f32 v118, v236, v237\n"
    "v_cvt_pk_bf16_f32 v119, v238, v239\n"
    "s_xor_b32 s100, s18, 0\n"
    "s_mul_i32 s100, s100, 6144\n"
    "s_add_u32 s100, s100, 0x2a000\n"
    "buffer_load_dwordx4 v[112:115], v170, s[44:47], s100 offen offset:128\n"
    "s_waitcnt vmcnt(0)\n"
    "s_nop 1\n"
    "v_mfma_f32_16x16x32_bf16 v[176:179], v[112:115], v[116:119], v[176:179]\n"
    "s_nop 7\n"
    "s_xor_b32 s100, s18, 1\n"
    "s_mul_i32 s100, s100, 6144\n"
    "s_add_u32 s100, s100, 0x2a000\n"
    "buffer_load_dwordx4 v[112:115], v170, s[44:47], s100 offen offset:128\n"
    "s_waitcnt vmcnt(0)\n"
    "s_nop 1\n"
    "v_mfma_f32_16x16x32_bf16 v[184:187], v[112:115], v[116:119], v[184:187]\n"
    "s_nop 7\n"
    "buffer_load_dwordx4 v[116:119], v170, s[40:43], s19 offen offset:192\n"
    "s_waitcnt vmcnt(0)\n"
    "v_lshlrev_b32_e32 v236, 16, v116\n"
    "v_and_b32_e32 v237, 0xffff0000, v116\n"
    "v_lshlrev_b32_e32 v238, 16, v117\n"
    "v_and_b32_e32 v239, 0xffff0000, v117\n"
    "v_mul_f32_e32 v236, s36, v236\n"
    "v_mul_f32_e32 v237, s36, v237\n"
    "v_mul_f32_e32 v238, s36, v238\n"
    "v_mul_f32_e32 v239, s36, v239\n"
    "v_cvt_pk_bf16_f32 v116, v236, v237\n"
    "v_cvt_pk_bf16_f32 v117, v238, v239\n"
    "v_lshlrev_b32_e32 v236, 16, v118\n"
    "v_and_b32_e32 v237, 0xffff0000, v118\n"
    "v_lshlrev_b32_e32 v238, 16, v119\n"
    "v_and_b32_e32 v239, 0xffff0000, v119\n"
    "v_mul_f32_e32 v236, s36, v236\n"
    "v_mul_f32_e32 v237, s36, v237\n"
    "v_mul_f32_e32 v238, s36, v238\n"
    "v_mul_f32_e32 v239, s36, v239\n"
    "v_cvt_pk_bf16_f32 v118, v236, v237\n"
    "v_cvt_pk_bf16_f32 v119, v238, v239\n"
    "s_xor_b32 s100, s18, 0\n"
    "s_mul_i32 s100, s100, 6144\n"
    "s_add_u32 s100, s100, 0x2a000\n"
    "buffer_load_dwordx4 v[112:115], v170, s[44:47], s100 offen offset:192\n"
    "s_waitcnt vmcnt(0)\n"
    "s_nop 1\n"
    "v_mfma_f32_16x16x32_bf16 v[176:179], v[112:115], v[116:119], v[176:179]\n"
    "s_nop 7\n"
    "s_xor_b32 s100, s18, 1\n"
    "s_mul_i32 s100, s100, 6144\n"
    "s_add_u32 s100, s100, 0x2a000\n"
    "buffer_load_dwordx4 v[112:115], v170, s[44:47], s100 offen offset:192\n"
    "s_waitcnt vmcnt(0)\n"
    "s_nop 1\n"
    "v_mfma_f32_16x16x32_bf16 v[184:187], v[112:115], v[116:119], v[184:187]\n"
    "s_nop 7\n"
    "buffer_load_dwordx4 v[116:119], v170, s[40:43], s19 offen offset:256\n"
    "s_waitcnt vmcnt(0)\n"
    "v_lshlrev_b32_e32 v236, 16, v116\n"
    "v_and_b32_e32 v237, 0xffff0000, v116\n"
    "v_lshlrev_b32_e32 v238, 16, v117\n"
    "v_and_b32_e32 v239, 0xffff0000, v117\n"
    "v_mul_f32_e32 v236, s36, v236\n"
    "v_mul_f32_e32 v237, s36, v237\n"
    "v_mul_f32_e32 v238, s36, v238\n"
    "v_mul_f32_e32 v239, s36, v239\n"
    "v_cvt_pk_bf16_f32 v116, v236, v237\n"
    "v_cvt_pk_bf16_f32 v117, v238, v239\n"
    "v_lshlrev_b32_e32 v236, 16, v118\n"
    "v_and_b32_e32 v237, 0xffff0000, v118\n"
    "v_lshlrev_b32_e32 v238, 16, v119\n"
    "v_and_b32_e32 v239, 0xffff0000, v119\n"
    "v_mul_f32_e32 v236, s36, v236\n"
    "v_mul_f32_e32 v237, s36, v237\n"
    "v_mul_f32_e32 v238, s36, v238\n"
    "v_mul_f32_e32 v239, s36, v239\n"
    "v_cvt_pk_bf16_f32 v118, v236, v237\n"
    "v_cvt_pk_bf16_f32 v119, v238, v239\n"
    "s_xor_b32 s100, s18, 0\n"
    "s_mul_i32 s100, s100, 6144\n"
    "s_add_u32 s100, s100, 0x2a000\n"
    "buffer_load_dwordx4 v[112:115], v170, s[44:47], s100 offen offset:256\n"
    "s_waitcnt vmcnt(0)\n"
    "s_nop 1\n"
    "v_mfma_f32_16x16x32_bf16 v[176:179], v[112:115], v[116:119], v[176:179]\n"
    "s_nop 7\n"
    "s_xor_b32 s100, s18, 1\n"
    "s_mul_i32 s100, s100, 6144\n"
    "s_add_u32 s100, s100, 0x2a000\n"
    "buffer_load_dwordx4 v[112:115], v170, s[44:47], s100 offen offset:256\n"
    "s_waitcnt vmcnt(0)\n"
    "s_nop 1\n"
    "v_mfma_f32_16x16x32_bf16 v[184:187], v[112:115], v[116:119], v[184:187]\n"
    "s_nop 7\n"
    "buffer_load_dwordx4 v[116:119], v170, s[40:43], s19 offen offset:320\n"
    "s_waitcnt vmcnt(0)\n"
    "v_lshlrev_b32_e32 v236, 16, v116\n"
    "v_and_b32_e32 v237, 0xffff0000, v116\n"
    "v_lshlrev_b32_e32 v238, 16, v117\n"
    "v_and_b32_e32 v239, 0xffff0000, v117\n"
    "v_mul_f32_e32 v236, s36, v236\n"
    "v_mul_f32_e32 v237, s36, v237\n"
    "v_mul_f32_e32 v238, s36, v238\n"
    "v_mul_f32_e32 v239, s36, v239\n"
    "v_cvt_pk_bf16_f32 v116, v236, v237\n"
    "v_cvt_pk_bf16_f32 v117, v238, v239\n"
    "v_lshlrev_b32_e32 v236, 16, v118\n"
    "v_and_b32_e32 v237, 0xffff0000, v118\n"
    "v_lshlrev_b32_e32 v238, 16, v119\n"
    "v_and_b32_e32 v239, 0xffff0000, v119\n"
    "v_mul_f32_e32 v236, s36, v236\n"
    "v_mul_f32_e32 v237, s36, v237\n"
    "v_mul_f32_e32 v238, s36, v238\n"
    "v_mul_f32_e32 v239, s36, v239\n"
    "v_cvt_pk_bf16_f32 v118, v236, v237\n"
    "v_cvt_pk_bf16_f32 v119, v238, v239\n"
    "s_xor_b32 s100, s18, 0\n"
    "s_mul_i32 s100, s100, 6144\n"
    "s_add_u32 s100, s100, 0x2a000\n"
    "buffer_load_dwordx4 v[112:115], v170, s[44:47], s100 offen offset:320\n"
    "s_waitcnt vmcnt(0)\n"
    "s_nop 1\n"
    "v_mfma_f32_16x16x32_bf16 v[176:179], v[112:115], v[116:119], v[176:179]\n"
    "s_nop 7\n"
    "s_xor_b32 s100, s18, 1\n"
    "s_mul_i32 s100, s100, 6144\n"
    "s_add_u32 s100, s100, 0x2a000\n"
    "buffer_load_dwordx4 v[112:115], v170, s[44:47], s100 offen offset:320\n"
    "s_waitcnt vmcnt(0)\n"
    "s_nop 1\n"
    "v_mfma_f32_16x16x32_bf16 v[184:187], v[112:115], v[116:119], v[184:187]\n"
    "s_nop 7\n"
    "s_add_u32 s19, s63, 6144\n"
    "buffer_load_dwordx4 v[116:119], v170, s[40:43], s19 offen\n"
    "s_waitcnt vmcnt(0)\n"
    "v_lshlrev_b32_e32 v236, 16, v116\n"
    "v_and_b32_e32 v237, 0xffff0000, v116\n"
    "v_lshlrev_b32_e32 v238, 16, v117\n"
    "v_and_b32_e32 v239, 0xffff0000, v117\n"
    "v_mul_f32_e32 v236, s36, v236\n"
    "v_mul_f32_e32 v237, s36, v237\n"
    "v_mul_f32_e32 v238, s36, v238\n"
    "v_mul_f32_e32 v239, s36, v239\n"
    "v_cvt_pk_bf16_f32 v116, v236, v237\n"
    "v_cvt_pk_bf16_f32 v117, v238, v239\n"
    "v_lshlrev_b32_e32 v236, 16, v118\n"
    "v_and_b32_e32 v237, 0xffff0000, v118\n"
    "v_lshlrev_b32_e32 v238, 16, v119\n"
    "v_and_b32_e32 v239, 0xffff0000, v119\n"
    "v_mul_f32_e32 v236, s36, v236\n"
    "v_mul_f32_e32 v237, s36, v237\n"
    "v_mul_f32_e32 v238, s36, v238\n"
    "v_mul_f32_e32 v239, s36, v239\n"
    "v_cvt_pk_bf16_f32 v118, v236, v237\n"
    "v_cvt_pk_bf16_f32 v119, v238, v239\n"
    "s_xor_b32 s100, s18, 0\n"
    "s_mul_i32 s100, s100, 6144\n"
    "s_add_u32 s100, s100, 0x2a000\n"
    "buffer_load_dwordx4 v[112:115], v170, s[44:47], s100 offen\n"
    "s_waitcnt vmcnt(0)\n"
    "s_nop 1\n"
    "v_mfma_f32_16x16x32_bf16 v[180:183], v[112:115], v[116:119], 0\n"
    "s_nop 7\n"
    "s_xor_b32 s100, s18, 1\n"
    "s_mul_i32 s100, s100, 6144\n"
    "s_add_u32 s100, s100, 0x2a000\n"
    "buffer_load_dwordx4 v[112:115], v170, s[44:47], s100 offen\n"
    "s_waitcnt vmcnt(0)\n"
    "s_nop 1\n"
    "v_mfma_f32_16x16x32_bf16 v[188:191], v[112:115], v[116:119], 0\n"
    "s_nop 7\n"
    "buffer_load_dwordx4 v[116:119], v170, s[40:43], s19 offen offset:64\n"
    "s_waitcnt vmcnt(0)\n"
    "v_lshlrev_b32_e32 v236, 16, v116\n"
    "v_and_b32_e32 v237, 0xffff0000, v116\n"
    "v_lshlrev_b32_e32 v238, 16, v117\n"
    "v_and_b32_e32 v239, 0xffff0000, v117\n"
    "v_mul_f32_e32 v236, s36, v236\n"
    "v_mul_f32_e32 v237, s36, v237\n"
    "v_mul_f32_e32 v238, s36, v238\n"
    "v_mul_f32_e32 v239, s36, v239\n"
    "v_cvt_pk_bf16_f32 v116, v236, v237\n"
    "v_cvt_pk_bf16_f32 v117, v238, v239\n"
    "v_lshlrev_b32_e32 v236, 16, v118\n"
    "v_and_b32_e32 v237, 0xffff0000, v118\n"
    "v_lshlrev_b32_e32 v238, 16, v119\n"
    "v_and_b32_e32 v239, 0xffff0000, v119\n"
    "v_mul_f32_e32 v236, s36, v236\n"
    "v_mul_f32_e32 v237, s36, v237\n"
    "v_mul_f32_e32 v238, s36, v238\n"
    "v_mul_f32_e32 v239, s36, v239\n"
    "v_cvt_pk_bf16_f32 v118, v236, v237\n"
    "v_cvt_pk_bf16_f32 v119, v238, v239\n"
    "s_xor_b32 s100, s18, 0\n"
    "s_mul_i32 s100, s100, 6144\n"
    "s_add_u32 s100, s100, 0x2a000\n"
    "buffer_load_dwordx4 v[112:115], v170, s[44:47], s100 offen offset:64\n"
    "s_waitcnt vmcnt(0)\n"
    "s_nop 1\n"
    "v_mfma_f32_16x16x32_bf16 v[180:183], v[112:115], v[116:119], v[180:183]\n"
    "s_nop 7\n"
    "s_xor_b32 s100, s18, 1\n"
    "s_mul_i32 s100, s100, 6144\n"
    "s_add_u32 s100, s100, 0x2a000\n"
    "buffer_load_dwordx4 v[112:115], v170, s[44:47], s100 offen offset:64\n"
    "s_waitcnt vmcnt(0)\n"
    "s_nop 1\n"
    "v_mfma_f32_16x16x32_bf16 v[188:191], v[112:115], v[116:119], v[188:191]\n"
    "s_nop 7\n"
    "buffer_load_dwordx4 v[116:119], v170, s[40:43], s19 offen offset:128\n"
    "s_waitcnt vmcnt(0)\n"
    "v_lshlrev_b32_e32 v236, 16, v116\n"
    "v_and_b32_e32 v237, 0xffff0000, v116\n"
    "v_lshlrev_b32_e32 v238, 16, v117\n"
    "v_and_b32_e32 v239, 0xffff0000, v117\n"
    "v_mul_f32_e32 v236, s36, v236\n"
    "v_mul_f32_e32 v237, s36, v237\n"
    "v_mul_f32_e32 v238, s36, v238\n"
    "v_mul_f32_e32 v239, s36, v239\n"
    "v_cvt_pk_bf16_f32 v116, v236, v237\n"
    "v_cvt_pk_bf16_f32 v117, v238, v239\n"
    "v_lshlrev_b32_e32 v236, 16, v118\n"
    "v_and_b32_e32 v237, 0xffff0000, v118\n"
    "v_lshlrev_b32_e32 v238, 16, v119\n"
    "v_and_b32_e32 v239, 0xffff0000, v119\n"
    "v_mul_f32_e32 v236, s36, v236\n"
    "v_mul_f32_e32 v237, s36, v237\n"
    "v_mul_f32_e32 v238, s36, v238\n"
    "v_mul_f32_e32 v239, s36, v239\n"
    "v_cvt_pk_bf16_f32 v118, v236, v237\n"
    "v_cvt_pk_bf16_f32 v119, v238, v239\n"
    "s_xor_b32 s100, s18, 0\n"
    "s_mul_i32 s100, s100, 6144\n"
    "s_add_u32 s100, s100, 0x2a000\n"
    "buffer_load_dwordx4 v[112:115], v170, s[44:47], s100 offen offset:128\n"
    "s_waitcnt vmcnt(0)\n"
    "s_nop 1\n"
    "v_mfma_f32_16x16x32_bf16 v[180:183], v[112:115], v[116:119], v[180:183]\n"
    "s_nop 7\n"
    "s_xor_b32 s100, s18, 1\n"
    "s_mul_i32 s100, s100, 6144\n"
    "s_add_u32 s100, s100, 0x2a000\n"
    "buffer_load_dwordx4 v[112:115], v170, s[44:47], s100 offen offset:128\n"
    "s_waitcnt vmcnt(0)\n"
    "s_nop 1\n"
    "v_mfma_f32_16x16x32_bf16 v[188:191], v[112:115], v[116:119], v[188:191]\n"
    "s_nop 7\n"
    "buffer_load_dwordx4 v[116:119], v170, s[40:43], s19 offen offset:192\n"
    "s_waitcnt vmcnt(0)\n"
    "v_lshlrev_b32_e32 v236, 16, v116\n"
    "v_and_b32_e32 v237, 0xffff0000, v116\n"
    "v_lshlrev_b32_e32 v238, 16, v117\n"
    "v_and_b32_e32 v239, 0xffff0000, v117\n"
    "v_mul_f32_e32 v236, s36, v236\n"
    "v_mul_f32_e32 v237, s36, v237\n"
    "v_mul_f32_e32 v238, s36, v238\n"
    "v_mul_f32_e32 v239, s36, v239\n"
    "v_cvt_pk_bf16_f32 v116, v236, v237\n"
    "v_cvt_pk_bf16_f32 v117, v238, v239\n"
    "v_lshlrev_b32_e32 v236, 16, v118\n"
    "v_and_b32_e32 v237, 0xffff0000, v118\n"
    "v_lshlrev_b32_e32 v238, 16, v119\n"
    "v_and_b32_e32 v239, 0xffff0000, v119\n"
    "v_mul_f32_e32 v236, s36, v236\n"
    "v_mul_f32_e32 v237, s36, v237\n"
    "v_mul_f32_e32 v238, s36, v238\n"
    "v_mul_f32_e32 v239, s36, v239\n"
    "v_cvt_pk_bf16_f32 v118, v236, v237\n"
    "v_cvt_pk_bf16_f32 v119, v238, v239\n"
    "s_xor_b32 s100, s18, 0\n"
    "s_mul_i32 s100, s100, 6144\n"
    "s_add_u32 s100, s100, 0x2a000\n"
    "buffer_load_dwordx4 v[112:115], v170, s[44:47], s100 offen offset:192\n"
    "s_waitcnt vmcnt(0)\n"
    "s_nop 1\n"
    "v_mfma_f32_16x16x32_bf16 v[180:183], v[112:115], v[116:119], v[180:183]\n"
    "s_nop 7\n"
    "s_xor_b32 s100, s18, 1\n"
    "s_mul_i32 s100, s100, 6144\n"
    "s_add_u32 s100, s100, 0x2a000\n"
    "buffer_load_dwordx4 v[112:115], v170, s[44:47], s100 offen offset:192\n"
    "s_waitcnt vmcnt(0)\n"
    "s_nop 1\n"
    "v_mfma_f32_16x16x32_bf16 v[188:191], v[112:115], v[116:119], v[188:191]\n"
    "s_nop 7\n"
    "buffer_load_dwordx4 v[116:119], v170, s[40:43], s19 offen offset:256\n"
    "s_waitcnt vmcnt(0)\n"
    "v_lshlrev_b32_e32 v236, 16, v116\n"
    "v_and_b32_e32 v237, 0xffff0000, v116\n"
    "v_lshlrev_b32_e32 v238, 16, v117\n"
    "v_and_b32_e32 v239, 0xffff0000, v117\n"
    "v_mul_f32_e32 v236, s36, v236\n"
    "v_mul_f32_e32 v237, s36, v237\n"
    "v_mul_f32_e32 v238, s36, v238\n"
    "v_mul_f32_e32 v239, s36, v239\n"
    "v_cvt_pk_bf16_f32 v116, v236, v237\n"
    "v_cvt_pk_bf16_f32 v117, v238, v239\n"
    "v_lshlrev_b32_e32 v236, 16, v118\n"
    "v_and_b32_e32 v237, 0xffff0000, v118\n"
    "v_lshlrev_b32_e32 v238, 16, v119\n"
    "v_and_b32_e32 v239, 0xffff0000, v119\n"
    "v_mul_f32_e32 v236, s36, v236\n"
    "v_mul_f32_e32 v237, s36, v237\n"
    "v_mul_f32_e32 v238, s36, v238\n"
    "v_mul_f32_e32 v239, s36, v239\n"
    "v_cvt_pk_bf16_f32 v118, v236, v237\n"
    "v_cvt_pk_bf16_f32 v119, v238, v239\n"
    "s_xor_b32 s100, s18, 0\n"
    "s_mul_i32 s100, s100, 6144\n"
    "s_add_u32 s100, s100, 0x2a000\n"
    "buffer_load_dwordx4 v[112:115], v170, s[44:47], s100 offen offset:256\n"
    "s_waitcnt vmcnt(0)\n"
    "s_nop 1\n"
    "v_mfma_f32_16x16x32_bf16 v[180:183], v[112:115], v[116:119], v[180:183]\n"
    "s_nop 7\n"
    "s_xor_b32 s100, s18, 1\n"
    "s_mul_i32 s100, s100, 6144\n"
    "s_add_u32 s100, s100, 0x2a000\n"
    "buffer_load_dwordx4 v[112:115], v170, s[44:47], s100 offen offset:256\n"
    "s_waitcnt vmcnt(0)\n"
    "s_nop 1\n"
    "v_mfma_f32_16x16x32_bf16 v[188:191], v[112:115], v[116:119], v[188:191]\n"
    "s_nop 7\n"
    "buffer_load_dwordx4 v[116:119], v170, s[40:43], s19 offen offset:320\n"
    "s_waitcnt vmcnt(0)\n"
    "v_lshlrev_b32_e32 v236, 16, v116\n"
    "v_and_b32_e32 v237, 0xffff0000, v116\n"
    "v_lshlrev_b32_e32 v238, 16, v117\n"
    "v_and_b32_e32 v239, 0xffff0000, v117\n"
    "v_mul_f32_e32 v236, s36, v236\n"
    "v_mul_f32_e32 v237, s36, v237\n"
    "v_mul_f32_e32 v238, s36, v238\n"
    "v_mul_f32_e32 v239, s36, v239\n"
    "v_cvt_pk_bf16_f32 v116, v236, v237\n"
    "v_cvt_pk_bf16_f32 v117, v238, v239\n"
    "v_lshlrev_b32_e32 v236, 16, v118\n"
    "v_and_b32_e32 v237, 0xffff0000, v118\n"
    "v_lshlrev_b32_e32 v238, 16, v119\n"
    "v_and_b32_e32 v239, 0xffff0000, v119\n"
    "v_mul_f32_e32 v236, s36, v236\n"
    "v_mul_f32_e32 v237, s36, v237\n"
    "v_mul_f32_e32 v238, s36, v238\n"
    "v_mul_f32_e32 v239, s36, v239\n"
    "v_cvt_pk_bf16_f32 v118, v236, v237\n"
    "v_cvt_pk_bf16_f32 v119, v238, v239\n"
    "s_xor_b32 s100, s18, 0\n"
    "s_mul_i32 s100, s100, 6144\n"
    "s_add_u32 s100, s100, 0x2a000\n"
    "buffer_load_dwordx4 v[112:115], v170, s[44:47], s100 offen offset:320\n"
    "s_waitcnt vmcnt(0)\n"
    "s_nop 1\n"
    "v_mfma_f32_16x16x32_bf16 v[180:183], v[112:115], v[116:119], v[180:183]\n"
    "s_nop 7\n"
    "s_xor_b32 s100, s18, 1\n"
    "s_mul_i32 s100, s100, 6144\n"
    "s_add_u32 s100, s100, 0x2a000\n"
    "buffer_load_dwordx4 v[112:115], v170, s[44:47], s100 offen offset:320\n"
    "s_waitcnt vmcnt(0)\n"
    "s_nop 1\n"
    "v_mfma_f32_16x16x32_bf16 v[188:191], v[112:115], v[116:119], v[188:191]\n"
    "s_nop 7\n"
    "s_nop 7\n"
    "s_nop 7\n"
    "v_max3_f32 v124, v176, v177, v178\n"
    "v_max3_f32 v124, v124, v179, v184\n"
    "v_max3_f32 v124, v124, v185, v186\n"
    "v_max_f32_e32 v124, v124, v187\n"
    "v_max3_f32 v125, v180, v181, v182\n"
    "v_max3_f32 v125, v125, v183, v188\n"
    "v_max3_f32 v125, v125, v189, v190\n"
    "v_max_f32_e32 v125, v125, v191\n"
    "v_mov_b32_e32 v236, v124\n"
    "s_nop 1\n"
    "v_permlane16_swap_b32_e32 v124, v236\n"
    "v_max_f32_e32 v124, v124, v236\n"
    "v_mov_b32_e32 v236, v124\n"
    "s_nop 1\n"
    "v_permlane32_swap_b32_e32 v124, v236\n"
    "v_max_f32_e32 v124, v124, v236\n"
    "v_mov_b32_e32 v236, v125\n"
    "s_nop 1\n"
    "v_permlane16_swap_b32_e32 v125, v236\n"
    "v_max_f32_e32 v125, v125, v236\n"
    "v_mov_b32_e32 v236, v125\n"
    "s_nop 1\n"
    "v_permlane32_swap_b32_e32 v125, v236\n"
    "v_max_f32_e32 v125, v125, v236\n"
    "v_floor_f32_e64 v124, -v124\n"
    "v_min_f32_e32 v124, v124, v171\n"
    "v_sub_f32_e32 v236, v171, v124\n"
    "v_mov_b32_e32 v171, v124\n"
    "v_sub_f32_e32 v192, v192, v236\n"
    "v_sub_f32_e32 v193, v193, v236\n"
    "v_sub_f32_e32 v194, v194, v236\n"
    "v_sub_f32_e32 v195, v195, v236\n"
    "v_sub_f32_e32 v200, v200, v236\n"
    "v_sub_f32_e32 v201, v201, v236\n"
    "v_sub_f32_e32 v202, v202, v236\n"
    "v_sub_f32_e32 v203, v203, v236\n"
    "v_min_f32_e32 v236, 0x437f0000, v236\n"
    "v_cvt_u32_f32_e32 v236, v236\n"
    "v_sub_u32_e32 v124, 0, v236\n"
    "v_ldexp_f32 v252, v252, v124\n"
    "v_ldexp_f32 v136, v136, v124\n"
    "v_ldexp_f32 v137, v137, v124\n"
    "v_ldexp_f32 v138, v138, v124\n"
    "v_ldexp_f32 v139, v139, v124\n"
    "v_ldexp_f32 v144, v144, v124\n"
    "v_ldexp_f32 v145, v145, v124\n"
    "v_ldexp_f32 v146, v146, v124\n"
    "v_ldexp_f32 v147, v147, v124\n"
    "v_ldexp_f32 v152, v152, v124\n"
    "v_ldexp_f32 v153, v153, v124\n"
    "v_ldexp_f32 v154, v154, v124\n"
    "v_ldexp_f32 v155, v155, v124\n"
    "v_ldexp_f32 v128, v128, v124\n"
    "v_ldexp_f32 v129, v129, v124\n"
    "v_ldexp_f32 v130, v130, v124\n"
    "v_ldexp_f32 v131, v131, v124\n"
    "v_lshlrev_b32_e32 v236, 7, v236\n"
    "v_lshl_or_b32 v236, v236, 16, v236\n"
    "v_pk_sub_u16 v0, v0, v236 clamp\n"
    "v_pk_sub_u16 v1, v1, v236 clamp\n"
    "v_pk_sub_u16 v2, v2, v236 clamp\n"
    "v_pk_sub_u16 v3, v3, v236 clamp\n"
    "v_pk_sub_u16 v8, v8, v236 clamp\n"
    "v_pk_sub_u16 v9, v9, v236 clamp\n"
    "v_pk_sub_u16 v10, v10, v236 clamp\n"
    "v_pk_sub_u16 v11, v11, v236 clamp\n"
    "v_pk_sub_u16 v16, v16, v236 clamp\n"
    "v_pk_sub_u16 v17, v17, v236 clamp\n"
    "v_pk_sub_u16 v18, v18, v236 clamp\n"
    "v_pk_sub_u16 v19, v19, v236 clamp\n"
    "v_pk_sub_u16 v24, v24, v236 clamp\n"
    "v_pk_sub_u16 v25, v25, v236 clamp\n"
    "v_pk_sub_u16 v26, v26, v236 clamp\n"
    "v_pk_sub_u16 v27, v27, v236 clamp\n"
    "v_pk_sub_u16 v32, v32, v236 clamp\n"
    "v_pk_sub_u16 v33, v33, v236 clamp\n"
    "v_pk_sub_u16 v34, v34, v236 clamp\n"
    "v_pk_sub_u16 v35, v35, v236 clamp\n"
    "v_pk_sub_u16 v40, v40, v236 clamp\n"
    "v_pk_sub_u16 v41, v41, v236 clamp\n"
    "v_pk_sub_u16 v42, v42, v236 clamp\n"
    "v_pk_sub_u16 v43, v43, v236 clamp\n"
    "v_pk_sub_u16 v48, v48, v236 clamp\n"
    "v_pk_sub_u16 v49, v49, v236 clamp\n"
    "v_pk_sub_u16 v50, v50, v236 clamp\n"
    "v_pk_sub_u16 v51, v51, v236 clamp\n"
    "v_pk_sub_u16 v56, v56, v236 clamp\n"
    "v_pk_sub_u16 v57, v57, v236 clamp\n"
    "v_pk_sub_u16 v58, v58, v236 clamp\n"
    "v_pk_sub_u16 v59, v59, v236 clamp\n"
    "v_pk_sub_u16 v64, v64, v236 clamp\n"
    "v_pk_sub_u16 v65, v65, v236 clamp\n"
    "v_pk_sub_u16 v66, v66, v236 clamp\n"
    "v_pk_sub_u16 v67, v67, v236 clamp\n"
    "v_pk_sub_u16 v72, v72, v236 clamp\n"
    "v_pk_sub_u16 v73, v73, v236 clamp\n"
    "v_pk_sub_u16 v74, v74, v236 clamp\n"
    "v_pk_sub_u16 v75, v75, v236 clamp\n"
    "v_pk_sub_u16 v80, v80, v236 clamp\n"
    "v_pk_sub_u16 v81, v81, v236 clamp\n"
    "v_pk_sub_u16 v82, v82, v236 clamp\n"
    "v_pk_sub_u16 v83, v83, v236 clamp\n"
    "v_pk_sub_u16 v88, v88, v236 clamp\n"
    "v_pk_sub_u16 v89, v89, v236 clamp\n"
    "v_pk_sub_u16 v90, v90, v236 clamp\n"
    "v_pk_sub_u16 v91, v91, v236 clamp\n"
    "v_pk_sub_u16 v96, v96, v236 clamp\n"
    "v_pk_sub_u16 v97, v97, v236 clamp\n"
    "v_pk_sub_u16 v98, v98, v236 clamp\n"
    "v_pk_sub_u16 v99, v99, v236 clamp\n"
    "v_pk_sub_u16 v104, v104, v236 clamp\n"
    "v_pk_sub_u16 v105, v105, v236 clamp\n"
    "v_pk_sub_u16 v106, v106, v236 clamp\n"
    "v_pk_sub_u16 v107, v107, v236 clamp\n"
    "v_floor_f32_e64 v125, -v125\n"
    "v_min_f32_e32 v125, v125, v172\n"
    "v_sub_f32_e32 v236, v172, v125\n"
    "v_mov_b32_e32 v172, v125\n"
    "v_sub_f32_e32 v196, v196, v236\n"
    "v_sub_f32_e32 v197, v197, v236\n"
    "v_sub_f32_e32 v198, v198, v236\n"
    "v_sub_f32_e32 v199, v199, v236\n"
    "v_sub_f32_e32 v204, v204, v236\n"
    "v_sub_f32_e32 v205, v205, v236\n"
    "v_sub_f32_e32 v206, v206, v236\n"
    "v_sub_f32_e32 v207, v207, v236\n"
    "v_min_f32_e32 v236, 0x437f0000, v236\n"
    "v_cvt_u32_f32_e32 v236, v236\n"
    "v_sub_u32_e32 v125, 0, v236\n"
    "v_ldexp_f32 v253, v253, v125\n"
    "v_ldexp_f32 v140, v140, v125\n"
    "v_ldexp_f32 v141, v141, v125\n"
    "v_ldexp_f32 v142, v142, v125\n"
    "v_ldexp_f32 v143, v143, v125\n"
    "v_ldexp_f32 v148, v148, v125\n"
    "v_ldexp_f32 v149, v149, v125\n"
    "v_ldexp_f32 v150, v150, v125\n"
    "v_ldexp_f32 v151, v151, v125\n"
    "v_ldexp_f32 v156, v156, v125\n"
    "v_ldexp_f32 v157, v157, v125\n"
    "v_ldexp_f32 v158, v158, v125\n"
    "v_ldexp_f32 v159, v159, v125\n"
    "v_ldexp_f32 v132, v132, v125\n"
    "v_ldexp_f32 v133, v133, v125\n"
    "v_ldexp_f32 v134, v134, v125\n"
    "v_ldexp_f32 v135, v135, v125\n"
    "v_lshlrev_b32_e32 v236, 7, v236\n"
    "v_lshl_or_b32 v236, v236, 16, v236\n"
    "v_pk_sub_u16 v4, v4, v236 clamp\n"
    "v_pk_sub_u16 v5, v5, v236 clamp\n"
    "v_pk_sub_u16 v6, v6, v236 clamp\n"
    "v_pk_sub_u16 v7, v7, v236 clamp\n"
    "v_pk_sub_u16 v12, v12, v236 clamp\n"
    "v_pk_sub_u16 v13, v13, v236 clamp\n"
    "v_pk_sub_u16 v14, v14, v236 clamp\n"
    "v_pk_sub_u16 v15, v15, v236 clamp\n"
    "v_pk_sub_u16 v20, v20, v236 clamp\n"
    "v_pk_sub_u16 v21, v21, v236 clamp\n"
    "v_pk_sub_u16 v22, v22, v236 clamp\n"
    "v_pk_sub_u16 v23, v23, v236 clamp\n"
    "v_pk_sub_u16 v28, v28, v236 clamp\n"
    "v_pk_sub_u16 v29, v29, v236 clamp\n"
    "v_pk_sub_u16 v30, v30, v236 clamp\n"
    "v_pk_sub_u16 v31, v31, v236 clamp\n"
    "v_pk_sub_u16 v36, v36, v236 clamp\n"
    "v_pk_sub_u16 v37, v37, v236 clamp\n"
    "v_pk_sub_u16 v38, v38, v236 clamp\n"
    "v_pk_sub_u16 v39, v39, v236 clamp\n"
    "v_pk_sub_u16 v44, v44, v236 clamp\n"
    "v_pk_sub_u16 v45, v45, v236 clamp\n"
    "v_pk_sub_u16 v46, v46, v236 clamp\n"
    "v_pk_sub_u16 v47, v47, v236 clamp\n"
    "v_pk_sub_u16 v52, v52, v236 clamp\n"
    "v_pk_sub_u16 v53, v53, v236 clamp\n"
    "v_pk_sub_u16 v54, v54, v236 clamp\n"
    "v_pk_sub_u16 v55, v55, v236 clamp\n"
    "v_pk_sub_u16 v60, v60, v236 clamp\n"
    "v_pk_sub_u16 v61, v61, v236 clamp\n"
    "v_pk_sub_u16 v62, v62, v236 clamp\n"
    "v_pk_sub_u16 v63, v63, v236 clamp\n"
    "v_pk_sub_u16 v68, v68, v236 clamp\n"
    "v_pk_sub_u16 v69, v69, v236 clamp\n"
    "v_pk_sub_u16 v70, v70, v236 clamp\n"
    "v_pk_sub_u16 v71, v71, v236 clamp\n"
    "v_pk_sub_u16 v76, v76, v236 clamp\n"
    "v_pk_sub_u16 v77, v77, v236 clamp\n"
    "v_pk_sub_u16 v78, v78, v236 clamp\n"
    "v_pk_sub_u16 v79, v79, v236 clamp\n"
    "v_pk_sub_u16 v84, v84, v236 clamp\n"
    "v_pk_sub_u16 v85, v85, v236 clamp\n"
    "v_pk_sub_u16 v86, v86, v236 clamp\n"
    "v_pk_sub_u16 v87, v87, v236 clamp\n"
    "v_pk_sub_u16 v92, v92, v236 clamp\n"
    "v_pk_sub_u16 v93, v93, v236 clamp\n"
    "v_pk_sub_u16 v94, v94, v236 clamp\n"
    "v_pk_sub_u16 v95, v95, v236 clamp\n"
    "v_pk_sub_u16 v100, v100, v236 clamp\n"
    "v_pk_sub_u16 v101, v101, v236 clamp\n"
    "v_pk_sub_u16 v102, v102, v236 clamp\n"
    "v_pk_sub_u16 v103, v103, v236 clamp\n"
    "v_pk_sub_u16 v108, v108, v236 clamp\n"
    "v_pk_sub_u16 v109, v109, v236 clamp\n"
    "v_pk_sub_u16 v110, v110, v236 clamp\n"
    "v_pk_sub_u16 v111, v111, v236 clamp\n"
    "v_add_f32_e32 v176, v171, v176\n"
    "v_add_f32_e32 v177, v171, v177\n"
    "v_add_f32_e32 v178, v171, v178\n"
    "v_exp_f32_e32 v176, v176\n"
    "v_add_f32_e32 v179, v171, v179\n"
    "v_exp_f32_e32 v177, v177\n"
    "v_add_f32_e32 v180, v172, v180\n"
    "v_exp_f32_e32 v178, v178\n"
    "v_add_f32_e32 v181, v172, v181\n"
    "v_exp_f32_e32 v179, v179\n"
    "v_add_f32_e32 v124, v176, v177\n"
    "v_cvt_pk_bf16_f32 v112, v176, v177\n"
    "v_add_f32_e32 v182, v172, v182\n"
    "v_exp_f32_e32 v180, v180\n"
    "v_add_f32_e32 v124, v178, v124\n"
    "v_add_f32_e32 v183, v172, v183\n"
    "v_exp_f32_e32 v181, v181\n"
    "v_add_f32_e32 v124, v179, v124\n"
    "v_cvt_pk_bf16_f32 v113, v178, v179\n"
    "v_add_f32_e32 v184, v171, v184\n"
    "v_exp_f32_e32 v182, v182\n"
    "v_add_f32_e32 v185, v171, v185\n"
    "v_exp_f32_e32 v183, v183\n"
    "v_add_f32_e32 v125, v180, v181\n"
    "v_cvt_pk_bf16_f32 v116, v180, v181\n"
    "v_add_f32_e32 v186, v171, v186\n"
    "v_exp_f32_e32 v184, v184\n"
    "v_add_f32_e32 v125, v182, v125\n"
    "v_add_f32_e32 v187, v171, v187\n"
    "v_exp_f32_e32 v185, v185\n"
    "v_add_f32_e32 v125, v183, v125\n"
    "v_cvt_pk_bf16_f32 v117, v182, v183\n"
    "v_add_f32_e32 v188, v172, v188\n"
    "v_exp_f32_e32 v186, v186\n"
    "v_add_f32_e32 v124, v184, v124\n"
    "v_add_f32_e32 v189, v172, v189\n"
    "v_exp_f32_e32 v187, v187\n"
    "v_add_f32_e32 v124, v185, v124\n"
    "v_cvt_pk_bf16_f32 v114, v184, v185\n"
    "v_add_f32_e32 v190, v172, v190\n"
    "v_exp_f32_e32 v188, v188\n"
    "v_add_f32_e32 v124, v186, v124\n"
    "v_add_f32_e32 v191, v172, v191\n"
    "v_exp_f32_e32 v189, v189\n"
    "v_add_f32_e32 v124, v187, v124\n"
    "v_cvt_pk_bf16_f32 v115, v186, v187\n"
    "v_exp_f32_e32 v190, v190\n"
    "v_add_f32_e32 v125, v188, v125\n"
    "v_exp_f32_e32 v191, v191\n"
    "v_add_f32_e32 v125, v189, v125\n"
    "v_cvt_pk_bf16_f32 v118, v188, v189\n"
    "v_add_f32_e32 v125, v190, v125\n"
    "v_add_f32_e32 v125, v191, v125\n"
    "v_cvt_pk_bf16_f32 v119, v190, v191\n"
    "s_nop 7\n"
    "s_nop 7\n"
    "s_branch .Lback_a14\n"
    ".Lrare_a15:\n"
    "s_nop 7\n"
    "s_nop 7\n"
    "v_mbcnt_lo_u32_b32 v168, -1, 0\n"
    "v_mbcnt_hi_u32_b32 v168, -1, v168\n"
    "v_and_b32_e32 v169, 15, v168\n"
    "v_mul_u32_u24_e32 v169, 0x180, v169\n"
    "v_lshrrev_b32_e32 v168, 4, v168\n"
    "v_lshl_add_u32 v170, v168, 4, v169\n"
    "s_add_u32 s19, s63, 0\n"
    "buffer_load_dwordx4 v[124:127], v170, s[40:43], s19 offen\n"
    "s_waitcnt vmcnt(0)\n"
    "v_lshlrev_b32_e32 v236, 16, v124\n"
    "v_and_b32_e32 v237, 0xffff0000, v124\n"
    "v_lshlrev_b32_e32 v238, 16, v125\n"
    "v_and_b32_e32 v239, 0xffff0000, v125\n"
    "v_mul_f32_e32 v236, s36, v236\n"
    "v_mul_f32_e32 v237, s36, v237\n"
    "v_mul_f32_e32 v238, s36, v238\n"
    "v_mul_f32_e32 v239, s36, v239\n"
    "v_cvt_pk_bf16_f32 v124, v236, v237\n"
    "v_cvt_pk_bf16_f32 v125, v238, v239\n"
    "v_lshlrev_b32_e32 v236, 16, v126\n"
    "v_and_b32_e32 v237, 0xffff0000, v126\n"
    "v_lshlrev_b32_e32 v238, 16, v127\n"
    "v_and_b32_e32 v239, 0xffff0000, v127\n"
    "v_mul_f32_e32 v236, s36, v236\n"
    "v_mul_f32_e32 v237, s36, v237\n"
    "v_mul_f32_e32 v238, s36, v238\n"
    "v_mul_f32_e32 v239, s36, v239\n"
    "v_cvt_pk_bf16_f32 v126, v236, v237\n"
    "v_cvt_pk_bf16_f32 v127, v238, v239\n"
    "s_xor_b32 s100, s18, 2\n"
    "s_mul_i32 s100, s100, 6144\n"
    "s_add_u32 s100, s100, 0x2a000\n"
    "buffer_load_dwordx4 v[120:123], v170, s[44:47], s100 offen\n"
    "s_waitcnt vmcnt(0)\n"
    "s_nop 1\n"
    "v_mfma_f32_16x16x32_bf16 v[192:195], v[120:123], v[124:127], 0\n"
    "s_nop 7\n"
    "s_xor_b32 s100, s18, 3\n"
    "s_mul_i32 s100, s100, 6144\n"
    "s_add_u32 s100, s100, 0x2a000\n"
    "buffer_load_dwordx4 v[120:123], v170, s[44:47], s100 offen\n"
    "s_waitcnt vmcnt(0)\n"
    "s_nop 1\n"
    "v_mfma_f32_16x16x32_bf16 v[200:203], v[120:123], v[124:127], 0\n"
    "s_nop 7\n"
    "buffer_load_dwordx4 v[124:127], v170, s[40:43], s19 offen offset:64\n"
    "s_waitcnt vmcnt(0)\n"
    "v_lshlrev_b32_e32 v236, 16, v124\n"
    "v_and_b32_e32 v237, 0xffff0000, v124\n"
    "v_lshlrev_b32_e32 v238, 16, v125\n"
    "v_and_b32_e32 v239, 0xffff0000, v125\n"
    "v_mul_f32_e32 v236, s36, v236\n"
    "v_mul_f32_e32 v237, s36, v237\n"
    "v_mul_f32_e32 v238, s36, v238\n"
    "v_mul_f32_e32 v239, s36, v239\n"
    "v_cvt_pk_bf16_f32 v124, v236, v237\n"
    "v_cvt_pk_bf16_f32 v125, v238, v239\n"
    "v_lshlrev_b32_e32 v236, 16, v126\n"
    "v_and_b32_e32 v237, 0xffff0000, v126\n"
    "v_lshlrev_b32_e32 v238, 16, v127\n"
    "v_and_b32_e32 v239, 0xffff0000, v127\n"
    "v_mul_f32_e32 v236, s36, v236\n"
    "v_mul_f32_e32 v237, s36, v237\n"
    "v_mul_f32_e32 v238, s36, v238\n"
    "v_mul_f32_e32 v239, s36, v239\n"
    "v_cvt_pk_bf16_f32 v126, v236, v237\n"
    "v_cvt_pk_bf16_f32 v127, v238, v239\n"
    "s_xor_b32 s100, s18, 2\n"
    "s_mul_i32 s100, s100, 6144\n"
    "s_add_u32 s100, s100, 0x2a000\n"
    "buffer_load_dwordx4 v[120:123], v170, s[44:47], s100 offen offset:64\n"
    "s_waitcnt vmcnt(0)\n"
    "s_nop 1\n"
    "v_mfma_f32_16x16x32_bf16 v[192:195], v[120:123], v[124:127], v[192:195]\n"
    "s_nop 7\n"
    "s_xor_b32 s100, s18, 3\n"
    "s_mul_i32 s100, s100, 6144\n"
    "s_add_u32 s100, s100, 0x2a000\n"
    "buffer_load_dwordx4 v[120:123], v170, s[44:47], s100 offen offset:64\n"
    "s_waitcnt vmcnt(0)\n"
    "s_nop 1\n"
    "v_mfma_f32_16x16x32_bf16 v[200:203], v[120:123], v[124:127], v[200:203]\n"
    "s_nop 7\n"
    "buffer_load_dwordx4 v[124:127], v170, s[40:43], s19 offen offset:128\n"
    "s_waitcnt vmcnt(0)\n"
    "v_lshlrev_b32_e32 v236, 16, v124\n"
    "v_and_b32_e32 v237, 0xffff0000, v124\n"
    "v_lshlrev_b32_e32 v238, 16, v125\n"
    "v_and_b32_e32 v239, 0xffff0000, v125\n"
    "v_mul_f32_e32 v236, s36, v236\n"
    "v_mul_f32_e32 v237, s36, v237\n"
    "v_mul_f32_e32 v238, s36, v238\n"
    "v_mul_f32_e32 v239, s36, v239\n"
    "v_cvt_pk_bf16_f32 v124, v236, v237\n"
    "v_cvt_pk_bf16_f32 v125, v238, v239\n"
    "v_lshlrev_b32_e32 v236, 16, v126\n"
    "v_and_b32_e32 v237, 0xffff0000, v126\n"
    "v_lshlrev_b32_e32 v238, 16, v127\n"
    "v_and_b32_e32 v239, 0xffff0000, v127\n"
    "v_mul_f32_e32 v236, s36, v236\n"
    "v_mul_f32_e32 v237, s36, v237\n"
    "v_mul_f32_e32 v238, s36, v238\n"
    "v_mul_f32_e32 v239, s36, v239\n"
    "v_cvt_pk_bf16_f32 v126, v236, v237\n"
    "v_cvt_pk_bf16_f32 v127, v238, v239\n"
    "s_xor_b32 s100, s18, 2\n"
    "s_mul_i32 s100, s100, 6144\n"
    "s_add_u32 s100, s100, 0x2a000\n"
    "buffer_load_dwordx4 v[120:123], v170, s[44:47], s100 offen offset:128\n"
    "s_waitcnt vmcnt(0)\n"
    "s_nop 1\n"
    "v_mfma_f32_16x16x32_bf16 v[192:195], v[120:123], v[124:127], v[192:195]\n"
    "s_nop 7\n"
    "s_xor_b32 s100, s18, 3\n"
    "s_mul_i32 s100, s100, 6144\n"
    "s_add_u32 s100, s100, 0x2a000\n"
    "buffer_load_dwordx4 v[120:123], v170, s[44:47], s100 offen offset:128\n"
    "s_waitcnt vmcnt(0)\n"
    "s_nop 1\n"
    "v_mfma_f32_16x16x32_bf16 v[200:203], v[120:123], v[124:127], v[200:203]\n"
    "s_nop 7\n"
    "buffer_load_dwordx4 v[124:127], v170, s[40:43], s19 offen offset:192\n"
    "s_waitcnt vmcnt(0)\n"
    "v_lshlrev_b32_e32 v236, 16, v124\n"
    "v_and_b32_e32 v237, 0xffff0000, v124\n"
    "v_lshlrev_b32_e32 v238, 16, v125\n"
    "v_and_b32_e32 v239, 0xffff0000, v125\n"
    "v_mul_f32_e32 v236, s36, v236\n"
    "v_mul_f32_e32 v237, s36, v237\n"
    "v_mul_f32_e32 v238, s36, v238\n"
    "v_mul_f32_e32 v239, s36, v239\n"
    "v_cvt_pk_bf16_f32 v124, v236, v237\n"
    "v_cvt_pk_bf16_f32 v125, v238, v239\n"
    "v_lshlrev_b32_e32 v236, 16, v126\n"
    "v_and_b32_e32 v237, 0xffff0000, v126\n"
    "v_lshlrev_b32_e32 v238, 16, v127\n"
    "v_and_b32_e32 v239, 0xffff0000, v127\n"
    "v_mul_f32_e32 v236, s36, v236\n"
    "v_mul_f32_e32 v237, s36, v237\n"
    "v_mul_f32_e32 v238, s36, v238\n"
    "v_mul_f32_e32 v239, s36, v239\n"
    "v_cvt_pk_bf16_f32 v126, v236, v237\n"
    "v_cvt_pk_bf16_f32 v127, v238, v239\n"
    "s_xor_b32 s100, s18, 2\n"
    "s_mul_i32 s100, s100, 6144\n"
    "s_add_u32 s100, s100, 0x2a000\n"
    "buffer_load_dwordx4 v[120:123], v170, s[44:47], s100 offen offset:192\n"
    "s_waitcnt vmcnt(0)\n"
    "s_nop 1\n"
    "v_mfma_f32_16x16x32_bf16 v[192:195], v[120:123], v[124:127], v[192:195]\n"
    "s_nop 7\n"
    "s_xor_b32 s100, s18, 3\n"
    "s_mul_i32 s100, s100, 6144\n"
    "s_add_u32 s100, s100, 0x2a000\n"
    "buffer_load_dwordx4 v[120:123], v170, s[44:47], s100 offen offset:192\n"
    "s_waitcnt vmcnt(0)\n"
    "s_nop 1\n"
    "v_mfma_f32_16x16x32_bf16 v[200:203], v[120:123], v[124:127], v[200:203]\n"
    "s_nop 7\n"
    "buffer_load_dwordx4 v[124:127], v170, s[40:43], s19 offen offset:256\n"
    "s_waitcnt vmcnt(0)\n"
    "v_lshlrev_b32_e32 v236, 16, v124\n"
    "v_and_b32_e32 v237, 0xffff0000, v124\n"
    "v_lshlrev_b32_e32 v238, 16, v125\n"
    "v_and_b32_e32 v239, 0xffff0000, v125\n"
    "v_mul_f32_e32 v236, s36, v236\n"
    "v_mul_f32_e32 v237, s36, v237\n"
    "v_mul_f32_e32 v238, s36, v238\n"
    "v_mul_f32_e32 v239, s36, v239\n"
    "v_cvt_pk_bf16_f32 v124, v236, v237\n"
    "v_cvt_pk_bf16_f32 v125, v238, v239\n"
    "v_lshlrev_b32_e32 v236, 16, v126\n"
    "v_and_b32_e32 v237, 0xffff0000, v126\n"
    "v_lshlrev_b32_e32 v238, 16, v127\n"
    "v_and_b32_e32 v239, 0xffff0000, v127\n"
    "v_mul_f32_e32 v236, s36, v236\n"
    "v_mul_f32_e32 v237, s36, v237\n"
    "v_mul_f32_e32 v238, s36, v238\n"
    "v_mul_f32_e32 v239, s36, v239\n"
    "v_cvt_pk_bf16_f32 v126, v236, v237\n"
    "v_cvt_pk_bf16_f32 v127, v238, v239\n"
    "s_xor_b32 s100, s18, 2\n"
    "s_mul_i32 s100, s100, 6144\n"
    "s_add_u32 s100, s100, 0x2a000\n"
    "buffer_load_dwordx4 v[120:123], v170, s[44:47], s100 offen offset:256\n"
    "s_waitcnt vmcnt(0)\n"
    "s_nop 1\n"
    "v_mfma_f32_16x16x32_bf16 v[192:195], v[120:123], v[124:127], v[192:195]\n"
    "s_nop 7\n"
    "s_xor_b32 s100, s18, 3\n"
    "s_mul_i32 s100, s100, 6144\n"
    "s_add_u32 s100, s100, 0x2a000\n"
    "buffer_load_dwordx4 v[120:123], v170, s[44:47], s100 offen offset:256\n"
    "s_waitcnt vmcnt(0)\n"
    "s_nop 1\n"
    "v_mfma_f32_16x16x32_bf16 v[200:203], v[120:123], v[124:127], v[200:203]\n"
    "s_nop 7\n"
    "buffer_load_dwordx4 v[124:127], v170, s[40:43], s19 offen offset:320\n"
    "s_waitcnt vmcnt(0)\n"
    "v_lshlrev_b32_e32 v236, 16, v124\n"
    "v_and_b32_e32 v237, 0xffff0000, v124\n"
    "v_lshlrev_b32_e32 v238, 16, v125\n"
    "v_and_b32_e32 v239, 0xffff0000, v125\n"
    "v_mul_f32_e32 v236, s36, v236\n"
    "v_mul_f32_e32 v237, s36, v237\n"
    "v_mul_f32_e32 v238, s36, v238\n"
    "v_mul_f32_e32 v239, s36, v239\n"
    "v_cvt_pk_bf16_f32 v124, v236, v237\n"
    "v_cvt_pk_bf16_f32 v125, v238, v239\n"
    "v_lshlrev_b32_e32 v236, 16, v126\n"
    "v_and_b32_e32 v237, 0xffff0000, v126\n"
    "v_lshlrev_b32_e32 v238, 16, v127\n"
    "v_and_b32_e32 v239, 0xffff0000, v127\n"
    "v_mul_f32_e32 v236, s36, v236\n"
    "v_mul_f32_e32 v237, s36, v237\n"
    "v_mul_f32_e32 v238, s36, v238\n"
    "v_mul_f32_e32 v239, s36, v239\n"
    "v_cvt_pk_bf16_f32 v126, v236, v237\n"
    "v_cvt_pk_bf16_f32 v127, v238, v239\n"
    "s_xor_b32 s100, s18, 2\n"
    "s_mul_i32 s100, s100, 6144\n"
    "s_add_u32 s100, s100, 0x2a000\n"
    "buffer_load_dwordx4 v[120:123], v170, s[44:47], s100 offen offset:320\n"
    "s_waitcnt vmcnt(0)\n"
    "s_nop 1\n"
    "v_mfma_f32_16x16x32_bf16 v[192:195], v[120:123], v[124:127], v[192:195]\n"
    "s_nop 7\n"
    "s_xor_b32 s100, s18, 3\n"
    "s_mul_i32 s100, s100, 6144\n"
    "s_add_u32 s100, s100, 0x2a000\n"
    "buffer_load_dwordx4 v[120:123], v170, s[44:47], s100 offen offset:320\n"
    "s_waitcnt vmcnt(0)\n"
    "s_nop 1\n"
    "v_mfma_f32_16x16x32_bf16 v[200:203], v[120:123], v[124:127], v[200:203]\n"
    "s_nop 7\n"
    "s_add_u32 s19, s63, 6144\n"
    "buffer_load_dwordx4 v[124:127], v170, s[40:43], s19 offen\n"
    "s_waitcnt vmcnt(0)\n"
    "v_lshlrev_b32_e32 v236, 16, v124\n"
    "v_and_b32_e32 v237, 0xffff0000, v124\n"
    "v_lshlrev_b32_e32 v238, 16, v125\n"
    "v_and_b32_e32 v239, 0xffff0000, v125\n"
    "v_mul_f32_e32 v236, s36, v236\n"
    "v_mul_f32_e32 v237, s36, v237\n"
    "v_mul_f32_e32 v238, s36, v238\n"
    "v_mul_f32_e32 v239, s36, v239\n"
    "v_cvt_pk_bf16_f32 v124, v236, v237\n"
    "v_cvt_pk_bf16_f32 v125, v238, v239\n"
    "v_lshlrev_b32_e32 v236, 16, v126\n"
    "v_and_b32_e32 v237, 0xffff0000, v126\n"
    "v_lshlrev_b32_e32 v238, 16, v127\n"
    "v_and_b32_e32 v239, 0xffff0000, v127\n"
    "v_mul_f32_e32 v236, s36, v236\n"
    "v_mul_f32_e32 v237, s36, v237\n"
    "v_mul_f32_e32 v238, s36, v238\n"
    "v_mul_f32_e32 v239, s36, v239\n"
    "v_cvt_pk_bf16_f32 v126, v236, v237\n"
    "v_cvt_pk_bf16_f32 v127, v238, v239\n"
    "s_xor_b32 s100, s18, 2\n"
    "s_mul_i32 s100, s100, 6144\n"
    "s_add_u32 s100, s100, 0x2a000\n"
    "buffer_load_dwordx4 v[120:123], v170, s[44:47], s100 offen\n"
    "s_waitcnt vmcnt(0)\n"
    "s_nop 1\n"
    "v_mfma_f32_16x16x32_bf16 v[196:199], v[120:123], v[124:127], 0\n"
    "s_nop 7\n"
    "s_xor_b32 s100, s18, 3\n"
    "s_mul_i32 s100, s100, 6144\n"
    "s_add_u32 s100, s100, 0x2a000\n"
    "buffer_load_dwordx4 v[120:123], v170, s[44:47], s100 offen\n"
    "s_waitcnt vmcnt(0)\n"
    "s_nop 1\n"
    "v_mfma_f32_16x16x32_bf16 v[204:207], v[120:123], v[124:127], 0\n"
    "s_nop 7\n"
    "buffer_load_dwordx4 v[124:127], v170, s[40:43], s19 offen offset:64\n"
    "s_waitcnt vmcnt(0)\n"
    "v_lshlrev_b32_e32 v236, 16, v124\n"
    "v_and_b32_e32 v237, 0xffff0000, v124\n"
    "v_lshlrev_b32_e32 v238, 16, v125\n"
    "v_and_b32_e32 v239, 0xffff0000, v125\n"
    "v_mul_f32_e32 v236, s36, v236\n"
    "v_mul_f32_e32 v237, s36, v237\n"
    "v_mul_f32_e32 v238, s36, v238\n"
    "v_mul_f32_e32 v239, s36, v239\n"
    "v_cvt_pk_bf16_f32 v124, v236, v237\n"
    "v_cvt_pk_bf16_f32 v125, v238, v239\n"
    "v_lshlrev_b32_e32 v236, 16, v126\n"
    "v_and_b32_e32 v237, 0xffff0000, v126\n"
    "v_lshlrev_b32_e32 v238, 16, v127\n"
    "v_and_b32_e32 v239, 0xffff0000, v127\n"
    "v_mul_f32_e32 v236, s36, v236\n"
    "v_mul_f32_e32 v237, s36, v237\n"
    "v_mul_f32_e32 v238, s36, v238\n"
    "v_mul_f32_e32 v239, s36, v239\n"
    "v_cvt_pk_bf16_f32 v126, v236, v237\n"
    "v_cvt_pk_bf16_f32 v127, v238, v239\n"
    "s_xor_b32 s100, s18, 2\n"
    "s_mul_i32 s100, s100, 6144\n"
    "s_add_u32 s100, s100, 0x2a000\n"
    "buffer_load_dwordx4 v[120:123], v170, s[44:47], s100 offen offset:64\n"
    "s_waitcnt vmcnt(0)\n"
    "s_nop 1\n"
    "v_mfma_f32_16x16x32_bf16 v[196:199], v[120:123], v[124:127], v[196:199]\n"
    "s_nop 7\n"
    "s_xor_b32 s100, s18, 3\n"
    "s_mul_i32 s100, s100, 6144\n"
    "s_add_u32 s100, s100, 0x2a000\n"
    "buffer_load_dwordx4 v[120:123], v170, s[44:47], s100 offen offset:64\n"
    "s_waitcnt vmcnt(0)\n"
    "s_nop 1\n"
    "v_mfma_f32_16x16x32_bf16 v[204:207], v[120:123], v[124:127], v[204:207]\n"
    "s_nop 7\n"
    "buffer_load_dwordx4 v[124:127], v170, s[40:43], s19 offen offset:128\n"
    "s_waitcnt vmcnt(0)\n"
    "v_lshlrev_b32_e32 v236, 16, v124\n"
    "v_and_b32_e32 v237, 0xffff0000, v124\n"
    "v_lshlrev_b32_e32 v238, 16, v125\n"
    "v_and_b32_e32 v239, 0xffff0000, v125\n"
    "v_mul_f32_e32 v236, s36, v236\n"
    "v_mul_f32_e32 v237, s36, v237\n"
    "v_mul_f32_e32 v238, s36, v238\n"
    "v_mul_f32_e32 v239, s36, v239\n"
    "v_cvt_pk_bf16_f32 v124, v236, v237\n"
    "v_cvt_pk_bf16_f32 v125, v238, v239\n"
    "v_lshlrev_b32_e32 v236, 16, v126\n"
    "v_and_b32_e32 v237, 0xffff0000, v126\n"
    "v_lshlrev_b32_e32 v238, 16, v127\n"
    "v_and_b32_e32 v239, 0xffff0000, v127\n"
    "v_mul_f32_e32 v236, s36, v236\n"
    "v_mul_f32_e32 v237, s36, v237\n"
    "v_mul_f32_e32 v238, s36, v238\n"
    "v_mul_f32_e32 v239, s36, v239\n"
    "v_cvt_pk_bf16_f32 v126, v236, v237\n"
    "v_cvt_pk_bf16_f32 v127, v238, v239\n"
    "s_xor_b32 s100, s18, 2\n"
    "s_mul_i32 s100, s100, 6144\n"
    "s_add_u32 s100, s100, 0x2a000\n"
    "buffer_load_dwordx4 v[120:123], v170, s[44:47], s100 offen offset:128\n"
    "s_waitcnt vmcnt(0)\n"
    "s_nop 1\n"
    "v_mfma_f32_16x16x32_bf16 v[196:199], v[120:123], v[124:127], v[196:199]\n"
    "s_nop 7\n"
    "s_xor_b32 s100, s18, 3\n"
    "s_mul_i32 s100, s100, 6144\n"
    "s_add_u32 s100, s100, 0x2a000\n"
    "buffer_load_dwordx4 v[120:123], v170, s[44:47], s100 offen offset:128\n"
    "s_waitcnt vmcnt(0)\n"
    "s_nop 1\n"
    "v_mfma_f32_16x16x32_bf16 v[204:207], v[120:123], v[124:127], v[204:207]\n"
    "s_nop 7\n"
    "buffer_load_dwordx4 v[124:127], v170, s[40:43], s19 offen offset:192\n"
    "s_waitcnt vmcnt(0)\n"
    "v_lshlrev_b32_e32 v236, 16, v124\n"
    "v_and_b32_e32 v237, 0xffff0000, v124\n"
    "v_lshlrev_b32_e32 v238, 16, v125\n"
    "v_and_b32_e32 v239, 0xffff0000, v125\n"
    "v_mul_f32_e32 v236, s36, v236\n"
    "v_mul_f32_e32 v237, s36, v237\n"
    "v_mul_f32_e32 v238, s36, v238\n"
    "v_mul_f32_e32 v239, s36, v239\n"
    "v_cvt_pk_bf16_f32 v124, v236, v237\n"
    "v_cvt_pk_bf16_f32 v125, v238, v239\n"
    "v_lshlrev_b32_e32 v236, 16, v126\n"
    "v_and_b32_e32 v237, 0xffff0000, v126\n"
    "v_lshlrev_b32_e32 v238, 16, v127\n"
    "v_and_b32_e32 v239, 0xffff0000, v127\n"
    "v_mul_f32_e32 v236, s36, v236\n"
    "v_mul_f32_e32 v237, s36, v237\n"
    "v_mul_f32_e32 v238, s36, v238\n"
    "v_mul_f32_e32 v239, s36, v239\n"
    "v_cvt_pk_bf16_f32 v126, v236, v237\n"
    "v_cvt_pk_bf16_f32 v127, v238, v239\n"
    "s_xor_b32 s100, s18, 2\n"
    "s_mul_i32 s100, s100, 6144\n"
    "s_add_u32 s100, s100, 0x2a000\n"
    "buffer_load_dwordx4 v[120:123], v170, s[44:47], s100 offen offset:192\n"
    "s_waitcnt vmcnt(0)\n"
    "s_nop 1\n"
    "v_mfma_f32_16x16x32_bf16 v[196:199], v[120:123], v[124:127], v[196:199]\n"
    "s_nop 7\n"
    "s_xor_b32 s100, s18, 3\n"
    "s_mul_i32 s100, s100, 6144\n"
    "s_add_u32 s100, s100, 0x2a000\n"
    "buffer_load_dwordx4 v[120:123], v170, s[44:47], s100 offen offset:192\n"
    "s_waitcnt vmcnt(0)\n"
    "s_nop 1\n"
    "v_mfma_f32_16x16x32_bf16 v[204:207], v[120:123], v[124:127], v[204:207]\n"
    "s_nop 7\n"
    "buffer_load_dwordx4 v[124:127], v170, s[40:43], s19 offen offset:256\n"
    "s_waitcnt vmcnt(0)\n"
    "v_lshlrev_b32_e32 v236, 16, v124\n"
    "v_and_b32_e32 v237, 0xffff0000, v124\n"
    "v_lshlrev_b32_e32 v238, 16, v125\n"
    "v_and_b32_e32 v239, 0xffff0000, v125\n"
    "v_mul_f32_e32 v236, s36, v236\n"
    "v_mul_f32_e32 v237, s36, v237\n"
    "v_mul_f32_e32 v238, s36, v238\n"
    "v_mul_f32_e32 v239, s36, v239\n"
    "v_cvt_pk_bf16_f32 v124, v236, v237\n"
    "v_cvt_pk_bf16_f32 v125, v238, v239\n"
    "v_lshlrev_b32_e32 v236, 16, v126\n"
    "v_and_b32_e32 v237, 0xffff0000, v126\n"
    "v_lshlrev_b32_e32 v238, 16, v127\n"
    "v_and_b32_e32 v239, 0xffff0000, v127\n"
    "v_mul_f32_e32 v236, s36, v236\n"
    "v_mul_f32_e32 v237, s36, v237\n"
    "v_mul_f32_e32 v238, s36, v238\n"
    "v_mul_f32_e32 v239, s36, v239\n"
    "v_cvt_pk_bf16_f32 v126, v236, v237\n"
    "v_cvt_pk_bf16_f32 v127, v238, v239\n"
    "s_xor_b32 s100, s18, 2\n"
    "s_mul_i32 s100, s100, 6144\n"
    "s_add_u32 s100, s100, 0x2a000\n"
    "buffer_load_dwordx4 v[120:123], v170, s[44:47], s100 offen offset:256\n"
    "s_waitcnt vmcnt(0)\n"
    "s_nop 1\n"
    "v_mfma_f32_16x16x32_bf16 v[196:199], v[120:123], v[124:127], v[196:199]\n"
    "s_nop 7\n"
    "s_xor_b32 s100, s18, 3\n"
    "s_mul_i32 s100, s100, 6144\n"
    "s_add_u32 s100, s100, 0x2a000\n"
    "buffer_load_dwordx4 v[120:123], v170, s[44:47], s100 offen offset:256\n"
    "s_waitcnt vmcnt(0)\n"
    "s_nop 1\n"
    "v_mfma_f32_16x16x32_bf16 v[204:207], v[120:123], v[124:127], v[204:207]\n"
    "s_nop 7\n"
    "buffer_load_dwordx4 v[124:127], v170, s[40:43], s19 offen offset:320\n"
    "s_waitcnt vmcnt(0)\n"
    "v_lshlrev_b32_e32 v236, 16, v124\n"
    "v_and_b32_e32 v237, 0xffff0000, v124\n"
    "v_lshlrev_b32_e32 v238, 16, v125\n"
    "v_and_b32_e32 v239, 0xffff0000, v125\n"
    "v_mul_f32_e32 v236, s36, v236\n"
    "v_mul_f32_e32 v237, s36, v237\n"
    "v_mul_f32_e32 v238, s36, v238\n"
    "v_mul_f32_e32 v239, s36, v239\n"
    "v_cvt_pk_bf16_f32 v124, v236, v237\n"
    "v_cvt_pk_bf16_f32 v125, v238, v239\n"
    "v_lshlrev_b32_e32 v236, 16, v126\n"
    "v_and_b32_e32 v237, 0xffff0000, v126\n"
    "v_lshlrev_b32_e32 v238, 16, v127\n"
    "v_and_b32_e32 v239, 0xffff0000, v127\n"
    "v_mul_f32_e32 v236, s36, v236\n"
    "v_mul_f32_e32 v237, s36, v237\n"
    "v_mul_f32_e32 v238, s36, v238\n"
    "v_mul_f32_e32 v239, s36, v239\n"
    "v_cvt_pk_bf16_f32 v126, v236, v237\n"
    "v_cvt_pk_bf16_f32 v127, v238, v239\n"
    "s_xor_b32 s100, s18, 2\n"
    "s_mul_i32 s100, s100, 6144\n"
    "s_add_u32 s100, s100, 0x2a000\n"
    "buffer_load_dwordx4 v[120:123], v170, s[44:47], s100 offen offset:320\n"
    "s_waitcnt vmcnt(0)\n"
    "s_nop 1\n"
    "v_mfma_f32_16x16x32_bf16 v[196:199], v[120:123], v[124:127], v[196:199]\n"
    "s_nop 7\n"
    "s_xor_b32 s100, s18, 3\n"
    "s_mul_i32 s100, s100, 6144\n"
    "s_add_u32 s100, s100, 0x2a000\n"
    "buffer_load_dwordx4 v[120:123], v170, s[44:47], s100 offen offset:320\n"
    "s_waitcnt vmcnt(0)\n"
    "s_nop 1\n"
    "v_mfma_f32_16x16x32_bf16 v[204:207], v[120:123], v[124:127], v[204:207]\n"
    "s_nop 7\n"
    "s_nop 7\n"
    "s_nop 7\n"
    "v_max3_f32 v168, v192, v193, v194\n"
    "v_max3_f32 v168, v168, v195, v200\n"
    "v_max3_f32 v168, v168, v201, v202\n"
    "v_max_f32_e32 v168, v168, v203\n"
    "v_max3_f32 v169, v196, v197, v198\n"
    "v_max3_f32 v169, v169, v199, v204\n"
    "v_max3_f32 v169, v169, v205, v206\n"
    "v_max_f32_e32 v169, v169, v207\n"
    "v_mov_b32_e32 v236, v168\n"
    "s_nop 1\n"
    "v_permlane16_swap_b32_e32 v168, v236\n"
    "v_max_f32_e32 v168, v168, v236\n"
    "v_mov_b32_e32 v236, v168\n"
    "s_nop 1\n"
    "v_permlane32_swap_b32_e32 v168, v236\n"
    "v_max_f32_e32 v168, v168, v236\n"
    "v_mov_b32_e32 v236, v169\n"
    "s_nop 1\n"
    "v_permlane16_swap_b32_e32 v169, v236\n"
    "v_max_f32_e32 v169, v169, v236\n"
    "v_mov_b32_e32 v236, v169\n"
    "s_nop 1\n"
    "v_permlane32_swap_b32_e32 v169, v236\n"
    "v_max_f32_e32 v169, v169, v236\n"
    "v_floor_f32_e64 v168, -v168\n"
    "v_min_f32_e32 v168, v168, v171\n"
    "v_sub_f32_e32 v236, v171, v168\n"
    "v_mov_b32_e32 v171, v168\n"
    "v_min_f32_e32 v236, 0x437f0000, v236\n"
    "v_cvt_u32_f32_e32 v236, v236\n"
    "v_sub_u32_e32 v168, 0, v236\n"
    "v_ldexp_f32 v252, v252, v168\n"
    "v_ldexp_f32 v136, v136, v168\n"
    "v_ldexp_f32 v137, v137, v168\n"
    "v_ldexp_f32 v138, v138, v168\n"
    "v_ldexp_f32 v139, v139, v168\n"
    "v_ldexp_f32 v144, v144, v168\n"
    "v_ldexp_f32 v145, v145, v168\n"
    "v_ldexp_f32 v146, v146, v168\n"
    "v_ldexp_f32 v147, v147, v168\n"
    "v_ldexp_f32 v152, v152, v168\n"
    "v_ldexp_f32 v153, v153, v168\n"
    "v_ldexp_f32 v154, v154, v168\n"
    "v_ldexp_f32 v155, v155, v168\n"
    "v_ldexp_f32 v128, v128, v168\n"
    "v_ldexp_f32 v129, v129, v168\n"
    "v_ldexp_f32 v130, v130, v168\n"
    "v_ldexp_f32 v131, v131, v168\n"
    "v_lshlrev_b32_e32 v236, 7, v236\n"
    "v_lshl_or_b32 v236, v236, 16, v236\n"
    "v_pk_sub_u16 v0, v0, v236 clamp\n"
    "v_pk_sub_u16 v1, v1, v236 clamp\n"
    "v_pk_sub_u16 v2, v2, v236 clamp\n"
    "v_pk_sub_u16 v3, v3, v236 clamp\n"
    "v_pk_sub_u16 v8, v8, v236 clamp\n"
    "v_pk_sub_u16 v9, v9, v236 clamp\n"
    "v_pk_sub_u16 v10, v10, v236 clamp\n"
    "v_pk_sub_u16 v11, v11, v236 clamp\n"
    "v_pk_sub_u16 v16, v16, v236 clamp\n"
    "v_pk_sub_u16 v17, v17, v236 clamp\n"
    "v_pk_sub_u16 v18, v18, v236 clamp\n"
    "v_pk_sub_u16 v19, v19, v236 clamp\n"
    "v_pk_sub_u16 v24, v24, v236 clamp\n"
    "v_pk_sub_u16 v25, v25, v236 clamp\n"
    "v_pk_sub_u16 v26, v26, v236 clamp\n"
    "v_pk_sub_u16 v27, v27, v236 clamp\n"
    "v_pk_sub_u16 v32, v32, v236 clamp\n"
    "v_pk_sub_u16 v33, v33, v236 clamp\n"
    "v_pk_sub_u16 v34, v34, v236 clamp\n"
    "v_pk_sub_u16 v35, v35, v236 clamp\n"
    "v_pk_sub_u16 v40, v40, v236 clamp\n"
    "v_pk_sub_u16 v41, v41, v236 clamp\n"
    "v_pk_sub_u16 v42, v42, v236 clamp\n"
    "v_pk_sub_u16 v43, v43, v236 clamp\n"
    "v_pk_sub_u16 v48, v48, v236 clamp\n"
    "v_pk_sub_u16 v49, v49, v236 clamp\n"
    "v_pk_sub_u16 v50, v50, v236 clamp\n"
    "v_pk_sub_u16 v51, v51, v236 clamp\n"
    "v_pk_sub_u16 v56, v56, v236 clamp\n"
    "v_pk_sub_u16 v57, v57, v236 clamp\n"
    "v_pk_sub_u16 v58, v58, v236 clamp\n"
    "v_pk_sub_u16 v59, v59, v236 clamp\n"
    "v_pk_sub_u16 v64, v64, v236 clamp\n"
    "v_pk_sub_u16 v65, v65, v236 clamp\n"
    "v_pk_sub_u16 v66, v66, v236 clamp\n"
    "v_pk_sub_u16 v67, v67, v236 clamp\n"
    "v_pk_sub_u16 v72, v72, v236 clamp\n"
    "v_pk_sub_u16 v73, v73, v236 clamp\n"
    "v_pk_sub_u16 v74, v74, v236 clamp\n"
    "v_pk_sub_u16 v75, v75, v236 clamp\n"
    "v_pk_sub_u16 v80, v80, v236 clamp\n"
    "v_pk_sub_u16 v81, v81, v236 clamp\n"
    "v_pk_sub_u16 v82, v82, v236 clamp\n"
    "v_pk_sub_u16 v83, v83, v236 clamp\n"
    "v_pk_sub_u16 v88, v88, v236 clamp\n"
    "v_pk_sub_u16 v89, v89, v236 clamp\n"
    "v_pk_sub_u16 v90, v90, v236 clamp\n"
    "v_pk_sub_u16 v91, v91, v236 clamp\n"
    "v_pk_sub_u16 v96, v96, v236 clamp\n"
    "v_pk_sub_u16 v97, v97, v236 clamp\n"
    "v_pk_sub_u16 v98, v98, v236 clamp\n"
    "v_pk_sub_u16 v99, v99, v236 clamp\n"
    "v_pk_sub_u16 v104, v104, v236 clamp\n"
    "v_pk_sub_u16 v105, v105, v236 clamp\n"
    "v_pk_sub_u16 v106, v106, v236 clamp\n"
    "v_pk_sub_u16 v107, v107, v236 clamp\n"
    "v_pk_sub_u16 v112, v112, v236 clamp\n"
    "v_pk_sub_u16 v113, v113, v236 clamp\n"
    "v_pk_sub_u16 v114, v114, v236 clamp\n"
    "v_pk_sub_u16 v115, v115, v236 clamp\n"
    "v_floor_f32_e64 v169, -v169\n"
    "v_min_f32_e32 v169, v169, v172\n"
    "v_sub_f32_e32 v236, v172, v169\n"
    "v_mov_b32_e32 v172, v169\n"
    "v_min_f32_e32 v236, 0x437f0000, v236\n"
    "v_cvt_u32_f32_e32 v236, v236\n"
    "v_sub_u32_e32 v169, 0, v236\n"
    "v_ldexp_f32 v253, v253, v169\n"
    "v_ldexp_f32 v140, v140, v169\n"
    "v_ldexp_f32 v141, v141, v169\n"
    "v_ldexp_f32 v142, v142, v169\n"
    "v_ldexp_f32 v143, v143, v169\n"
    "v_ldexp_f32 v148, v148, v169\n"
    "v_ldexp_f32 v149, v149, v169\n"
    "v_ldexp_f32 v150, v150, v169\n"
    "v_ldexp_f32 v151, v151, v169\n"
    "v_ldexp_f32 v156, v156, v169\n"
    "v_ldexp_f32 v157, v157, v169\n"
    "v_ldexp_f32 v158, v158, v169\n"
    "v_ldexp_f32 v159, v159, v169\n"
    "v_ldexp_f32 v132, v132, v169\n"
    "v_ldexp_f32 v133, v133, v169\n"
    "v_ldexp_f32 v134, v134, v169\n"
    "v_ldexp_f32 v135, v135, v169\n"
    "v_lshlrev_b32_e32 v236, 7, v236\n"
    "v_lshl_or_b32 v236, v236, 16, v236\n"
    "v_pk_sub_u16 v4, v4, v236 clamp\n"
    "v_pk_sub_u16 v5, v5, v236 clamp\n"
    "v_pk_sub_u16 v6, v6, v236 clamp\n"
    "v_pk_sub_u16 v7, v7, v236 clamp\n"
    "v_pk_sub_u16 v12, v12, v236 clamp\n"
    "v_pk_sub_u16 v13, v13, v236 clamp\n"
    "v_pk_sub_u16 v14, v14, v236 clamp\n"
    "v_pk_sub_u16 v15, v15, v236 clamp\n"
    "v_pk_sub_u16 v20, v20, v236 clamp\n"
    "v_pk_sub_u16 v21, v21, v236 clamp\n"
    "v_pk_sub_u16 v22, v22, v236 clamp\n"
    "v_pk_sub_u16 v23, v23, v236 clamp\n"
    "v_pk_sub_u16 v28, v28, v236 clamp\n"
    "v_pk_sub_u16 v29, v29, v236 clamp\n"
    "v_pk_sub_u16 v30, v30, v236 clamp\n"
    "v_pk_sub_u16 v31, v31, v236 clamp\n"
    "v_pk_sub_u16 v36, v36, v236 clamp\n"
    "v_pk_sub_u16 v37, v37, v236 clamp\n"
    "v_pk_sub_u16 v38, v38, v236 clamp\n"
    "v_pk_sub_u16 v39, v39, v236 clamp\n"
    "v_pk_sub_u16 v44, v44, v236 clamp\n"
    "v_pk_sub_u16 v45, v45, v236 clamp\n"
    "v_pk_sub_u16 v46, v46, v236 clamp\n"
    "v_pk_sub_u16 v47, v47, v236 clamp\n"
    "v_pk_sub_u16 v52, v52, v236 clamp\n"
    "v_pk_sub_u16 v53, v53, v236 clamp\n"
    "v_pk_sub_u16 v54, v54, v236 clamp\n"
    "v_pk_sub_u16 v55, v55, v236 clamp\n"
    "v_pk_sub_u16 v60, v60, v236 clamp\n"
    "v_pk_sub_u16 v61, v61, v236 clamp\n"
    "v_pk_sub_u16 v62, v62, v236 clamp\n"
    "v_pk_sub_u16 v63, v63, v236 clamp\n"
    "v_pk_sub_u16 v68, v68, v236 clamp\n"
    "v_pk_sub_u16 v69, v69, v236 clamp\n"
    "v_pk_sub_u16 v70, v70, v236 clamp\n"
    "v_pk_sub_u16 v71, v71, v236 clamp\n"
    "v_pk_sub_u16 v76, v76, v236 clamp\n"
    "v_pk_sub_u16 v77, v77, v236 clamp\n"
    "v_pk_sub_u16 v78, v78, v236 clamp\n"
    "v_pk_sub_u16 v79, v79, v236 clamp\n"
    "v_pk_sub_u16 v84, v84, v236 clamp\n"
    "v_pk_sub_u16 v85, v85, v236 clamp\n"
    "v_pk_sub_u16 v86, v86, v236 clamp\n"
    "v_pk_sub_u16 v87, v87, v236 clamp\n"
    "v_pk_sub_u16 v92, v92, v236 clamp\n"
    "v_pk_sub_u16 v93, v93, v236 clamp\n"
    "v_pk_sub_u16 v94, v94, v236 clamp\n"
    "v_pk_sub_u16 v95, v95, v236 clamp\n"
    "v_pk_sub_u16 v100, v100, v236 clamp\n"
    "v_pk_sub_u16 v101, v101, v236 clamp\n"
    "v_pk_sub_u16 v102, v102, v236 clamp\n"
    "v_pk_sub_u16 v103, v103, v236 clamp\n"
    "v_pk_sub_u16 v108, v108, v236 clamp\n"
    "v_pk_sub_u16 v109, v109, v236 clamp\n"
    "v_pk_sub_u16 v110, v110, v236 clamp\n"
    "v_pk_sub_u16 v111, v111, v236 clamp\n"
    "v_pk_sub_u16 v116, v116, v236 clamp\n"
    "v_pk_sub_u16 v117, v117, v236 clamp\n"
    "v_pk_sub_u16 v118, v118, v236 clamp\n"
    "v_pk_sub_u16 v119, v119, v236 clamp\n"
    "v_add_f32_e32 v192, v171, v192\n"
    "v_add_f32_e32 v193, v171, v193\n"
    "v_add_f32_e32 v194, v171, v194\n"
    "v_exp_f32_e32 v192, v192\n"
    "v_add_f32_e32 v195, v171, v195\n"
    "v_exp_f32_e32 v193, v193\n"
    "v_add_f32_e32 v196, v172, v196\n"
    "v_exp_f32_e32 v194, v194\n"
    "v_add_f32_e32 v197, v172, v197\n"
    "v_exp_f32_e32 v195, v195\n"
    "v_add_f32_e32 v168, v192, v193\n"
    "v_cvt_pk_bf16_f32 v120, v192, v193\n"
    "v_add_f32_e32 v198, v172, v198\n"
    "v_exp_f32_e32 v196, v196\n"
    "v_add_f32_e32 v168, v194, v168\n"
    "v_add_f32_e32 v199, v172, v199\n"
    "v_exp_f32_e32 v197, v197\n"
    "v_add_f32_e32 v168, v195, v168\n"
    "v_cvt_pk_bf16_f32 v121, v194, v195\n"
    "v_add_f32_e32 v200, v171, v200\n"
    "v_exp_f32_e32 v198, v198\n"
    "v_add_f32_e32 v201, v171, v201\n"
    "v_exp_f32_e32 v199, v199\n"
    "v_add_f32_e32 v169, v196, v197\n"
    "v_cvt_pk_bf16_f32 v124, v196, v197\n"
    "v_add_f32_e32 v202, v171, v202\n"
    "v_exp_f32_e32 v200, v200\n"
    "v_add_f32_e32 v169, v198, v169\n"
    "v_add_f32_e32 v203, v171, v203\n"
    "v_exp_f32_e32 v201, v201\n"
    "v_add_f32_e32 v169, v199, v169\n"
    "v_cvt_pk_bf16_f32 v125, v198, v199\n"
    "v_add_f32_e32 v204, v172, v204\n"
    "v_exp_f32_e32 v202, v202\n"
    "v_add_f32_e32 v168, v200, v168\n"
    "v_add_f32_e32 v205, v172, v205\n"
    "v_exp_f32_e32 v203, v203\n"
    "v_add_f32_e32 v168, v201, v168\n"
    "v_cvt_pk_bf16_f32 v122, v200, v201\n"
    "v_add_f32_e32 v206, v172, v206\n"
    "v_exp_f32_e32 v204, v204\n"
    "v_add_f32_e32 v168, v202, v168\n"
    "v_add_f32_e32 v207, v172, v207\n"
    "v_exp_f32_e32 v205, v205\n"
    "v_add_f32_e32 v168, v203, v168\n"
    "v_cvt_pk_bf16_f32 v123, v202, v203\n"
    "v_exp_f32_e32 v206, v206\n"
    "v_add_f32_e32 v169, v204, v169\n"
    "v_exp_f32_e32 v207, v207\n"
    "v_add_f32_e32 v169, v205, v169\n"
    "v_cvt_pk_bf16_f32 v126, v204, v205\n"
    "v_add_f32_e32 v169, v206, v169\n"
    "v_add_f32_e32 v169, v207, v169\n"
    "v_cvt_pk_bf16_f32 v127, v206, v207\n"
    "s_nop 7\n"
    "s_nop 7\n"
    "s_branch .Lback_a15\n"
    : "+v"(tid)
    : "s"((unsigned long long)__builtin_amdgcn_kernarg_segment_ptr()), "n"(0), "n"(0), "n"(0), "s"(head)
    : "memory", "vcc", "scc", "v1", "v2", "v3", "v4", "v5", "v6", "v7", "v8", "v9", "v10", "v11", "v12", "v13", "v14", "v15", "v16", "v17", "v18", "v19", "v20", "v21", "v22", "v23", "v24", "v25", "v26", "v27", "v28", "v29", "v30", "v31", "v32", "v33", "v34", "v35", "v36", "v37", "v38", "v39", "v40", "v41", "v42", "v43", "v44", "v45", "v46", "v47", "v48", "v49", "v50", "v51", "v52", "v53", "v54", "v55", "v56", "v57", "v58", "v59", "v60", "v61", "v62", "v63", "v64", "v65", "v66", "v67", "v68", "v69", "v70", "v71", "v72", "v73", "v74", "v75", "v76", "v77", "v78", "v79", "v80", "v81", "v82", "v83", "v84", "v85", "v86", "v87", "v88", "v89", "v90", "v91", "v92", "v93", "v94", "v95", "v96", "v97", "v98", "v99", "v100", "v101", "v102", "v103", "v104", "v105", "v106", "v107", "v108", "v109", "v110", "v111", "v112", "v113", "v114", "v115", "v116", "v117", "v118", "v119", "v120", "v121", "v122", "v123", "v124", "v125", "v126", "v127", "v128", "v129", "v130", "v131", "v132", "v133", "v134", "v135", "v136", "v137", "v138", "v139", "v140", "v141", "v142", "v143", "v144", "v145", "v146", "v147", "v148", "v149", "v150", "v151", "v152", "v153", "v154", "v155", "v156", "v157", "v158", "v159", "v160", "v161", "v162", "v163", "v164", "v165", "v166", "v167", "v168", "v169", "v170", "v171", "v172", "v173", "v174", "v175", "v176", "v177", "v178", "v179", "v180", "v181", "v182", "v183", "v184", "v185", "v186", "v187", "v188", "v189", "v190", "v191", "v192", "v193", "v194", "v195", "v196", "v197", "v198", "v199", "v200", "v201", "v202", "v203", "v204", "v205", "v206", "v207", "v208", "v209", "v210", "v211", "v212", "v213", "v214", "v215", "v216", "v217", "v218", "v219", "v220", "v221", "v222", "v223", "v224", "v225", "v226", "v227", "v228", "v229", "v230", "v231", "v232", "v233", "v234", "v235", "v236", "v237", "v238", "v239", "v240", "v241", "v242", "v243", "v244", "v245", "v246", "v247", "v248", "v249", "v250", "v251", "v252", "v253", "v254", "v255", "s16", "s17", "s18", "s19", "s20", "s21", "s22", "s23", "s24", "s25", "s26", "s27", "s28", "s29", "s30", "s31", "s32", "s33", "s34", "s35", "s36", "s37", "s38", "s39", "s40", "s41", "s42", "s43", "s44", "s45", "s46", "s47", "s48", "s49", "s50", "s51", "s52", "s53", "s54", "s55", "s56", "s57", "s58", "s59", "s60", "s61", "s62", "s63", "s64", "s65", "s66", "s67", "s68", "s69", "s70", "s71", "s72", "s73", "s74", "s75", "s76", "s77", "s78", "s79", "s80", "s81", "s82", "s83", "s84", "s85", "s86", "s87", "s88", "s89", "s90", "s91", "s92", "s93", "s94", "s95", "s96", "s97", "s98", "s99", "s100", "s101");
  asm volatile("" :: "s"(Q), "s"(K), "s"(V), "s"(O) : "memory");
}

extern "C" void kernel_launch(void* const* d_in, const int* in_sizes, int n_in,
                              void* d_out, int out_size, void* d_ws, size_t ws_size,
                              hipStream_t stream) {
  (void)hipFuncSetAttribute((const void*)attn_fwd, hipFuncAttributeMaxDynamicSharedMemorySize, 163840);
  attn_fwd<<<256, 512, 163840, stream>>>((const char*)d_in[0], (const char*)d_in[1], (const char*)d_in[2], (char*)d_out);
}
```
